```python
import torch

def _softmax(x, axis=-1):
    return torch.softmax(x, dim=axis)
def _gelu(x):
    return torch.nn.functional.gelu(x, approximate="tanh")
def _silu(x):
    return torch.nn.functional.silu(x)
def _sigmoid(x):
    return torch.sigmoid(x)
def _layernorm(x, axis=-1, eps=1e-5):
    m = x.mean(dim=axis, keepdim=True)
    v = x.var(dim=axis, keepdim=True, unbiased=False)
    return (x - m) / torch.sqrt(v + eps)
def _scatter_add(tgt, idx, src, axis=0):
    return tgt.clone().index_add_(axis, idx, src)
def _segment_sum(src, seg, n):
    out = torch.zeros((n,) + src.shape[1:], dtype=src.dtype)
    return out.index_add_(0, seg, src)
def _pad(x, pw):
    flat = tuple(v for pair in reversed(pw) for v in pair)
    return torch.nn.functional.pad(x, flat)

kernel_name = 'interleave_layers__gqa_block_cached__dense_activation_silu__decode'

def setup_inputs():

    _w = (lambda a: (lambda t: t.to(torch.bfloat16) if t.is_floating_point() else t)(torch.as_tensor(a)))
    def _A_setup():
        B, Sq, D = 2, 1, 640
        Hq, Hkv, Sk, Dh = 32, 32, 8192, 128
        assert Hq % Hkv == 0, (Hq, Hkv)
        return {
            "x": _w((lambda *s: torch.randn(*s, dtype=torch.bfloat16) * 0.1)(B, Sq, D)),
            "k_cache": _w((lambda *s: torch.randn(*s, dtype=torch.bfloat16) * 1.6)(B, Hkv, Sk, Dh)),
            "v_cache": _w((lambda *s: torch.randn(*s, dtype=torch.bfloat16) * 3.2)(B, Hkv, Sk, Dh)),
            "wq": _w((lambda *s: torch.randn(*s, dtype=torch.bfloat16) * 1.6)(D, Hq * Dh) * (D ** -0.5)),
            "wo": _w((lambda *s: torch.randn(*s, dtype=torch.bfloat16) * 3.2)(Hq * Dh, D) * ((Hq * Dh) ** -0.5)),
        }
    _A_in = _A_setup()

    def _B_setup():
        B, S, D, F = 2, 1, 640, 1728
        return {
            "x": _w((lambda *s: torch.randn(*s, dtype=torch.bfloat16) * 0.1)(B, S, D)),
            "w1": _w((lambda *s: torch.randn(*s, dtype=torch.bfloat16) * 3.2)(D, F) * (D ** -0.5)),
            "w2": _w((lambda *s: torch.randn(*s, dtype=torch.bfloat16) * 0.8)(F, D) * (F ** -0.5)),
        }
    _B_in = _B_setup()

    return {
        "A_x": _A_in["x"],
        "A_k_cache": _A_in["k_cache"],
        "A_v_cache": _A_in["v_cache"],
        "A_wq": _A_in["wq"],
        "A_wo": _A_in["wo"],
        "B_x": _B_in["x"],
        "B_w1": _B_in["w1"],
        "B_w2": _B_in["w2"],
    }

def reference(A_x, A_k_cache, A_v_cache, A_wq, A_wo, B_x, B_w1, B_w2):
    def _A_ref(x, k_cache, v_cache, wq, wo):
        B, Sq, D = x.shape
        Hq, Dh = 32, 128
        Hkv = k_cache.shape[1]
        rep = Hq // Hkv
        q = (lambda x, *d: x.permute(*d))((x @ wq).reshape(B, Sq, Hq, Dh), 0, 2, 1, 3)
        k = (lambda x, n, axis: torch.repeat_interleave(x, n, dim=axis))(k_cache, rep, axis=1)
        v = (lambda x, n, axis: torch.repeat_interleave(x, n, dim=axis))(v_cache, rep, axis=1)
        scores = torch.einsum("bhqd,bhkd->bhqk", q, k) * (Dh ** -0.5)
        p = _softmax(scores, axis=-1)
        o = torch.einsum("bhqk,bhkd->bhqd", p, v)
        return (lambda x, *d: x.permute(*d))(o, 0, 2, 1, 3).reshape(B, Sq, Hq * Dh) @ wo

    def _B_ref(x, w1, w2):
        h = torch.einsum("bsd,df->bsf", x, w1)
        h = _silu(h)
        out = torch.einsum("bsf,fd->bsd", h, w2)
        return out

    x = A_x
    x = x + _A_ref(_layernorm(x, axis=-1), A_k_cache, A_v_cache, A_wq, A_wo)
    x = x + _B_ref(_layernorm(x, axis=-1), B_w1, B_w2)
    x = x + _A_ref(_layernorm(x, axis=-1), A_k_cache, A_v_cache, A_wq, A_wo)
    x = x + _B_ref(_layernorm(x, axis=-1), B_w1, B_w2)
    return x
```

```cpp
#include <hip/hip_runtime.h>
#include <hip/hip_bf16.h>
#include <stdint.h>

typedef unsigned u32x4 __attribute__((ext_vector_type(4)));
typedef __bf16 bf16x2 __attribute__((ext_vector_type(2)));
constexpr int B=2, D=640, H=32, SK=8192, DH=128, F=1728, NS=8;

__device__ __forceinline__ float bf2f(unsigned short v){ return __uint_as_float(((unsigned)v)<<16); }
__device__ __forceinline__ float rbf(float f){ __bf16 b=(__bf16)f; return (float)b; }
__device__ __forceinline__ unsigned short f2bf(float f){ __bf16 b=(__bf16)f; return __builtin_bit_cast(unsigned short,b); }

__device__ float block_sum(float v, float* sm){
  for(int o=32;o>0;o>>=1) v+=__shfl_xor(v,o);
  int w=threadIdx.x>>6; __syncthreads();
  if((threadIdx.x&63)==0) sm[w]=v;
  __syncthreads();
  float s=0; for(int i=0;i<(int)(blockDim.x>>6);++i) s+=sm[i];
  return s;
}
__device__ void layernorm(const float* x, float* xn, float* sm){
  float s=0; for(int i=threadIdx.x;i<D;i+=blockDim.x) s+=x[i];
  s=block_sum(s,sm); float mean=s/D;
  float v=0; for(int i=threadIdx.x;i<D;i+=blockDim.x){float d=x[i]-mean; v+=d*d;}
  v=block_sum(v,sm); v/=D;
  float mb=rbf(mean), vb=rbf(v); float den=rbf(sqrtf(rbf(vb+1e-5f)));
  for(int i=threadIdx.x;i<D;i+=blockDim.x) xn[i]=rbf(rbf(x[i]-mb)/den);
  __syncthreads();
}
__global__ void k_init(const unsigned short* x, float* xf){ int i=blockIdx.x*blockDim.x+threadIdx.x; if(i<B*D) xf[i]=bf2f(x[i]); }
__global__ void k_ln_gemv(const float* x, const unsigned short* W, float* y, int N, int act){
  __shared__ float xn[D]; __shared__ float sm[8]; __shared__ float red[4][64];
  int b=blockIdx.y; layernorm(x+b*D,xn,sm);
  int n=blockIdx.x*64+(threadIdx.x&63), w=threadIdx.x>>6; float a=0;
  for(int k=w;k<D;k+=4) a+=xn[k]*bf2f(W[(size_t)k*N+n]);
  red[w][threadIdx.x&63]=a; __syncthreads();
  if(w==0){ float r=red[0][threadIdx.x]+red[1][threadIdx.x]+red[2][threadIdx.x]+red[3][threadIdx.x]; r=rbf(r);
    if(act){ r=rbf(r/(1.f+__expf(-r))); } y[b*N+n]=r; }
}
__global__ void k_gemv_res(const float* a, const unsigned short* W, float* x, int K, unsigned short* out){
  __shared__ float red[16][64];
  int b=blockIdx.y; int n=blockIdx.x*64+(threadIdx.x&63), w=threadIdx.x>>6; float s=0;
  for(int k=w;k<K;k+=16) s+=a[b*K+k]*bf2f(W[(size_t)k*D+n]);
  red[w][threadIdx.x&63]=s; __syncthreads();
  if(w==0){ float r=0; for(int i=0;i<16;++i) r+=red[i][threadIdx.x]; float v=rbf(x[b*D+n]+rbf(r)); x[b*D+n]=v; if(out) out[b*D+n]=f2bf(v);}
}
__global__ void __launch_bounds__(256) k_attn(const float* q, const unsigned short* K, const unsigned short* V, float* part){
  __shared__ float sm[4][132];
  int s=blockIdx.x,h=blockIdx.y,b=blockIdx.z; int lane=threadIdx.x&63,w=threadIdx.x>>6; int r=lane>>4,c=lane&15;
  float qf[8]; for(int j=0;j<8;++j) qf[j]=q[b*H*DH+h*DH+8*c+j];
  const int KPS=SK/NS; size_t base=((size_t)(b*H+h)*SK+(size_t)s*KPS)*DH;
  float m=-INFINITY,l=0,acc[8]={0,0,0,0,0,0,0,0};
  for(int t=w*16;t<KPS;t+=64){
    u32x4 kk[4],vv[4];
    for(int i=0;i<4;++i){ size_t o=base+(size_t)(t+4*i+r)*DH+8*c; kk[i]=*(const u32x4*)(K+o); vv[i]=*(const u32x4*)(V+o);}
    float sc[4];
    for(int i=0;i<4;++i){ float d=0; for(int j=0;j<4;++j){ d+=qf[2*j]*__uint_as_float(kk[i][j]<<16)+qf[2*j+1]*__uint_as_float(kk[i][j]&0xffff0000u);}
      d+=__shfl_xor(d,1); d+=__shfl_xor(d,2); d+=__shfl_xor(d,4); d+=__shfl_xor(d,8); sc[i]=d*0.08838834764831845f; }
    float mx=fmaxf(fmaxf(sc[0],sc[1]),fmaxf(sc[2],sc[3])); float mn=fmaxf(m,mx); float al=__expf(m-mn); m=mn; l*=al; for(int j=0;j<8;++j) acc[j]*=al;
    for(int i=0;i<4;++i){ float p=__expf(sc[i]-m); l+=p; for(int j=0;j<4;++j){ acc[2*j]+=p*__uint_as_float(vv[i][j]<<16); acc[2*j+1]+=p*__uint_as_float(vv[i][j]&0xffff0000u);} }
  }
  float M=fmaxf(m,__shfl_xor(m,16)); M=fmaxf(M,__shfl_xor(M,32)); float f=__expf(m-M); l*=f; l+=__shfl_xor(l,16); l+=__shfl_xor(l,32);
  for(int j=0;j<8;++j){ float a=acc[j]*f; a+=__shfl_xor(a,16); a+=__shfl_xor(a,32); acc[j]=a; }
  if(r==0){ for(int j=0;j<8;++j) sm[w][8*c+j]=acc[j]; if(c==0){ sm[w][128]=M; sm[w][129]=l; } }
  __syncthreads();
  if(threadIdx.x<128){ float MM=fmaxf(fmaxf(sm[0][128],sm[1][128]),fmaxf(sm[2][128],sm[3][128])); float L=0,o=0; for(int i=0;i<4;++i){ float g=__expf(sm[i][128]-MM); L+=g*sm[i][129]; o+=g*sm[i][threadIdx.x]; }
    float* p=part+((size_t)(b*H+h)*NS+s)*132; p[threadIdx.x]=o; if(threadIdx.x==0){p[128]=MM;p[129]=L;} }
}
__global__ void k_comb(const float* part, float* o){ int bh=blockIdx.x; int d=threadIdx.x; const float* p=part+(size_t)bh*NS*132; float M=-INFINITY; for(int s=0;s<NS;++s) M=fmaxf(M,p[s*132+128]); float L=0,a=0; for(int s=0;s<NS;++s){ float g=__expf(p[s*132+128]-M); L+=g*p[s*132+129]; a+=g*p[s*132+d]; } o[bh*DH+d]=rbf(a/L); }

extern "C" void kernel_launch(void* const* d_in, const int* in_sizes, int n_in, void* d_out, int out_size, void* d_ws, size_t ws_size, hipStream_t stream) {
  const unsigned short* x=(const unsigned short*)d_in[0]; const unsigned short* K=(const unsigned short*)d_in[1]; const unsigned short* V=(const unsigned short*)d_in[2];
  const unsigned short* wq=(const unsigned short*)d_in[3]; const unsigned short* wo=(const unsigned short*)d_in[4]; const unsigned short* w1=(const unsigned short*)d_in[6]; const unsigned short* w2=(const unsigned short*)d_in[7];
  float* ws=(float*)d_ws; float* xf=ws; float* q=ws+4096; float* o=q+8192; float* h=o+8192; float* part=h+8192;
  k_init<<<5,256,0,stream>>>(x,xf);
  for(int l=0;l<2;++l){
    k_ln_gemv<<<dim3(H*DH/64,B),256,0,stream>>>(xf,wq,q,H*DH,0);
    k_attn<<<dim3(NS,H,B),256,0,stream>>>(q,K,V,part);
    k_comb<<<B*H,128,0,stream>>>(part,o);
    k_gemv_res<<<dim3(D/64,B),1024,0,stream>>>(o,wo,xf,H*DH,nullptr);
    k_ln_gemv<<<dim3(F/64,B),256,0,stream>>>(xf,w1,h,F,1);
    k_gemv_res<<<dim3(D/64,B),1024,0,stream>>>(h,w2,xf,F,l==1?(unsigned short*)d_out:nullptr);
  }
}
```

```cpp
#include <hip/hip_runtime.h>
#include <stdint.h>
#define DBG 0

typedef unsigned u32;
typedef unsigned long long u64;
typedef int i32x4 __attribute__((ext_vector_type(4)));
typedef __attribute__((address_space(3))) char lds_t;

constexpr u32 W_CH = 16384, W_JOB = 262144, W_FA = 2097152, W_FC1 = W_FA + 8192, W_FC2 = W_FA + 8704, W_EP = W_FA + 16384, W_Q1 = W_FA + 32768, W_DBG = 3145728, W_END = 4194304;

__device__ __forceinline__ i32x4 srd(const void* p, u32 off, u32 total) {
  const u64 a = (u64)p + off;
  return i32x4{(int)(u32)a, (int)((u32)(a >> 32) & 0xffffu), (int)(total - off), 0x00020000};
}

__global__ void __launch_bounds__(256, 1) fused_layers(const void* __restrict__ x, const void* __restrict__ kc, const void* __restrict__ vc, const void* __restrict__ wq,
                                                       const void* __restrict__ wo, const void* __restrict__ w1, const void* __restrict__ w2, void* out, void* ws) {
  __shared__ __attribute__((aligned(1024))) char smem[163840];
  const u32 bid = blockIdx.x, xcd = bid & 7, slot = bid >> 3;
  {
    u64 pc; u32 t0, t1;
    asm volatile(".Lk0:\n s_getpc_b64 %0\n s_lshl_b32 %1, %4, 12\n v_lshl_add_u32 %2, %3, 4, %1\n s_mov_b32 %1, .Lenda-.Lk0-20\n s_max_i32 %1, %1, .Lendc-.Lk0-20\n"
                 "v_min_u32 %2, %1, %2\n global_load_dwordx4 v[252:255], %2, %0"
                 : "=&s"(pc), "=&s"(t0), "=&v"(t1) : "v"(threadIdx.x), "s"(slot) : "v252", "v253", "v254", "v255", "scc", "memory");
    __builtin_amdgcn_sched_barrier(0);
  }
  const u32 w = __builtin_amdgcn_readfirstlane(threadIdx.x >> 6);
  const u32 sp = slot - 4, h = xcd * 4 + (sp * 37 >> 8), c = sp - (sp * 37 >> 8) * 7;
  const u32 off = (h * 8192 + c * 128) * 256u;
  const u32 rot = (h * 1 + c * 1) % 8u * 4096u;
  asm volatile("" :: "s"(rot), "s"(w), "s"(off), "s"(h), "s"(c));
  __builtin_amdgcn_sched_barrier(0);
  const u32 lb = (u32)(uintptr_t)(lds_t*)smem;
  if (slot < 4) {
    const u64 wa = ((u64)ws + 2097151ull) & ~2097151ull, ep = wa + W_EP;
    const i32x4 rw = srd((const void*)wa, 0, W_END);
    const u32 j = xcd * 4 + slot, jj = j < 27 ? j : 0;
    const u32 kvo = ((w & 1) ? 0u : 67108864u) + (j * 8192 + 8176) * 256;
    const i32x4 rq = srd(wq, 160 * w * 8192 + 256 * j, 640 * 4096 * 2), ro = srd(wo, (512 * xcd + 128 * w) * 1280 + 320 * slot, 4096 * 640 * 2);
    i32x4 r1 = srd(w1, 160 * w * 3456 + 128 * jj, 640 * 1728 * 2), r2 = srd(w2, 64 * jj * 1280 + 320 * w, 1728 * 640 * 2);
    if (j >= 27) { r1[2] = 0; r2[2] = 0; }
    const i32x4 rx = srd(x, 320 * w, 2560), rk = srd(kc, kvo, 134217728), rv = srd(vc, kvo, 134217728), rout = srd(out, 0, 2560);
    asm volatile(
R"ASM(s_load_dwordx2 s[60:61], %[ep], 0x0
s_memrealtime s[80:81]
v_mbcnt_lo_u32_b32 v22, -1, 0
v_mbcnt_hi_u32_b32 v22, -1, v22
s_mov_b32 s62, 0x9c40
s_mov_b32 s97, 0x5040100
s_mov_b32 s98, 0x7060302
s_mov_b32 s88, 0x3f803f80
s_mov_b32 s84, 0x37373737
s_mov_b32 s85, 0x37373737
s_mov_b32 s99, 0x3fb8aa3b
s_mov_b32 s96, 0x5
v_lshrrev_b32 v23, 4, v22
v_and_b32 v24, 15, v22
v_lshlrev_b32 v0, 4, v22
v_lshlrev_b32 v11, 2, v22
v_lshlrev_b32 v12, 3, v22
v_lshlrev_b32 v25, 4, v24
v_lshl_add_u32 v26, v23, 15, v25
v_lshl_add_u32 v27, v23, 16, v25
v_mov_b32 v32, -1
v_mov_b32 v33, -1
v_mov_b32 v34, -1
v_mov_b32 v35, -1
s_cmp_lg_u32 %[w], 3
s_cbranch_scc1 Cnoq1c_%=
s_lshl_b32 s89, %[j], 9
s_add_u32 s89, s89, 2129920
s_bfm_b64 exec, 32, 0
buffer_store_dwordx4 v[32:35], v0, %[rw], s89 offen sc1
s_mov_b64 exec, -1
Cnoq1c_%=:
s_mov_b32 s89, 0
buffer_load_dwordx4 v[96:99], v26, %[rq], s89 offen nt
s_mov_b32 s89, 8192
buffer_load_dwordx4 v[100:103], v26, %[rq], s89 offen nt
s_mov_b32 s89, 262144
buffer_load_dwordx4 v[104:107], v26, %[rq], s89 offen nt
s_mov_b32 s89, 270336
buffer_load_dwordx4 v[108:111], v26, %[rq], s89 offen nt
s_mov_b32 s89, 16384
buffer_load_dwordx4 v[112:115], v26, %[rq], s89 offen nt
s_mov_b32 s89, 24576
buffer_load_dwordx4 v[116:119], v26, %[rq], s89 offen nt
s_mov_b32 s89, 278528
buffer_load_dwordx4 v[120:123], v26, %[rq], s89 offen nt
s_mov_b32 s89, 286720
buffer_load_dwordx4 v[124:127], v26, %[rq], s89 offen nt
v_and_b32 v31, 7, v24
v_lshlrev_b32 v31, 2, v31
s_mov_b32 s63, 0x44314420
v_bfe_u32 v31, s63, v31, 4
v_lshlrev_b32 v32, 4, v23
v_lshl_add_u32 v7, v31, 7, v32
v_lshl_add_u32 v18, v31, 6, v32
v_lshrrev_b32 v31, 3, v24
v_mul_u32_u24 v30, 0x500, v31
v_add_u32 v30, v30, v18
buffer_load_dwordx4 v[84:87], v30, %[rx], 0 offen
v_lshl_add_u32 v28, v24, 8, v32
v_lshl_add_u32 v29, v23, 10, v25
buffer_load_dwordx4 v[40:43], v28, %[rk], 0 offen nt
buffer_load_dwordx4 v[44:47], v28, %[rk], 0 offen offset:64 nt
buffer_load_dwordx4 v[48:51], v28, %[rk], 0 offen offset:128 nt
buffer_load_dwordx4 v[52:55], v28, %[rk], 0 offen offset:192 nt
buffer_load_dwordx4 v[56:59], v29, %[rv], 0 offen nt
buffer_load_dwordx4 v[60:63], v29, %[rv], 0 offen offset:256 nt
buffer_load_dwordx4 v[64:67], v29, %[rv], 0 offen offset:512 nt
buffer_load_dwordx4 v[68:71], v29, %[rv], 0 offen offset:768 nt
s_mov_b32 s89, 131072
buffer_load_dwordx4 v[128:131], v26, %[rq], s89 offen nt
s_mov_b32 s89, 139264
buffer_load_dwordx4 v[132:135], v26, %[rq], s89 offen nt
s_mov_b32 s89, 393216
buffer_load_dwordx4 v[136:139], v26, %[rq], s89 offen nt
s_mov_b32 s89, 401408
buffer_load_dwordx4 v[140:143], v26, %[rq], s89 offen nt
s_mov_b32 s89, 147456
buffer_load_dwordx4 v[144:147], v26, %[rq], s89 offen nt
s_mov_b32 s89, 155648
buffer_load_dwordx4 v[148:151], v26, %[rq], s89 offen nt
s_mov_b32 s89, 409600
buffer_load_dwordx4 v[152:155], v26, %[rq], s89 offen nt
s_mov_b32 s89, 417792
buffer_load_dwordx4 v[156:159], v26, %[rq], s89 offen nt
s_mov_b32 s89, 524288
buffer_load_dwordx4 v[160:163], v26, %[rq], s89 offen nt
s_mov_b32 s89, 532480
buffer_load_dwordx4 v[164:167], v26, %[rq], s89 offen nt
s_mov_b32 s89, 786432
buffer_load_dwordx4 v[168:171], v26, %[rq], s89 offen nt
s_mov_b32 s89, 794624
buffer_load_dwordx4 v[172:175], v26, %[rq], s89 offen nt
s_mov_b32 s89, 540672
buffer_load_dwordx4 v[176:179], v26, %[rq], s89 offen nt
s_mov_b32 s89, 548864
buffer_load_dwordx4 v[180:183], v26, %[rq], s89 offen nt
s_mov_b32 s89, 802816
buffer_load_dwordx4 v[184:187], v26, %[rq], s89 offen nt
s_mov_b32 s89, 811008
buffer_load_dwordx4 v[188:191], v26, %[rq], s89 offen nt
s_mov_b32 s89, 655360
buffer_load_dwordx4 v[192:195], v26, %[rq], s89 offen nt
s_mov_b32 s89, 663552
buffer_load_dwordx4 v[196:199], v26, %[rq], s89 offen nt
s_mov_b32 s89, 917504
buffer_load_dwordx4 v[200:203], v26, %[rq], s89 offen nt
s_mov_b32 s89, 925696
buffer_load_dwordx4 v[204:207], v26, %[rq], s89 offen nt
s_mov_b32 s89, 671744
buffer_load_dwordx4 v[208:211], v26, %[rq], s89 offen nt
s_mov_b32 s89, 679936
buffer_load_dwordx4 v[212:215], v26, %[rq], s89 offen nt
s_mov_b32 s89, 933888
buffer_load_dwordx4 v[216:219], v26, %[rq], s89 offen nt
s_mov_b32 s89, 942080
buffer_load_dwordx4 v[220:223], v26, %[rq], s89 offen nt
s_mov_b32 s89, 1048576
buffer_load_dwordx4 v[224:227], v27, %[rq], s89 offen nt
s_mov_b32 s89, 1056768
buffer_load_dwordx4 v[228:231], v27, %[rq], s89 offen nt
s_mov_b32 s89, 1081344
buffer_load_dwordx4 v[232:235], v27, %[rq], s89 offen nt
s_mov_b32 s89, 1089536
buffer_load_dwordx4 v[236:239], v27, %[rq], s89 offen nt
s_mov_b32 s89, 1064960
buffer_load_dwordx4 v[240:243], v27, %[rq], s89 offen nt
s_mov_b32 s89, 1073152
buffer_load_dwordx4 v[244:247], v27, %[rq], s89 offen nt
s_mov_b32 s89, 1097728
buffer_load_dwordx4 v[248:251], v27, %[rq], s89 offen nt
s_mov_b32 s89, 1105920
buffer_load_dwordx4 v[252:255], v27, %[rq], s89 offen nt
s_mul_i32 s63, %[w], 320
s_add_u32 s63, s63, 13312
v_add_u32 v18, s63, v18
v_mul_u32_u24 v36, 0xa00, v31
v_add_u32 v30, v18, v36
v_and_b32 v36, 7, v24
v_lshlrev_b32 v36, 3, v36
v_lshl_add_u32 v36, v23, 1, v36
v_add_u32 v16, s63, v36
v_add_u32 v18, 0x500, v18
v_lshrrev_b32 v36, 5, v22
v_and_b32 v37, 31, v22
v_lshlrev_b32 v37, 4, v37
v_mul_u32_u24 v38, 0x280, v36
v_add_u32 v1, v37, v38
v_and_b32 v37, 7, v22
v_xor_b32 v38, v37, v36
v_mul_u32_u24 v38, 0x280, v38
v_add_u32 v2, 0x200, v38
v_lshrrev_b32 v38, 3, v22
v_mul_u32_u24 v38, 0x280, v38
v_lshl_add_u32 v3, v37, 4, v38
s_lshl_b32 s63, %[w], 3
v_lshl_add_u32 v19, v36, 2, s63
v_lshl_add_u32 v19, v31, 5, v19
s_mul_i32 s63, %[w], 640
v_and_b32 v37, 1, v23
v_lshlrev_b32 v37, 4, v37
v_lshl_add_u32 v37, v24, 5, v37
v_mul_u32_u24 v38, 0xa00, v36
v_add3_u32 v8, v37, v38, s63
v_lshlrev_b32 v37, 2, v23
v_lshl_add_u32 v37, v24, 3, v37
v_add_u32 v9, s63, v37
s_lshl_b32 s63, %[w], 8
v_lshl_add_u32 v10, v37, 1, s63
s_mul_i32 s63, %[w], 24576
s_add_u32 s63, s63, 20480
v_add_u32 v20, s63, v0
v_mov_b32 v5, 0x7ffffff0
s_bfm_b64 exec, 24, 0
v_mov_b32 v5, v0
s_mov_b64 exec, -1
v_mov_b32 v17, 0x4444
s_mov_b32 exec_lo, 0xf0f0f0f0
s_mov_b32 exec_hi, 0xf0f0f0f0
v_mov_b32 v17, 0xeeee
s_mov_b64 exec, -1
v_lshrrev_b32 v36, 1, v23
v_and_b32 v37, 1, v23
v_lshl_add_u32 v38, v37, 5, v36
v_mul_u32_u24 v38, 0xd80, v38
v_lshl_add_u32 v88, v24, 3, v38
v_mul_u32_u24 v38, 0xd80, v23
v_lshl_add_u32 v89, v24, 3, v38
v_mul_u32_u24 v38, 0x2800, v23
v_lshl_add_u32 v90, v24, 4, v38
v_lshl_add_u32 v91, v24, 2, v38
v_lshlrev_b32 v37, 3, v37
v_lshl_add_u32 v38, v36, 6, v37
v_mul_u32_u24 v92, 0x500, v38
s_mul_i32 s86, %[j], 52
s_lshr_b32 s86, s86, 8
s_mul_i32 s63, s86, 5
s_sub_u32 s87, %[j], s63
s_waitcnt lgkmcnt(0)
s_add_u32 s60, s60, 1
s_addc_u32 s61, s61, 0
s_waitcnt vmcnt(41)
s_cmp_lg_u32 %[w], 3
s_cbranch_scc1 Cnofc1_%=
v_mov_b32 v32, s60
v_mov_b32 v33, s61
s_lshl_b32 s89, %[j], 3
s_add_u32 s89, s89, 2105344
s_mov_b64 exec, 0x1
buffer_store_dwordx2 v[32:33], off, %[rw], s89 sc1
s_mov_b64 exec, -1
s_nop 4
Cnofc1_%=:
v_perm_b32 v72, v100, v96, s97
v_accvgpr_write_b32 a0, v72
v_perm_b32 v73, v108, v104, s97
v_accvgpr_write_b32 a1, v73
v_perm_b32 v74, v116, v112, s97
v_accvgpr_write_b32 a2, v74
v_perm_b32 v75, v124, v120, s97
v_accvgpr_write_b32 a3, v75
v_perm_b32 v72, v100, v96, s98
v_accvgpr_write_b32 a8, v72
v_perm_b32 v73, v108, v104, s98
v_accvgpr_write_b32 a9, v73
v_perm_b32 v74, v116, v112, s98
v_accvgpr_write_b32 a10, v74
v_perm_b32 v75, v124, v120, s98
v_accvgpr_write_b32 a11, v75
v_perm_b32 v72, v101, v97, s97
v_accvgpr_write_b32 a16, v72
v_perm_b32 v73, v109, v105, s97
v_accvgpr_write_b32 a17, v73
v_perm_b32 v74, v117, v113, s97
v_accvgpr_write_b32 a18, v74
v_perm_b32 v75, v125, v121, s97
v_accvgpr_write_b32 a19, v75
v_perm_b32 v72, v101, v97, s98
v_accvgpr_write_b32 a24, v72
v_perm_b32 v73, v109, v105, s98
v_accvgpr_write_b32 a25, v73
v_perm_b32 v74, v117, v113, s98
v_accvgpr_write_b32 a26, v74
v_perm_b32 v75, v125, v121, s98
v_accvgpr_write_b32 a27, v75
v_perm_b32 v72, v102, v98, s97
v_accvgpr_write_b32 a32, v72
v_perm_b32 v73, v110, v106, s97
v_accvgpr_write_b32 a33, v73
v_perm_b32 v74, v118, v114, s97
v_accvgpr_write_b32 a34, v74
v_perm_b32 v75, v126, v122, s97
v_accvgpr_write_b32 a35, v75
v_perm_b32 v72, v102, v98, s98
v_accvgpr_write_b32 a40, v72
v_perm_b32 v73, v110, v106, s98
v_accvgpr_write_b32 a41, v73
v_perm_b32 v74, v118, v114, s98
v_accvgpr_write_b32 a42, v74
v_perm_b32 v75, v126, v122, s98
v_accvgpr_write_b32 a43, v75
v_perm_b32 v72, v103, v99, s97
v_accvgpr_write_b32 a48, v72
v_perm_b32 v73, v111, v107, s97
v_accvgpr_write_b32 a49, v73
v_perm_b32 v74, v119, v115, s97
v_accvgpr_write_b32 a50, v74
v_perm_b32 v75, v127, v123, s97
v_accvgpr_write_b32 a51, v75
v_perm_b32 v72, v103, v99, s98
v_accvgpr_write_b32 a56, v72
v_perm_b32 v73, v111, v107, s98
v_accvgpr_write_b32 a57, v73
v_perm_b32 v74, v119, v115, s98
v_accvgpr_write_b32 a58, v74
v_perm_b32 v75, v127, v123, s98
v_accvgpr_write_b32 a59, v75
s_waitcnt vmcnt(32)
ds_write_b128 v30, v[84:87]
s_and_b32 s63, %[w], 1
s_xor_b32 s63, s63, 1
s_lshl_b32 s63, s63, 13
s_add_u32 s82, s63, 118784
v_add_u32 v36, s82, v0
ds_write_b128 v36, v[40:43] offset:0
ds_write_b128 v36, v[44:47] offset:1024
ds_write_b128 v36, v[48:51] offset:2048
ds_write_b128 v36, v[52:55] offset:3072
v_perm_b32 v76, v60, v56, s97
v_perm_b32 v77, v68, v64, s97
v_perm_b32 v78, v60, v56, s98
v_perm_b32 v79, v68, v64, s98
ds_write_b128 v36, v[76:79] offset:4096
v_perm_b32 v80, v61, v57, s97
v_perm_b32 v81, v69, v65, s97
v_perm_b32 v82, v61, v57, s98
v_perm_b32 v83, v69, v65, s98
ds_write_b128 v36, v[80:83] offset:5120
v_perm_b32 v76, v62, v58, s97
v_perm_b32 v77, v70, v66, s97
v_perm_b32 v78, v62, v58, s98
v_perm_b32 v79, v70, v66, s98
ds_write_b128 v36, v[76:79] offset:6144
v_perm_b32 v80, v63, v59, s97
v_perm_b32 v81, v71, v67, s97
v_perm_b32 v82, v63, v59, s98
v_perm_b32 v83, v71, v67, s98
ds_write_b128 v36, v[80:83] offset:7168
s_mov_b32 s89, 0
buffer_load_dwordx2 v[96:97], v88, %[r1], s89 offen nt
s_mov_b32 s89, 13824
buffer_load_dwordx2 v[98:99], v88, %[r1], s89 offen nt
s_mov_b32 s89, 55296
buffer_load_dwordx2 v[100:101], v88, %[r1], s89 offen nt
s_mov_b32 s89, 69120
buffer_load_dwordx2 v[102:103], v88, %[r1], s89 offen nt
s_mov_b32 s89, 27648
buffer_load_dwordx2 v[104:105], v88, %[r1], s89 offen nt
s_mov_b32 s89, 41472
buffer_load_dwordx2 v[106:107], v88, %[r1], s89 offen nt
s_mov_b32 s89, 82944
buffer_load_dwordx2 v[108:109], v88, %[r1], s89 offen nt
s_mov_b32 s89, 96768
buffer_load_dwordx2 v[110:111], v88, %[r1], s89 offen nt
s_mov_b32 s89, 6912
buffer_load_dwordx2 v[112:113], v88, %[r1], s89 offen nt
s_mov_b32 s89, 20736
buffer_load_dwordx2 v[114:115], v88, %[r1], s89 offen nt
s_mov_b32 s89, 62208
buffer_load_dwordx2 v[116:117], v88, %[r1], s89 offen nt
s_mov_b32 s89, 76032
buffer_load_dwordx2 v[118:119], v88, %[r1], s89 offen nt
s_mov_b32 s89, 34560
buffer_load_dwordx2 v[120:121], v88, %[r1], s89 offen nt
s_mov_b32 s89, 48384
buffer_load_dwordx2 v[122:123], v88, %[r1], s89 offen nt
s_mov_b32 s89, 89856
buffer_load_dwordx2 v[124:125], v88, %[r1], s89 offen nt
s_mov_b32 s89, 103680
buffer_load_dwordx2 v[126:127], v88, %[r1], s89 offen nt
s_waitcnt vmcnt(40)
v_perm_b32 v72, v132, v128, s97
v_accvgpr_write_b32 a4, v72
v_perm_b32 v73, v140, v136, s97
v_accvgpr_write_b32 a5, v73
v_perm_b32 v74, v148, v144, s97
v_accvgpr_write_b32 a6, v74
v_perm_b32 v75, v156, v152, s97
v_accvgpr_write_b32 a7, v75
v_perm_b32 v72, v132, v128, s98
v_accvgpr_write_b32 a12, v72
v_perm_b32 v73, v140, v136, s98
v_accvgpr_write_b32 a13, v73
v_perm_b32 v74, v148, v144, s98
v_accvgpr_write_b32 a14, v74
v_perm_b32 v75, v156, v152, s98
v_accvgpr_write_b32 a15, v75
v_perm_b32 v72, v133, v129, s97
v_accvgpr_write_b32 a20, v72
v_perm_b32 v73, v141, v137, s97
v_accvgpr_write_b32 a21, v73
v_perm_b32 v74, v149, v145, s97
v_accvgpr_write_b32 a22, v74
v_perm_b32 v75, v157, v153, s97
v_accvgpr_write_b32 a23, v75
v_perm_b32 v72, v133, v129, s98
v_accvgpr_write_b32 a28, v72
v_perm_b32 v73, v141, v137, s98
v_accvgpr_write_b32 a29, v73
v_perm_b32 v74, v149, v145, s98
v_accvgpr_write_b32 a30, v74
v_perm_b32 v75, v157, v153, s98
v_accvgpr_write_b32 a31, v75
v_perm_b32 v72, v134, v130, s97
v_accvgpr_write_b32 a36, v72
v_perm_b32 v73, v142, v138, s97
v_accvgpr_write_b32 a37, v73
v_perm_b32 v74, v150, v146, s97
v_accvgpr_write_b32 a38, v74
v_perm_b32 v75, v158, v154, s97
v_accvgpr_write_b32 a39, v75
v_perm_b32 v72, v134, v130, s98
v_accvgpr_write_b32 a44, v72
v_perm_b32 v73, v142, v138, s98
v_accvgpr_write_b32 a45, v73
v_perm_b32 v74, v150, v146, s98
v_accvgpr_write_b32 a46, v74
v_perm_b32 v75, v158, v154, s98
v_accvgpr_write_b32 a47, v75
v_perm_b32 v72, v135, v131, s97
v_accvgpr_write_b32 a52, v72
v_perm_b32 v73, v143, v139, s97
v_accvgpr_write_b32 a53, v73
v_perm_b32 v74, v151, v147, s97
v_accvgpr_write_b32 a54, v74
v_perm_b32 v75, v159, v155, s97
v_accvgpr_write_b32 a55, v75
v_perm_b32 v72, v135, v131, s98
v_accvgpr_write_b32 a60, v72
v_perm_b32 v73, v143, v139, s98
)ASM"
R"ASM(v_accvgpr_write_b32 a61, v73
v_perm_b32 v74, v151, v147, s98
v_accvgpr_write_b32 a62, v74
v_perm_b32 v75, v159, v155, s98
v_accvgpr_write_b32 a63, v75
s_mov_b32 s89, 221184
buffer_load_dwordx2 v[128:129], v88, %[r1], s89 offen nt
s_mov_b32 s89, 235008
buffer_load_dwordx2 v[130:131], v88, %[r1], s89 offen nt
s_mov_b32 s89, 276480
buffer_load_dwordx2 v[132:133], v88, %[r1], s89 offen nt
s_mov_b32 s89, 290304
buffer_load_dwordx2 v[134:135], v88, %[r1], s89 offen nt
s_mov_b32 s89, 248832
buffer_load_dwordx2 v[136:137], v88, %[r1], s89 offen nt
s_mov_b32 s89, 262656
buffer_load_dwordx2 v[138:139], v88, %[r1], s89 offen nt
s_mov_b32 s89, 304128
buffer_load_dwordx2 v[140:141], v88, %[r1], s89 offen nt
s_mov_b32 s89, 317952
buffer_load_dwordx2 v[142:143], v88, %[r1], s89 offen nt
s_mov_b32 s89, 228096
buffer_load_dwordx2 v[144:145], v88, %[r1], s89 offen nt
s_mov_b32 s89, 241920
buffer_load_dwordx2 v[146:147], v88, %[r1], s89 offen nt
s_mov_b32 s89, 283392
buffer_load_dwordx2 v[148:149], v88, %[r1], s89 offen nt
s_mov_b32 s89, 297216
buffer_load_dwordx2 v[150:151], v88, %[r1], s89 offen nt
s_mov_b32 s89, 255744
buffer_load_dwordx2 v[152:153], v88, %[r1], s89 offen nt
s_mov_b32 s89, 269568
buffer_load_dwordx2 v[154:155], v88, %[r1], s89 offen nt
s_mov_b32 s89, 311040
buffer_load_dwordx2 v[156:157], v88, %[r1], s89 offen nt
s_mov_b32 s89, 324864
buffer_load_dwordx2 v[158:159], v88, %[r1], s89 offen nt
s_waitcnt vmcnt(48)
v_perm_b32 v72, v164, v160, s97
v_accvgpr_write_b32 a64, v72
v_perm_b32 v73, v172, v168, s97
v_accvgpr_write_b32 a65, v73
v_perm_b32 v74, v180, v176, s97
v_accvgpr_write_b32 a66, v74
v_perm_b32 v75, v188, v184, s97
v_accvgpr_write_b32 a67, v75
v_perm_b32 v72, v164, v160, s98
v_accvgpr_write_b32 a72, v72
v_perm_b32 v73, v172, v168, s98
v_accvgpr_write_b32 a73, v73
v_perm_b32 v74, v180, v176, s98
v_accvgpr_write_b32 a74, v74
v_perm_b32 v75, v188, v184, s98
v_accvgpr_write_b32 a75, v75
v_perm_b32 v72, v165, v161, s97
v_accvgpr_write_b32 a80, v72
v_perm_b32 v73, v173, v169, s97
v_accvgpr_write_b32 a81, v73
v_perm_b32 v74, v181, v177, s97
v_accvgpr_write_b32 a82, v74
v_perm_b32 v75, v189, v185, s97
v_accvgpr_write_b32 a83, v75
v_perm_b32 v72, v165, v161, s98
v_accvgpr_write_b32 a88, v72
v_perm_b32 v73, v173, v169, s98
v_accvgpr_write_b32 a89, v73
v_perm_b32 v74, v181, v177, s98
v_accvgpr_write_b32 a90, v74
v_perm_b32 v75, v189, v185, s98
v_accvgpr_write_b32 a91, v75
v_perm_b32 v72, v166, v162, s97
v_accvgpr_write_b32 a96, v72
v_perm_b32 v73, v174, v170, s97
v_accvgpr_write_b32 a97, v73
v_perm_b32 v74, v182, v178, s97
v_accvgpr_write_b32 a98, v74
v_perm_b32 v75, v190, v186, s97
v_accvgpr_write_b32 a99, v75
v_perm_b32 v72, v166, v162, s98
v_accvgpr_write_b32 a104, v72
v_perm_b32 v73, v174, v170, s98
v_accvgpr_write_b32 a105, v73
v_perm_b32 v74, v182, v178, s98
v_accvgpr_write_b32 a106, v74
v_perm_b32 v75, v190, v186, s98
v_accvgpr_write_b32 a107, v75
v_perm_b32 v72, v167, v163, s97
v_accvgpr_write_b32 a112, v72
v_perm_b32 v73, v175, v171, s97
v_accvgpr_write_b32 a113, v73
v_perm_b32 v74, v183, v179, s97
v_accvgpr_write_b32 a114, v74
v_perm_b32 v75, v191, v187, s97
v_accvgpr_write_b32 a115, v75
v_perm_b32 v72, v167, v163, s98
v_accvgpr_write_b32 a120, v72
v_perm_b32 v73, v175, v171, s98
v_accvgpr_write_b32 a121, v73
v_perm_b32 v74, v183, v179, s98
v_accvgpr_write_b32 a122, v74
v_perm_b32 v75, v191, v187, s98
v_accvgpr_write_b32 a123, v75
s_mov_b32 s89, 442368
buffer_load_dwordx2 v[160:161], v89, %[r1], s89 offen nt
s_mov_b32 s89, 456192
buffer_load_dwordx2 v[162:163], v89, %[r1], s89 offen nt
s_mov_b32 s89, 497664
buffer_load_dwordx2 v[164:165], v89, %[r1], s89 offen nt
s_mov_b32 s89, 511488
buffer_load_dwordx2 v[166:167], v89, %[r1], s89 offen nt
s_mov_b32 s89, 470016
buffer_load_dwordx2 v[168:169], v89, %[r1], s89 offen nt
s_mov_b32 s89, 483840
buffer_load_dwordx2 v[170:171], v89, %[r1], s89 offen nt
s_mov_b32 s89, 525312
buffer_load_dwordx2 v[172:173], v89, %[r1], s89 offen nt
s_mov_b32 s89, 539136
buffer_load_dwordx2 v[174:175], v89, %[r1], s89 offen nt
v_add_u32 v91, 0x100, v91
s_mov_b32 s89, 0
buffer_load_dword v176, v91, %[r2], s89 offen nt
s_mov_b32 s89, 1280
buffer_load_dword v177, v91, %[r2], s89 offen nt
s_mov_b32 s89, 5120
buffer_load_dword v178, v91, %[r2], s89 offen nt
s_mov_b32 s89, 6400
buffer_load_dword v179, v91, %[r2], s89 offen nt
s_mov_b32 s89, 2560
s_waitcnt vmcnt(59)
buffer_load_dword v180, v91, %[r2], s89 offen nt
s_mov_b32 s89, 3840
s_waitcnt vmcnt(59)
buffer_load_dword v181, v91, %[r2], s89 offen nt
s_mov_b32 s89, 7680
s_waitcnt vmcnt(59)
buffer_load_dword v182, v91, %[r2], s89 offen nt
s_mov_b32 s89, 8960
s_waitcnt vmcnt(59)
buffer_load_dword v183, v91, %[r2], s89 offen nt
s_mov_b32 s89, 40960
s_waitcnt vmcnt(59)
buffer_load_dword v184, v91, %[r2], s89 offen nt
s_mov_b32 s89, 42240
s_waitcnt vmcnt(59)
buffer_load_dword v185, v91, %[r2], s89 offen nt
s_mov_b32 s89, 46080
s_waitcnt vmcnt(59)
buffer_load_dword v186, v91, %[r2], s89 offen nt
s_mov_b32 s89, 47360
s_waitcnt vmcnt(59)
buffer_load_dword v187, v91, %[r2], s89 offen nt
s_mov_b32 s89, 43520
s_waitcnt vmcnt(59)
buffer_load_dword v188, v91, %[r2], s89 offen nt
s_mov_b32 s89, 44800
s_waitcnt vmcnt(59)
buffer_load_dword v189, v91, %[r2], s89 offen nt
s_mov_b32 s89, 48640
s_waitcnt vmcnt(59)
buffer_load_dword v190, v91, %[r2], s89 offen nt
s_mov_b32 s89, 49920
s_waitcnt vmcnt(59)
buffer_load_dword v191, v91, %[r2], s89 offen nt
v_perm_b32 v72, v196, v192, s97
v_accvgpr_write_b32 a68, v72
v_perm_b32 v73, v204, v200, s97
v_accvgpr_write_b32 a69, v73
v_perm_b32 v74, v212, v208, s97
v_accvgpr_write_b32 a70, v74
v_perm_b32 v75, v220, v216, s97
v_accvgpr_write_b32 a71, v75
v_perm_b32 v72, v196, v192, s98
v_accvgpr_write_b32 a76, v72
v_perm_b32 v73, v204, v200, s98
v_accvgpr_write_b32 a77, v73
v_perm_b32 v74, v212, v208, s98
v_accvgpr_write_b32 a78, v74
v_perm_b32 v75, v220, v216, s98
v_accvgpr_write_b32 a79, v75
v_perm_b32 v72, v197, v193, s97
v_accvgpr_write_b32 a84, v72
v_perm_b32 v73, v205, v201, s97
v_accvgpr_write_b32 a85, v73
v_perm_b32 v74, v213, v209, s97
v_accvgpr_write_b32 a86, v74
v_perm_b32 v75, v221, v217, s97
v_accvgpr_write_b32 a87, v75
v_perm_b32 v72, v197, v193, s98
v_accvgpr_write_b32 a92, v72
v_perm_b32 v73, v205, v201, s98
v_accvgpr_write_b32 a93, v73
v_perm_b32 v74, v213, v209, s98
v_accvgpr_write_b32 a94, v74
v_perm_b32 v75, v221, v217, s98
v_accvgpr_write_b32 a95, v75
v_perm_b32 v72, v198, v194, s97
v_accvgpr_write_b32 a100, v72
v_perm_b32 v73, v206, v202, s97
v_accvgpr_write_b32 a101, v73
v_perm_b32 v74, v214, v210, s97
v_accvgpr_write_b32 a102, v74
v_perm_b32 v75, v222, v218, s97
v_accvgpr_write_b32 a103, v75
v_perm_b32 v72, v198, v194, s98
v_accvgpr_write_b32 a108, v72
v_perm_b32 v73, v206, v202, s98
v_accvgpr_write_b32 a109, v73
v_perm_b32 v74, v214, v210, s98
v_accvgpr_write_b32 a110, v74
v_perm_b32 v75, v222, v218, s98
v_accvgpr_write_b32 a111, v75
v_perm_b32 v72, v199, v195, s97
v_accvgpr_write_b32 a116, v72
v_perm_b32 v73, v207, v203, s97
v_accvgpr_write_b32 a117, v73
v_perm_b32 v74, v215, v211, s97
v_accvgpr_write_b32 a118, v74
v_perm_b32 v75, v223, v219, s97
v_accvgpr_write_b32 a119, v75
v_perm_b32 v72, v199, v195, s98
v_accvgpr_write_b32 a124, v72
v_perm_b32 v73, v207, v203, s98
v_accvgpr_write_b32 a125, v73
v_perm_b32 v74, v215, v211, s98
v_accvgpr_write_b32 a126, v74
v_perm_b32 v75, v223, v219, s98
v_accvgpr_write_b32 a127, v75
s_mov_b32 s89, 0
s_waitcnt vmcnt(59)
buffer_load_dwordx4 v[192:195], v90, %[r2], s89 offen nt
s_mov_b32 s89, 1280
s_waitcnt vmcnt(59)
buffer_load_dwordx4 v[196:199], v90, %[r2], s89 offen nt
s_mov_b32 s89, 5120
s_waitcnt vmcnt(59)
buffer_load_dwordx4 v[200:203], v90, %[r2], s89 offen nt
s_mov_b32 s89, 6400
s_waitcnt vmcnt(59)
buffer_load_dwordx4 v[204:207], v90, %[r2], s89 offen nt
s_mov_b32 s89, 2560
s_waitcnt vmcnt(59)
buffer_load_dwordx4 v[208:211], v90, %[r2], s89 offen nt
s_mov_b32 s89, 3840
s_waitcnt vmcnt(59)
buffer_load_dwordx4 v[212:215], v90, %[r2], s89 offen nt
s_mov_b32 s89, 7680
s_waitcnt vmcnt(59)
buffer_load_dwordx4 v[216:219], v90, %[r2], s89 offen nt
s_mov_b32 s89, 8960
s_waitcnt vmcnt(59)
buffer_load_dwordx4 v[220:223], v90, %[r2], s89 offen nt
v_perm_b32 v72, v228, v224, s97
v_accvgpr_write_b32 a128, v72
v_perm_b32 v73, v236, v232, s97
v_accvgpr_write_b32 a129, v73
v_perm_b32 v74, v244, v240, s97
v_accvgpr_write_b32 a130, v74
v_perm_b32 v75, v252, v248, s97
v_accvgpr_write_b32 a131, v75
v_perm_b32 v72, v228, v224, s98
v_accvgpr_write_b32 a132, v72
v_perm_b32 v73, v236, v232, s98
v_accvgpr_write_b32 a133, v73
v_perm_b32 v74, v244, v240, s98
v_accvgpr_write_b32 a134, v74
v_perm_b32 v75, v252, v248, s98
v_accvgpr_write_b32 a135, v75
v_perm_b32 v72, v229, v225, s97
v_accvgpr_write_b32 a136, v72
v_perm_b32 v73, v237, v233, s97
v_accvgpr_write_b32 a137, v73
v_perm_b32 v74, v245, v241, s97
v_accvgpr_write_b32 a138, v74
v_perm_b32 v75, v253, v249, s97
v_accvgpr_write_b32 a139, v75
v_perm_b32 v72, v229, v225, s98
v_accvgpr_write_b32 a140, v72
v_perm_b32 v73, v237, v233, s98
v_accvgpr_write_b32 a141, v73
v_perm_b32 v74, v245, v241, s98
v_accvgpr_write_b32 a142, v74
v_perm_b32 v75, v253, v249, s98
v_accvgpr_write_b32 a143, v75
v_perm_b32 v72, v230, v226, s97
v_accvgpr_write_b32 a144, v72
v_perm_b32 v73, v238, v234, s97
v_accvgpr_write_b32 a145, v73
v_perm_b32 v74, v246, v242, s97
v_accvgpr_write_b32 a146, v74
v_perm_b32 v75, v254, v250, s97
v_accvgpr_write_b32 a147, v75
v_perm_b32 v72, v230, v226, s98
v_accvgpr_write_b32 a148, v72
v_perm_b32 v73, v238, v234, s98
v_accvgpr_write_b32 a149, v73
v_perm_b32 v74, v246, v242, s98
v_accvgpr_write_b32 a150, v74
v_perm_b32 v75, v254, v250, s98
v_accvgpr_write_b32 a151, v75
v_perm_b32 v72, v231, v227, s97
v_accvgpr_write_b32 a152, v72
v_perm_b32 v73, v239, v235, s97
v_accvgpr_write_b32 a153, v73
v_perm_b32 v74, v247, v243, s97
v_accvgpr_write_b32 a154, v74
v_perm_b32 v75, v255, v251, s97
v_accvgpr_write_b32 a155, v75
v_perm_b32 v72, v231, v227, s98
v_accvgpr_write_b32 a156, v72
v_perm_b32 v73, v239, v235, s98
v_accvgpr_write_b32 a157, v73
v_perm_b32 v74, v247, v243, s98
v_accvgpr_write_b32 a158, v74
v_perm_b32 v75, v255, v251, s98
v_accvgpr_write_b32 a159, v75
s_mov_b32 s89, 40960
s_waitcnt vmcnt(59)
buffer_load_dwordx4 v[224:227], v90, %[r2], s89 offen nt
s_mov_b32 s89, 42240
s_waitcnt vmcnt(59)
buffer_load_dwordx4 v[228:231], v90, %[r2], s89 offen nt
s_mov_b32 s89, 46080
s_waitcnt vmcnt(59)
buffer_load_dwordx4 v[232:235], v90, %[r2], s89 offen nt
s_mov_b32 s89, 47360
s_waitcnt vmcnt(59)
buffer_load_dwordx4 v[236:239], v90, %[r2], s89 offen nt
s_mov_b32 s89, 43520
s_waitcnt vmcnt(59)
buffer_load_dwordx4 v[240:243], v90, %[r2], s89 offen nt
s_mov_b32 s89, 44800
s_waitcnt vmcnt(59)
buffer_load_dwordx4 v[244:247], v90, %[r2], s89 offen nt
s_mov_b32 s89, 48640
s_waitcnt vmcnt(59)
buffer_load_dwordx4 v[248:251], v90, %[r2], s89 offen nt
s_mov_b32 s89, 49920
s_waitcnt vmcnt(59)
buffer_load_dwordx4 v[252:255], v90, %[r2], s89 offen nt
v_dot2_f32_bf16 v26, v84, s88, 0
v_dot2_f32_bf16 v27, v84, v84, 0
v_dot2_f32_bf16 v26, v85, s88, v26
v_dot2_f32_bf16 v27, v85, v85, v27
v_dot2_f32_bf16 v26, v86, s88, v26
v_dot2_f32_bf16 v27, v86, v86, v27
v_dot2_f32_bf16 v26, v87, s88, v26
v_dot2_f32_bf16 v27, v87, v87, v27
s_nop 1
v_cndmask_b32 v26, 0, v26, s[84:85]
v_cndmask_b32 v27, 0, v27, s[84:85]
s_nop 1
v_permlane32_swap_b32 v26, v27
v_add_f32 v26, v26, v27
v_mov_b32 v28, v26
s_nop 1
v_permlane16_swap_b32 v26, v28
v_add_f32 v26, v26, v28
s_nop 1
v_add_f32_dpp v26, v26, v26 quad_perm:[1,0,3,2] row_mask:0xf bank_mask:0xf
s_nop 1
v_add_f32_dpp v26, v26, v26 quad_perm:[2,3,0,1] row_mask:0xf bank_mask:0xf
s_nop 1
v_add_f32_dpp v26, v26, v26 row_half_mirror row_mask:0xf bank_mask:0xf
s_mov_b32 exec_lo, 0x101
s_mov_b32 exec_hi, 0x101
ds_write_b32 v19, v26 offset:12352
s_mov_b64 exec, -1
v_mov_b32 v40, 0
v_mov_b32 v44, 0
v_mov_b32 v48, 0
v_mov_b32 v52, 0
v_mov_b32 v56, 0
v_mov_b32 v60, 0
v_mov_b32 v64, 0
v_mov_b32 v68, 0
v_and_b32 v36, 32, v19
s_waitcnt lgkmcnt(0)
s_barrier
ds_read_b128 v[26:29], v36 offset:12352
ds_read_b128 v[30:33], v36 offset:12368
s_waitcnt lgkmcnt(0)
v_pk_add_f32 v[26:27], v[26:27], v[28:29]
v_pk_add_f32 v[30:31], v[30:31], v[32:33]
v_pk_add_f32 v[26:27], v[26:27], v[30:31]
v_mul_f32 v26, 0x3acccccd, v26
v_mul_f32 v27, 0x3acccccd, v27
v_fma_f32 v27, -v26, v26, v27
v_cvt_pk_bf16_f32 v26, 0, v26
v_max_f32 v27, 0, v27
v_cvt_pk_bf16_f32 v27, 0, v27
v_add_f32 v27, 0x3727c5ac, v27
v_cvt_pk_bf16_f32 v27, 0, v27
v_sqrt_f32 v27, v27
s_nop 0
v_cvt_pk_bf16_f32 v27, 0, v27
v_rcp_f32 v27, v27
v_lshlrev_b32 v28, 16, v84
v_and_b32 v29, 0xffff0000, v84
v_lshlrev_b32 v30, 16, v85
v_and_b32 v31, 0xffff0000, v85
)ASM"
R"ASM(v_lshlrev_b32 v32, 16, v86
v_and_b32 v33, 0xffff0000, v86
v_lshlrev_b32 v34, 16, v87
v_and_b32 v35, 0xffff0000, v87
v_sub_f32 v28, v28, v26
v_sub_f32 v29, v29, v26
v_sub_f32 v30, v30, v26
v_sub_f32 v31, v31, v26
v_sub_f32 v32, v32, v26
v_sub_f32 v33, v33, v26
v_sub_f32 v34, v34, v26
v_sub_f32 v35, v35, v26
v_cvt_pk_bf16_f32 v28, 0, v28
v_cvt_pk_bf16_f32 v29, 0, v29
v_cvt_pk_bf16_f32 v30, 0, v30
v_cvt_pk_bf16_f32 v31, 0, v31
v_cvt_pk_bf16_f32 v32, 0, v32
v_cvt_pk_bf16_f32 v33, 0, v33
v_cvt_pk_bf16_f32 v34, 0, v34
v_cvt_pk_bf16_f32 v35, 0, v35
v_mul_f32 v28, v28, v27
v_mul_f32 v29, v29, v27
v_mul_f32 v30, v30, v27
v_mul_f32 v31, v31, v27
v_mul_f32 v32, v32, v27
v_mul_f32 v33, v33, v27
v_mul_f32 v34, v34, v27
v_mul_f32 v35, v35, v27
v_cvt_pk_bf16_f32 v84, v28, v29
v_cvt_pk_bf16_f32 v85, v30, v31
v_cvt_pk_bf16_f32 v86, v32, v33
v_cvt_pk_bf16_f32 v87, v34, v35
s_nop 1
v_smfmac_f32_16x16x64_bf16 v[40:43], v[84:87], a[0:7], v17
v_mov_b32_dpp v28, v84 row_shl:1 row_mask:0xf bank_mask:0xf bound_ctrl:1
v_smfmac_f32_16x16x64_bf16 v[56:59], v[84:87], a[32:39], v17
v_mov_b32_dpp v29, v85 row_shl:1 row_mask:0xf bank_mask:0xf bound_ctrl:1
v_smfmac_f32_16x16x64_bf16 v[44:47], v[84:87], a[8:15], v17
v_mov_b32_dpp v30, v86 row_shl:1 row_mask:0xf bank_mask:0xf bound_ctrl:1
v_smfmac_f32_16x16x64_bf16 v[60:63], v[84:87], a[40:47], v17
v_mov_b32_dpp v31, v87 row_shl:1 row_mask:0xf bank_mask:0xf bound_ctrl:1
v_smfmac_f32_16x16x64_bf16 v[48:51], v[84:87], a[16:23], v17
v_mov_b32_dpp v32, v84 row_shl:2 row_mask:0xf bank_mask:0x5 bound_ctrl:1
v_smfmac_f32_16x16x64_bf16 v[64:67], v[84:87], a[48:55], v17
v_mov_b32_dpp v33, v85 row_shl:2 row_mask:0xf bank_mask:0x5 bound_ctrl:1
v_smfmac_f32_16x16x64_bf16 v[52:55], v[84:87], a[24:31], v17
v_mov_b32_dpp v32, v86 row_shr:2 row_mask:0xf bank_mask:0xa bound_ctrl:1
v_smfmac_f32_16x16x64_bf16 v[68:71], v[84:87], a[56:63], v17
v_mov_b32_dpp v33, v87 row_shr:2 row_mask:0xf bank_mask:0xa bound_ctrl:1
v_smfmac_f32_16x16x64_bf16 v[40:43], v[28:31], a[64:71], v17
v_smfmac_f32_16x16x64_bf16 v[56:59], v[28:31], a[96:103], v17
v_smfmac_f32_16x16x64_bf16 v[44:47], v[28:31], a[72:79], v17
v_smfmac_f32_16x16x64_bf16 v[60:63], v[28:31], a[104:111], v17
v_smfmac_f32_16x16x64_bf16 v[48:51], v[28:31], a[80:87], v17
v_smfmac_f32_16x16x64_bf16 v[64:67], v[28:31], a[112:119], v17
v_smfmac_f32_16x16x64_bf16 v[52:55], v[28:31], a[88:95], v17
v_smfmac_f32_16x16x64_bf16 v[68:71], v[28:31], a[120:127], v17
v_smfmac_f32_16x16x32_bf16 v[40:43], v[32:33], a[128:131], v17
v_smfmac_f32_16x16x32_bf16 v[56:59], v[32:33], a[144:147], v17
v_smfmac_f32_16x16x32_bf16 v[44:47], v[32:33], a[132:135], v17
v_smfmac_f32_16x16x32_bf16 v[60:63], v[32:33], a[148:151], v17
v_smfmac_f32_16x16x32_bf16 v[48:51], v[32:33], a[136:139], v17
v_smfmac_f32_16x16x32_bf16 v[64:67], v[32:33], a[152:155], v17
v_smfmac_f32_16x16x32_bf16 v[52:55], v[32:33], a[140:143], v17
v_smfmac_f32_16x16x32_bf16 v[68:71], v[32:33], a[156:159], v17
s_nop 1
v_permlane16_swap_b32 v40, v56
v_add_f32 v72, v40, v56
v_permlane16_swap_b32 v44, v60
v_add_f32 v73, v44, v60
v_permlane16_swap_b32 v48, v64
v_add_f32 v74, v48, v64
v_permlane16_swap_b32 v52, v68
v_add_f32 v75, v52, v68
ds_write_b128 v8, v[72:75] offset:2560
v_lshl_add_u32 v93, v24, 2, v92
v_add_u32 v93, 0x100, v93
v_lshl_add_u32 v92, v24, 4, v92
s_and_b32 s63, %[w], 1
s_xor_b32 s63, s63, 1
s_mul_i32 s64, s63, 2560
s_add_u32 s64, s64, 2560
v_add_u32 v36, s64, v12
s_waitcnt lgkmcnt(0)
s_barrier
s_cmp_lt_u32 %[w], 2
s_cbranch_scc1 Cnoq1_%=
ds_read2_b64 v[26:29], v36 offset1:80
ds_read2_b64 v[30:33], v36 offset0:160 offset1:240
s_lshl_b32 s64, s63, 8
s_lshl_b32 s89, %[j], 9
s_add_u32 s89, s89, s64
s_add_u32 s89, s89, 2129920
s_waitcnt lgkmcnt(0)
v_pk_add_f32 v[26:27], v[26:27], v[28:29]
v_pk_add_f32 v[30:31], v[30:31], v[32:33]
v_pk_add_f32 v[26:27], v[26:27], v[30:31]
v_cvt_pk_bf16_f32 v13, v26, v27
v_min_u32 v26, -2, v13
buffer_store_dword v26, v11, %[rw], s89 offen sc1
Cnoq1_%=:
s_nop 4
s_cmp_lg_u32 %[j], 31
s_cbranch_scc1 Cnomeas_%=
s_and_b32 s63, %[w], 1
s_lshl_b32 s63, s63, 13
s_lshl_b32 s89, %[w], 16
s_add_u32 s89, s89, s63
s_add_u32 s89, s89, 7168
s_mov_b32 s94, -1
s_mov_b32 s95, -1
s_mov_b64 exec, 0x1
s_memtime s[90:91]
s_waitcnt lgkmcnt(0)
buffer_store_dword v22, off, %[rw], s89 sc1
s_waitcnt vmcnt(0)
s_memtime s[92:93]
s_xor_b32 s89, s89, 0x2000
s_waitcnt lgkmcnt(0)
s_sub_u32 s90, s92, s90
s_memtime s[90:91]
s_waitcnt lgkmcnt(0)
buffer_store_dword v22, off, %[rw], s89 sc1
s_waitcnt vmcnt(0)
s_memtime s[92:93]
s_xor_b32 s89, s89, 0x2000
s_waitcnt lgkmcnt(0)
s_sub_u32 s90, s92, s90
s_min_u32 s95, s95, s90
s_memtime s[90:91]
s_waitcnt lgkmcnt(0)
buffer_store_dword v22, off, %[rw], s89 offset:128 sc1
s_waitcnt vmcnt(0)
s_memtime s[92:93]
s_xor_b32 s89, s89, 0x2000
s_waitcnt lgkmcnt(0)
s_sub_u32 s90, s92, s90
s_min_u32 s94, s94, s90
s_memtime s[90:91]
s_waitcnt lgkmcnt(0)
buffer_store_dword v22, off, %[rw], s89 offset:128 sc1
s_waitcnt vmcnt(0)
s_memtime s[92:93]
s_xor_b32 s89, s89, 0x2000
s_waitcnt lgkmcnt(0)
s_sub_u32 s90, s92, s90
s_min_u32 s95, s95, s90
s_memtime s[90:91]
s_waitcnt lgkmcnt(0)
buffer_store_dword v22, off, %[rw], s89 offset:256 sc1
s_waitcnt vmcnt(0)
s_memtime s[92:93]
s_xor_b32 s89, s89, 0x2000
s_waitcnt lgkmcnt(0)
s_sub_u32 s90, s92, s90
s_min_u32 s94, s94, s90
s_memtime s[90:91]
s_waitcnt lgkmcnt(0)
buffer_store_dword v22, off, %[rw], s89 offset:256 sc1
s_waitcnt vmcnt(0)
s_memtime s[92:93]
s_xor_b32 s89, s89, 0x2000
s_waitcnt lgkmcnt(0)
s_sub_u32 s90, s92, s90
s_min_u32 s95, s95, s90
s_memtime s[90:91]
s_waitcnt lgkmcnt(0)
buffer_store_dword v22, off, %[rw], s89 offset:384 sc1
s_waitcnt vmcnt(0)
s_memtime s[92:93]
s_xor_b32 s89, s89, 0x2000
s_waitcnt lgkmcnt(0)
s_sub_u32 s90, s92, s90
s_min_u32 s94, s94, s90
s_memtime s[90:91]
s_waitcnt lgkmcnt(0)
buffer_store_dword v22, off, %[rw], s89 offset:384 sc1
s_waitcnt vmcnt(0)
s_memtime s[92:93]
s_xor_b32 s89, s89, 0x2000
s_waitcnt lgkmcnt(0)
s_sub_u32 s90, s92, s90
s_min_u32 s95, s95, s90
s_memtime s[90:91]
s_waitcnt lgkmcnt(0)
buffer_store_dword v22, off, %[rw], s89 offset:512 sc1
s_waitcnt vmcnt(0)
s_memtime s[92:93]
s_xor_b32 s89, s89, 0x2000
s_waitcnt lgkmcnt(0)
s_sub_u32 s90, s92, s90
s_min_u32 s94, s94, s90
s_memtime s[90:91]
s_waitcnt lgkmcnt(0)
buffer_store_dword v22, off, %[rw], s89 offset:512 sc1
s_waitcnt vmcnt(0)
s_memtime s[92:93]
s_xor_b32 s89, s89, 0x2000
s_waitcnt lgkmcnt(0)
s_sub_u32 s90, s92, s90
s_min_u32 s95, s95, s90
s_memtime s[90:91]
s_waitcnt lgkmcnt(0)
buffer_store_dword v22, off, %[rw], s89 offset:640 sc1
s_waitcnt vmcnt(0)
s_memtime s[92:93]
s_xor_b32 s89, s89, 0x2000
s_waitcnt lgkmcnt(0)
s_sub_u32 s90, s92, s90
s_min_u32 s94, s94, s90
s_and_b32 s63, %[w], 1
s_lshl_b32 s64, %[w], 1
s_add_u32 s63, s63, s64
s_lshl_b32 s63, s63, 2
s_xor_b32 s64, s63, 4
v_mov_b32 v25, s64
v_mov_b32 v26, s95
ds_write_b32 v25, v26 offset:12928
v_mov_b32 v25, s63
v_mov_b32 v26, s94
ds_write_b32 v25, v26 offset:12928
s_waitcnt lgkmcnt(0)
s_barrier
v_mov_b32 v25, 0
ds_read_b128 v[26:29], v25 offset:12928
ds_read_b128 v[30:33], v25 offset:12944
s_waitcnt lgkmcnt(0)
v_min3_u32 v26, v26, v28, v30
v_min3_u32 v27, v27, v29, v31
v_min_u32 v26, v26, v32
v_min_u32 v27, v27, v33
v_cmp_lt_u32 vcc, v26, v27
v_mov_b32 v28, 8
s_nop 0
v_cndmask_b32 v25, 0, v28, vcc
v_mov_b32 v26, s60
v_mov_b32 v27, s61
s_cmp_lg_u32 %[w], 0
s_cbranch_scc1 Cnopub_%=
s_mov_b32 s89, 2109440
v_mov_b32 v28, s80
v_not_b32 v29, v27
v_cndmask_b32 v30, v26, v28, vcc
v_cndmask_b32 v31, v27, v29, vcc
v_cndmask_b32 v32, v28, v26, vcc
v_cndmask_b32 v33, v29, v27, vcc
buffer_store_dwordx4 v[30:33], off, %[rw], s89 sc1
Cnopub_%=:
s_mov_b64 exec, -1
s_nop 4
Cnomeas_%=:
v_perm_b32 v80, v98, v96, s97
v_accvgpr_write_b32 a160, v80
v_perm_b32 v81, v102, v100, s97
v_accvgpr_write_b32 a161, v81
v_perm_b32 v82, v106, v104, s97
v_accvgpr_write_b32 a162, v82
v_perm_b32 v83, v110, v108, s97
v_accvgpr_write_b32 a163, v83
v_perm_b32 v80, v98, v96, s98
v_accvgpr_write_b32 a168, v80
v_perm_b32 v81, v102, v100, s98
v_accvgpr_write_b32 a169, v81
v_perm_b32 v82, v106, v104, s98
v_accvgpr_write_b32 a170, v82
v_perm_b32 v83, v110, v108, s98
v_accvgpr_write_b32 a171, v83
v_perm_b32 v80, v99, v97, s97
v_accvgpr_write_b32 a176, v80
v_perm_b32 v81, v103, v101, s97
v_accvgpr_write_b32 a177, v81
v_perm_b32 v82, v107, v105, s97
v_accvgpr_write_b32 a178, v82
v_perm_b32 v83, v111, v109, s97
v_accvgpr_write_b32 a179, v83
v_perm_b32 v80, v99, v97, s98
v_accvgpr_write_b32 a184, v80
v_perm_b32 v81, v103, v101, s98
v_accvgpr_write_b32 a185, v81
v_perm_b32 v82, v107, v105, s98
v_accvgpr_write_b32 a186, v82
v_perm_b32 v83, v111, v109, s98
v_accvgpr_write_b32 a187, v83
s_waitcnt vmcnt(56)
v_perm_b32 v80, v114, v112, s97
v_accvgpr_write_b32 a164, v80
v_perm_b32 v81, v118, v116, s97
v_accvgpr_write_b32 a165, v81
v_perm_b32 v82, v122, v120, s97
v_accvgpr_write_b32 a166, v82
v_perm_b32 v83, v126, v124, s97
v_accvgpr_write_b32 a167, v83
v_perm_b32 v80, v114, v112, s98
v_accvgpr_write_b32 a172, v80
v_perm_b32 v81, v118, v116, s98
v_accvgpr_write_b32 a173, v81
v_perm_b32 v82, v122, v120, s98
v_accvgpr_write_b32 a174, v82
v_perm_b32 v83, v126, v124, s98
v_accvgpr_write_b32 a175, v83
v_perm_b32 v80, v115, v113, s97
v_accvgpr_write_b32 a180, v80
v_perm_b32 v81, v119, v117, s97
v_accvgpr_write_b32 a181, v81
v_perm_b32 v82, v123, v121, s97
v_accvgpr_write_b32 a182, v82
v_perm_b32 v83, v127, v125, s97
v_accvgpr_write_b32 a183, v83
v_perm_b32 v80, v115, v113, s98
v_accvgpr_write_b32 a188, v80
v_perm_b32 v81, v119, v117, s98
v_accvgpr_write_b32 a189, v81
v_perm_b32 v82, v123, v121, s98
v_accvgpr_write_b32 a190, v82
v_perm_b32 v83, v127, v125, s98
v_accvgpr_write_b32 a191, v83
v_mov_b32 v126, v92
v_mov_b32 v127, v93
s_mov_b32 s89, 0
buffer_load_dwordx4 v[22:25], v126, %[ro], s89 offen nt
s_mov_b32 s89, 1280
buffer_load_dwordx4 v[26:29], v126, %[ro], s89 offen nt
s_mov_b32 s89, 20480
buffer_load_dwordx4 v[30:33], v126, %[ro], s89 offen nt
s_mov_b32 s89, 21760
buffer_load_dwordx4 v[34:37], v126, %[ro], s89 offen nt
s_mov_b32 s89, 5120
s_waitcnt vmcnt(59)
buffer_load_dwordx4 v[38:41], v126, %[ro], s89 offen nt
s_mov_b32 s89, 6400
s_waitcnt vmcnt(59)
buffer_load_dwordx4 v[42:45], v126, %[ro], s89 offen nt
s_mov_b32 s89, 25600
s_waitcnt vmcnt(59)
buffer_load_dwordx4 v[46:49], v126, %[ro], s89 offen nt
s_mov_b32 s89, 26880
s_waitcnt vmcnt(59)
buffer_load_dwordx4 v[50:53], v126, %[ro], s89 offen nt
s_mov_b32 s89, 0
s_waitcnt vmcnt(59)
buffer_load_dword v54, v127, %[ro], s89 offen nt
s_mov_b32 s89, 1280
s_waitcnt vmcnt(59)
buffer_load_dword v55, v127, %[ro], s89 offen nt
s_mov_b32 s89, 20480
s_waitcnt vmcnt(59)
buffer_load_dword v56, v127, %[ro], s89 offen nt
s_mov_b32 s89, 21760
s_waitcnt vmcnt(59)
buffer_load_dword v57, v127, %[ro], s89 offen nt
s_mov_b32 s89, 5120
s_waitcnt vmcnt(59)
buffer_load_dword v58, v127, %[ro], s89 offen nt
s_mov_b32 s89, 6400
s_waitcnt vmcnt(59)
buffer_load_dword v59, v127, %[ro], s89 offen nt
s_mov_b32 s89, 25600
s_waitcnt vmcnt(59)
buffer_load_dword v60, v127, %[ro], s89 offen nt
s_mov_b32 s89, 26880
s_waitcnt vmcnt(59)
buffer_load_dword v61, v127, %[ro], s89 offen nt
s_mov_b32 s89, 2560
s_waitcnt vmcnt(59)
buffer_load_dwordx4 v[62:65], v126, %[ro], s89 offen nt
s_mov_b32 s89, 3840
s_waitcnt vmcnt(59)
buffer_load_dwordx4 v[66:69], v126, %[ro], s89 offen nt
s_mov_b32 s89, 23040
s_waitcnt vmcnt(59)
buffer_load_dwordx4 v[70:73], v126, %[ro], s89 offen nt
s_mov_b32 s89, 24320
s_waitcnt vmcnt(59)
buffer_load_dwordx4 v[74:77], v126, %[ro], s89 offen nt
s_mov_b32 s89, 7680
s_waitcnt vmcnt(59)
buffer_load_dwordx4 v[78:81], v126, %[ro], s89 offen nt
s_mov_b32 s89, 8960
s_waitcnt vmcnt(59)
buffer_load_dwordx4 v[82:85], v126, %[ro], s89 offen nt
s_mov_b32 s89, 28160
s_waitcnt vmcnt(59)
buffer_load_dwordx4 v[86:89], v126, %[ro], s89 offen nt
s_mov_b32 s89, 29440
s_waitcnt vmcnt(59)
buffer_load_dwordx4 v[90:93], v126, %[ro], s89 offen nt
s_mov_b32 s89, 2560
s_waitcnt vmcnt(59)
buffer_load_dword v94, v127, %[ro], s89 offen nt
s_mov_b32 s89, 3840
s_waitcnt vmcnt(59)
buffer_load_dword v95, v127, %[ro], s89 offen nt
s_mov_b32 s89, 23040
s_waitcnt vmcnt(59)
buffer_load_dword v96, v127, %[ro], s89 offen nt
s_mov_b32 s89, 24320
s_waitcnt vmcnt(59)
buffer_load_dword v97, v127, %[ro], s89 offen nt
s_mov_b32 s89, 7680
s_waitcnt vmcnt(59)
)ASM"
R"ASM(buffer_load_dword v98, v127, %[ro], s89 offen nt
s_mov_b32 s89, 8960
s_waitcnt vmcnt(59)
buffer_load_dword v99, v127, %[ro], s89 offen nt
s_mov_b32 s89, 28160
s_waitcnt vmcnt(59)
buffer_load_dword v100, v127, %[ro], s89 offen nt
s_mov_b32 s89, 29440
s_waitcnt vmcnt(59)
buffer_load_dword v101, v127, %[ro], s89 offen nt
v_perm_b32 v122, v130, v128, s97
v_accvgpr_write_b32 a192, v122
v_perm_b32 v123, v134, v132, s97
v_accvgpr_write_b32 a193, v123
v_perm_b32 v124, v138, v136, s97
v_accvgpr_write_b32 a194, v124
v_perm_b32 v125, v142, v140, s97
v_accvgpr_write_b32 a195, v125
v_perm_b32 v122, v130, v128, s98
v_accvgpr_write_b32 a200, v122
v_perm_b32 v123, v134, v132, s98
v_accvgpr_write_b32 a201, v123
v_perm_b32 v124, v138, v136, s98
v_accvgpr_write_b32 a202, v124
v_perm_b32 v125, v142, v140, s98
v_accvgpr_write_b32 a203, v125
v_perm_b32 v122, v131, v129, s97
v_accvgpr_write_b32 a208, v122
v_perm_b32 v123, v135, v133, s97
v_accvgpr_write_b32 a209, v123
v_perm_b32 v124, v139, v137, s97
v_accvgpr_write_b32 a210, v124
v_perm_b32 v125, v143, v141, s97
v_accvgpr_write_b32 a211, v125
v_perm_b32 v122, v131, v129, s98
v_accvgpr_write_b32 a216, v122
v_perm_b32 v123, v135, v133, s98
v_accvgpr_write_b32 a217, v123
v_perm_b32 v124, v139, v137, s98
v_accvgpr_write_b32 a218, v124
v_perm_b32 v125, v143, v141, s98
v_accvgpr_write_b32 a219, v125
v_perm_b32 v122, v146, v144, s97
v_accvgpr_write_b32 a196, v122
v_perm_b32 v123, v150, v148, s97
v_accvgpr_write_b32 a197, v123
v_perm_b32 v124, v154, v152, s97
v_accvgpr_write_b32 a198, v124
v_perm_b32 v125, v158, v156, s97
v_accvgpr_write_b32 a199, v125
v_perm_b32 v122, v146, v144, s98
v_accvgpr_write_b32 a204, v122
v_perm_b32 v123, v150, v148, s98
v_accvgpr_write_b32 a205, v123
v_perm_b32 v124, v154, v152, s98
v_accvgpr_write_b32 a206, v124
v_perm_b32 v125, v158, v156, s98
v_accvgpr_write_b32 a207, v125
v_perm_b32 v122, v147, v145, s97
v_accvgpr_write_b32 a212, v122
v_perm_b32 v123, v151, v149, s97
v_accvgpr_write_b32 a213, v123
v_perm_b32 v124, v155, v153, s97
v_accvgpr_write_b32 a214, v124
v_perm_b32 v125, v159, v157, s97
v_accvgpr_write_b32 a215, v125
v_perm_b32 v122, v147, v145, s98
v_accvgpr_write_b32 a220, v122
v_perm_b32 v123, v151, v149, s98
v_accvgpr_write_b32 a221, v123
v_perm_b32 v124, v155, v153, s98
v_accvgpr_write_b32 a222, v124
v_perm_b32 v125, v159, v157, s98
v_accvgpr_write_b32 a223, v125
v_perm_b32 v122, v162, v160, s97
v_accvgpr_write_b32 a224, v122
v_perm_b32 v123, v166, v164, s97
v_accvgpr_write_b32 a225, v123
v_perm_b32 v124, v170, v168, s97
v_accvgpr_write_b32 a226, v124
v_perm_b32 v125, v174, v172, s97
v_accvgpr_write_b32 a227, v125
v_perm_b32 v122, v162, v160, s98
v_accvgpr_write_b32 a228, v122
v_perm_b32 v123, v166, v164, s98
v_accvgpr_write_b32 a229, v123
v_perm_b32 v124, v170, v168, s98
v_accvgpr_write_b32 a230, v124
v_perm_b32 v125, v174, v172, s98
v_accvgpr_write_b32 a231, v125
v_perm_b32 v122, v163, v161, s97
v_accvgpr_write_b32 a232, v122
v_perm_b32 v123, v167, v165, s97
v_accvgpr_write_b32 a233, v123
v_perm_b32 v124, v171, v169, s97
v_accvgpr_write_b32 a234, v124
v_perm_b32 v125, v175, v173, s97
v_accvgpr_write_b32 a235, v125
v_perm_b32 v122, v163, v161, s98
v_accvgpr_write_b32 a236, v122
v_perm_b32 v123, v167, v165, s98
v_accvgpr_write_b32 a237, v123
v_perm_b32 v124, v171, v169, s98
v_accvgpr_write_b32 a238, v124
v_perm_b32 v125, v175, v173, s98
v_accvgpr_write_b32 a239, v125
s_waitcnt vmcnt(40)
v_perm_b32 v114, v196, v192, s97
v_perm_b32 v115, v204, v200, s97
v_perm_b32 v116, v212, v208, s97
v_perm_b32 v117, v220, v216, s97
ds_write_b128 v20, v[114:117] offset:0
v_perm_b32 v118, v196, v192, s98
v_perm_b32 v119, v204, v200, s98
v_perm_b32 v120, v212, v208, s98
v_perm_b32 v121, v220, v216, s98
ds_write_b128 v20, v[118:121] offset:2048
v_perm_b32 v114, v197, v193, s97
v_perm_b32 v115, v205, v201, s97
v_perm_b32 v116, v213, v209, s97
v_perm_b32 v117, v221, v217, s97
ds_write_b128 v20, v[114:117] offset:4096
v_perm_b32 v118, v197, v193, s98
v_perm_b32 v119, v205, v201, s98
v_perm_b32 v120, v213, v209, s98
v_perm_b32 v121, v221, v217, s98
ds_write_b128 v20, v[118:121] offset:6144
v_perm_b32 v114, v198, v194, s97
v_perm_b32 v115, v206, v202, s97
v_perm_b32 v116, v214, v210, s97
v_perm_b32 v117, v222, v218, s97
ds_write_b128 v20, v[114:117] offset:8192
v_perm_b32 v118, v198, v194, s98
v_perm_b32 v119, v206, v202, s98
v_perm_b32 v120, v214, v210, s98
v_perm_b32 v121, v222, v218, s98
ds_write_b128 v20, v[118:121] offset:10240
v_perm_b32 v114, v199, v195, s97
v_perm_b32 v115, v207, v203, s97
v_perm_b32 v116, v215, v211, s97
v_perm_b32 v117, v223, v219, s97
ds_write_b128 v20, v[114:117] offset:12288
v_perm_b32 v118, v199, v195, s98
v_perm_b32 v119, v207, v203, s98
v_perm_b32 v120, v215, v211, s98
v_perm_b32 v121, v223, v219, s98
ds_write_b128 v20, v[118:121] offset:14336
v_perm_b32 v122, v177, v176, s97
v_accvgpr_write_b32 a240, v122
v_perm_b32 v123, v179, v178, s97
v_accvgpr_write_b32 a241, v123
v_perm_b32 v124, v181, v180, s97
v_accvgpr_write_b32 a242, v124
v_perm_b32 v125, v183, v182, s97
v_accvgpr_write_b32 a243, v125
v_perm_b32 v122, v177, v176, s98
v_accvgpr_write_b32 a248, v122
v_perm_b32 v123, v179, v178, s98
v_accvgpr_write_b32 a249, v123
v_perm_b32 v124, v181, v180, s98
v_accvgpr_write_b32 a250, v124
v_perm_b32 v125, v183, v182, s98
v_accvgpr_write_b32 a251, v125
s_waitcnt vmcnt(32)
v_perm_b32 v114, v228, v224, s97
v_perm_b32 v115, v236, v232, s97
v_perm_b32 v116, v244, v240, s97
v_perm_b32 v117, v252, v248, s97
ds_write_b128 v20, v[114:117] offset:1024
v_perm_b32 v118, v228, v224, s98
v_perm_b32 v119, v236, v232, s98
v_perm_b32 v120, v244, v240, s98
v_perm_b32 v121, v252, v248, s98
ds_write_b128 v20, v[118:121] offset:3072
v_perm_b32 v114, v229, v225, s97
v_perm_b32 v115, v237, v233, s97
v_perm_b32 v116, v245, v241, s97
v_perm_b32 v117, v253, v249, s97
ds_write_b128 v20, v[114:117] offset:5120
v_perm_b32 v118, v229, v225, s98
v_perm_b32 v119, v237, v233, s98
v_perm_b32 v120, v245, v241, s98
v_perm_b32 v121, v253, v249, s98
ds_write_b128 v20, v[118:121] offset:7168
v_perm_b32 v114, v230, v226, s97
v_perm_b32 v115, v238, v234, s97
v_perm_b32 v116, v246, v242, s97
v_perm_b32 v117, v254, v250, s97
ds_write_b128 v20, v[114:117] offset:9216
v_perm_b32 v118, v230, v226, s98
v_perm_b32 v119, v238, v234, s98
v_perm_b32 v120, v246, v242, s98
v_perm_b32 v121, v254, v250, s98
ds_write_b128 v20, v[118:121] offset:11264
v_perm_b32 v114, v231, v227, s97
v_perm_b32 v115, v239, v235, s97
v_perm_b32 v116, v247, v243, s97
v_perm_b32 v117, v255, v251, s97
ds_write_b128 v20, v[114:117] offset:13312
v_perm_b32 v118, v231, v227, s98
v_perm_b32 v119, v239, v235, s98
v_perm_b32 v120, v247, v243, s98
v_perm_b32 v121, v255, v251, s98
s_waitcnt lgkmcnt(14)
ds_write_b128 v20, v[118:121] offset:15360
v_perm_b32 v122, v185, v184, s97
v_accvgpr_write_b32 a244, v122
v_perm_b32 v123, v187, v186, s97
v_accvgpr_write_b32 a245, v123
v_perm_b32 v124, v189, v188, s97
v_accvgpr_write_b32 a246, v124
v_perm_b32 v125, v191, v190, s97
v_accvgpr_write_b32 a247, v125
v_perm_b32 v122, v185, v184, s98
v_accvgpr_write_b32 a252, v122
v_perm_b32 v123, v187, v186, s98
v_accvgpr_write_b32 a253, v123
v_perm_b32 v124, v189, v188, s98
v_accvgpr_write_b32 a254, v124
v_perm_b32 v125, v191, v190, s98
v_accvgpr_write_b32 a255, v125
s_mov_b32 s89, 40960
buffer_load_dwordx4 v[196:199], v126, %[ro], s89 offen nt
s_mov_b32 s89, 42240
buffer_load_dwordx4 v[204:207], v126, %[ro], s89 offen nt
s_mov_b32 s89, 61440
buffer_load_dwordx4 v[212:215], v126, %[ro], s89 offen nt
s_mov_b32 s89, 62720
buffer_load_dwordx4 v[220:223], v126, %[ro], s89 offen nt
s_mov_b32 s89, 46080
buffer_load_dwordx4 v[228:231], v126, %[ro], s89 offen nt
s_mov_b32 s89, 47360
buffer_load_dwordx4 v[236:239], v126, %[ro], s89 offen nt
s_mov_b32 s89, 66560
buffer_load_dwordx4 v[244:247], v126, %[ro], s89 offen nt
s_mov_b32 s89, 67840
buffer_load_dwordx4 v[252:255], v126, %[ro], s89 offen nt
s_mov_b32 s89, 40960
buffer_load_dword v102, v127, %[ro], s89 offen nt
s_mov_b32 s89, 42240
buffer_load_dword v103, v127, %[ro], s89 offen nt
s_mov_b32 s89, 61440
buffer_load_dword v104, v127, %[ro], s89 offen nt
s_mov_b32 s89, 62720
buffer_load_dword v105, v127, %[ro], s89 offen nt
s_mov_b32 s89, 46080
buffer_load_dword v106, v127, %[ro], s89 offen nt
s_mov_b32 s89, 47360
buffer_load_dword v107, v127, %[ro], s89 offen nt
s_mov_b32 s89, 66560
buffer_load_dword v108, v127, %[ro], s89 offen nt
s_mov_b32 s89, 67840
buffer_load_dword v109, v127, %[ro], s89 offen nt
s_waitcnt vmcnt(32)
v_perm_b32 v128, v26, v22, s97
v_perm_b32 v129, v34, v30, s97
v_perm_b32 v130, v42, v38, s97
v_perm_b32 v131, v50, v46, s97
v_perm_b32 v136, v26, v22, s98
v_perm_b32 v137, v34, v30, s98
v_perm_b32 v138, v42, v38, s98
v_perm_b32 v139, v50, v46, s98
v_perm_b32 v144, v27, v23, s97
v_perm_b32 v145, v35, v31, s97
v_perm_b32 v146, v43, v39, s97
v_perm_b32 v147, v51, v47, s97
v_perm_b32 v152, v27, v23, s98
v_perm_b32 v153, v35, v31, s98
v_perm_b32 v154, v43, v39, s98
v_perm_b32 v155, v51, v47, s98
v_perm_b32 v160, v28, v24, s97
v_perm_b32 v161, v36, v32, s97
v_perm_b32 v162, v44, v40, s97
v_perm_b32 v163, v52, v48, s97
v_perm_b32 v168, v28, v24, s98
v_perm_b32 v169, v36, v32, s98
v_perm_b32 v170, v44, v40, s98
v_perm_b32 v171, v52, v48, s98
v_perm_b32 v176, v29, v25, s97
v_perm_b32 v177, v37, v33, s97
v_perm_b32 v178, v45, v41, s97
v_perm_b32 v179, v53, v49, s97
v_perm_b32 v184, v29, v25, s98
v_perm_b32 v185, v37, v33, s98
v_perm_b32 v186, v45, v41, s98
v_perm_b32 v187, v53, v49, s98
v_perm_b32 v114, v55, v54, s97
v_perm_b32 v115, v57, v56, s97
v_perm_b32 v116, v59, v58, s97
v_perm_b32 v117, v61, v60, s97
s_waitcnt lgkmcnt(14)
ds_write_b128 v20, v[114:117] offset:16384
v_perm_b32 v118, v55, v54, s98
v_perm_b32 v119, v57, v56, s98
v_perm_b32 v120, v59, v58, s98
v_perm_b32 v121, v61, v60, s98
s_waitcnt lgkmcnt(14)
ds_write_b128 v20, v[118:121] offset:18432
s_mov_b32 s89, 43520
buffer_load_dwordx4 v[22:25], v126, %[ro], s89 offen nt
s_mov_b32 s89, 44800
buffer_load_dwordx4 v[26:29], v126, %[ro], s89 offen nt
s_mov_b32 s89, 64000
buffer_load_dwordx4 v[30:33], v126, %[ro], s89 offen nt
s_mov_b32 s89, 65280
buffer_load_dwordx4 v[34:37], v126, %[ro], s89 offen nt
s_mov_b32 s89, 48640
buffer_load_dwordx4 v[38:41], v126, %[ro], s89 offen nt
s_mov_b32 s89, 49920
buffer_load_dwordx4 v[42:45], v126, %[ro], s89 offen nt
s_mov_b32 s89, 69120
buffer_load_dwordx4 v[46:49], v126, %[ro], s89 offen nt
s_mov_b32 s89, 70400
buffer_load_dwordx4 v[50:53], v126, %[ro], s89 offen nt
s_mov_b32 s89, 43520
buffer_load_dword v54, v127, %[ro], s89 offen nt
s_mov_b32 s89, 44800
buffer_load_dword v55, v127, %[ro], s89 offen nt
s_mov_b32 s89, 64000
buffer_load_dword v56, v127, %[ro], s89 offen nt
s_mov_b32 s89, 65280
buffer_load_dword v57, v127, %[ro], s89 offen nt
s_mov_b32 s89, 48640
buffer_load_dword v58, v127, %[ro], s89 offen nt
s_mov_b32 s89, 49920
buffer_load_dword v59, v127, %[ro], s89 offen nt
s_mov_b32 s89, 69120
buffer_load_dword v60, v127, %[ro], s89 offen nt
s_mov_b32 s89, 70400
buffer_load_dword v61, v127, %[ro], s89 offen nt
s_waitcnt vmcnt(32)
v_perm_b32 v132, v66, v62, s97
v_perm_b32 v133, v74, v70, s97
v_perm_b32 v134, v82, v78, s97
v_perm_b32 v135, v90, v86, s97
v_perm_b32 v140, v66, v62, s98
v_perm_b32 v141, v74, v70, s98
v_perm_b32 v142, v82, v78, s98
v_perm_b32 v143, v90, v86, s98
v_perm_b32 v148, v67, v63, s97
v_perm_b32 v149, v75, v71, s97
v_perm_b32 v150, v83, v79, s97
v_perm_b32 v151, v91, v87, s97
v_perm_b32 v156, v67, v63, s98
v_perm_b32 v157, v75, v71, s98
v_perm_b32 v158, v83, v79, s98
v_perm_b32 v159, v91, v87, s98
v_perm_b32 v164, v68, v64, s97
v_perm_b32 v165, v76, v72, s97
v_perm_b32 v166, v84, v80, s97
v_perm_b32 v167, v92, v88, s97
v_perm_b32 v172, v68, v64, s98
v_perm_b32 v173, v76, v72, s98
v_perm_b32 v174, v84, v80, s98
v_perm_b32 v175, v92, v88, s98
v_perm_b32 v180, v69, v65, s97
v_perm_b32 v181, v77, v73, s97
v_perm_b32 v182, v85, v81, s97
v_perm_b32 v183, v93, v89, s97
v_perm_b32 v188, v69, v65, s98
v_perm_b32 v189, v77, v73, s98
v_perm_b32 v190, v85, v81, s98
v_perm_b32 v191, v93, v89, s98
v_perm_b32 v114, v95, v94, s97
v_perm_b32 v115, v97, v96, s97
v_perm_b32 v116, v99, v98, s97
v_perm_b32 v117, v101, v100, s97
s_waitcnt lgkmcnt(14)
ds_write_b128 v20, v[114:117] offset:17408
v_perm_b32 v118, v95, v94, s98
v_perm_b32 v119, v97, v96, s98
v_perm_b32 v120, v99, v98, s98
v_perm_b32 v121, v101, v100, s98
s_waitcnt lgkmcnt(14)
ds_write_b128 v20, v[118:121] offset:19456
v_mov_b32 v62, s88
v_mov_b32 v63, s88
v_mov_b32 v64, s88
v_mov_b32 v65, s88
v_mov_b32 v66, 0
v_mov_b32 v70, 0
v_mov_b32 v74, 0
v_mov_b32 v78, 0
v_mov_b32 v82, 0
v_mov_b32 v86, 0
v_mov_b32 v90, 0
v_mov_b32 v94, 0
v_smfmac_f32_16x16x64_bf16 v[66:69], v[62:65], a[0:7], v17
v_smfmac_f32_16x16x64_bf16 v[82:85], v[62:65], a[32:39], v17
v_smfmac_f32_16x16x64_bf16 v[70:73], v[62:65], a[8:15], v17
v_smfmac_f32_16x16x64_bf16 v[86:89], v[62:65], a[40:47], v17
v_smfmac_f32_16x16x64_bf16 v[74:77], v[62:65], a[16:23], v17
v_smfmac_f32_16x16x64_bf16 v[90:93], v[62:65], a[48:55], v17
v_smfmac_f32_16x16x64_bf16 v[78:81], v[62:65], a[24:31], v17
v_smfmac_f32_16x16x64_bf16 v[94:97], v[62:65], a[56:63], v17
v_smfmac_f32_16x16x64_bf16 v[66:69], v[62:65], a[64:71], v17
v_smfmac_f32_16x16x64_bf16 v[82:85], v[62:65], a[96:103], v17
v_smfmac_f32_16x16x64_bf16 v[70:73], v[62:65], a[72:79], v17
v_smfmac_f32_16x16x64_bf16 v[86:89], v[62:65], a[104:111], v17
v_smfmac_f32_16x16x64_bf16 v[74:77], v[62:65], a[80:87], v17
)ASM"
R"ASM(v_smfmac_f32_16x16x64_bf16 v[90:93], v[62:65], a[112:119], v17
v_smfmac_f32_16x16x64_bf16 v[78:81], v[62:65], a[88:95], v17
v_smfmac_f32_16x16x64_bf16 v[94:97], v[62:65], a[120:127], v17
v_smfmac_f32_16x16x32_bf16 v[66:69], v[62:63], a[128:131], v17
v_smfmac_f32_16x16x32_bf16 v[82:85], v[62:63], a[144:147], v17
v_smfmac_f32_16x16x32_bf16 v[70:73], v[62:63], a[132:135], v17
v_smfmac_f32_16x16x32_bf16 v[86:89], v[62:63], a[148:151], v17
v_smfmac_f32_16x16x32_bf16 v[74:77], v[62:63], a[136:139], v17
v_smfmac_f32_16x16x32_bf16 v[90:93], v[62:63], a[152:155], v17
v_smfmac_f32_16x16x32_bf16 v[78:81], v[62:63], a[140:143], v17
v_smfmac_f32_16x16x32_bf16 v[94:97], v[62:63], a[156:159], v17
s_nop 1
v_permlane16_swap_b32 v66, v82
v_add_f32 v110, v66, v82
v_permlane16_swap_b32 v70, v86
v_add_f32 v111, v70, v86
v_permlane16_swap_b32 v74, v90
v_add_f32 v112, v74, v90
v_permlane16_swap_b32 v78, v94
v_add_f32 v113, v78, v94
s_bfm_b64 exec, 32, 0
s_waitcnt lgkmcnt(14)
ds_write_b128 v8, v[110:113] offset:7680
s_mov_b64 exec, -1
v_mov_b32 v66, 0
v_mov_b32 v70, 0
v_mov_b32 v74, 0
v_mov_b32 v78, 0
v_smfmac_f32_16x16x64_bf16 v[66:69], v[62:65], a[160:167], v17
v_smfmac_f32_16x16x64_bf16 v[70:73], v[62:65], a[168:175], v17
v_smfmac_f32_16x16x64_bf16 v[74:77], v[62:65], a[176:183], v17
v_smfmac_f32_16x16x64_bf16 v[78:81], v[62:65], a[184:191], v17
v_smfmac_f32_16x16x64_bf16 v[66:69], v[62:65], a[192:199], v17
v_smfmac_f32_16x16x64_bf16 v[70:73], v[62:65], a[200:207], v17
v_smfmac_f32_16x16x64_bf16 v[74:77], v[62:65], a[208:215], v17
v_smfmac_f32_16x16x64_bf16 v[78:81], v[62:65], a[216:223], v17
s_nop 2
v_smfmac_f32_16x16x32_bf16 v[66:69], v[62:63], a[224:227], v17
v_smfmac_f32_16x16x32_bf16 v[70:73], v[62:63], a[228:231], v17
v_smfmac_f32_16x16x32_bf16 v[74:77], v[62:63], a[232:235], v17
v_smfmac_f32_16x16x32_bf16 v[78:81], v[62:63], a[236:239], v17
s_nop 6
v_permlane16_swap_b32 v66, v74
v_permlane16_swap_b32 v70, v78
v_add_f32 v110, v66, v74
v_add_f32 v111, v70, v78
s_bfm_b64 exec, 32, 0
s_waitcnt lgkmcnt(14)
ds_write_b64 v10, v[110:111] offset:11264
s_mov_b64 exec, -1
s_waitcnt vmcnt(16)
v_perm_b32 v192, v204, v196, s97
v_perm_b32 v193, v220, v212, s97
v_perm_b32 v194, v236, v228, s97
v_perm_b32 v195, v252, v244, s97
v_perm_b32 v200, v204, v196, s98
v_perm_b32 v201, v220, v212, s98
v_perm_b32 v202, v236, v228, s98
v_perm_b32 v203, v252, v244, s98
v_perm_b32 v208, v205, v197, s97
v_perm_b32 v209, v221, v213, s97
v_perm_b32 v210, v237, v229, s97
v_perm_b32 v211, v253, v245, s97
v_perm_b32 v216, v205, v197, s98
v_perm_b32 v217, v221, v213, s98
v_perm_b32 v218, v237, v229, s98
v_perm_b32 v219, v253, v245, s98
v_perm_b32 v224, v206, v198, s97
v_perm_b32 v225, v222, v214, s97
v_perm_b32 v226, v238, v230, s97
v_perm_b32 v227, v254, v246, s97
v_perm_b32 v232, v206, v198, s98
v_perm_b32 v233, v222, v214, s98
v_perm_b32 v234, v238, v230, s98
v_perm_b32 v235, v254, v246, s98
v_perm_b32 v240, v207, v199, s97
v_perm_b32 v241, v223, v215, s97
v_perm_b32 v242, v239, v231, s97
v_perm_b32 v243, v255, v247, s97
v_perm_b32 v248, v207, v199, s98
v_perm_b32 v249, v223, v215, s98
v_perm_b32 v250, v239, v231, s98
v_perm_b32 v251, v255, v247, s98
v_perm_b32 v114, v103, v102, s97
v_perm_b32 v115, v105, v104, s97
v_perm_b32 v116, v107, v106, s97
v_perm_b32 v117, v109, v108, s97
s_waitcnt lgkmcnt(14)
ds_write_b128 v20, v[114:117] offset:20480
v_perm_b32 v118, v103, v102, s98
v_perm_b32 v119, v105, v104, s98
v_perm_b32 v120, v107, v106, s98
v_perm_b32 v121, v109, v108, s98
s_waitcnt lgkmcnt(14)
ds_write_b128 v20, v[118:121] offset:22528
s_waitcnt vmcnt(0)
v_perm_b32 v196, v26, v22, s97
v_perm_b32 v197, v34, v30, s97
v_perm_b32 v198, v42, v38, s97
v_perm_b32 v199, v50, v46, s97
v_perm_b32 v204, v26, v22, s98
v_perm_b32 v205, v34, v30, s98
v_perm_b32 v206, v42, v38, s98
v_perm_b32 v207, v50, v46, s98
v_perm_b32 v212, v27, v23, s97
v_perm_b32 v213, v35, v31, s97
v_perm_b32 v214, v43, v39, s97
v_perm_b32 v215, v51, v47, s97
v_perm_b32 v220, v27, v23, s98
v_perm_b32 v221, v35, v31, s98
v_perm_b32 v222, v43, v39, s98
v_perm_b32 v223, v51, v47, s98
v_perm_b32 v228, v28, v24, s97
v_perm_b32 v229, v36, v32, s97
v_perm_b32 v230, v44, v40, s97
v_perm_b32 v231, v52, v48, s97
v_perm_b32 v236, v28, v24, s98
v_perm_b32 v237, v36, v32, s98
v_perm_b32 v238, v44, v40, s98
v_perm_b32 v239, v52, v48, s98
v_perm_b32 v244, v29, v25, s97
v_perm_b32 v245, v37, v33, s97
v_perm_b32 v246, v45, v41, s97
v_perm_b32 v247, v53, v49, s97
v_perm_b32 v252, v29, v25, s98
v_perm_b32 v253, v37, v33, s98
v_perm_b32 v254, v45, v41, s98
v_perm_b32 v255, v53, v49, s98
v_perm_b32 v114, v55, v54, s97
v_perm_b32 v115, v57, v56, s97
v_perm_b32 v116, v59, v58, s97
v_perm_b32 v117, v61, v60, s97
s_waitcnt lgkmcnt(14)
ds_write_b128 v20, v[114:117] offset:21504
v_perm_b32 v118, v55, v54, s98
v_perm_b32 v119, v57, v56, s98
v_perm_b32 v120, v59, v58, s98
v_perm_b32 v121, v61, v60, s98
s_waitcnt lgkmcnt(14)
ds_write_b128 v20, v[118:121] offset:23552
v_lshrrev_b32 v122, 4, v0
v_lshrrev_b32 v123, 8, v0
v_and_b32 v124, 15, v122
Cpslow_%=:
s_mov_b32 s89, 2109440
buffer_load_dwordx4 v[76:79], off, %[rw], s89 sc1
s_waitcnt vmcnt(0)
v_readfirstlane_b32 s90, v76
v_readfirstlane_b32 s91, v77
v_readfirstlane_b32 s92, v78
v_readfirstlane_b32 s93, v79
s_mov_b32 s69, 0
s_cmp_eq_u64 s[90:91], s[60:61]
s_cbranch_scc1 Cpok_%=
s_mov_b32 s69, 1
s_cmp_eq_u64 s[92:93], s[60:61]
s_cbranch_scc1 Cpok_%=
s_sleep 4
s_sub_u32 s62, s62, 1
s_cmp_gt_i32 s62, 0
s_cbranch_scc1 Cpslow_%=
Cpok_%=:
s_lshr_b32 s63, %[j], 4
s_xor_b32 s68, s69, s63
s_lshl_b32 s68, s68, 13
s_and_b32 s63, %[j], 15
s_lshl_b32 s63, s63, 14
s_add_u32 s63, s63, s68
s_add_u32 s78, s63, 267264
s_add_u32 s79, s63, 4480
s_add_u32 s76, s63, 5376
s_add_u32 s75, s63, 1053696
s_and_b32 s63, %[j], 12
s_add_u32 s63, s63, %[w]
s_lshl_b32 s63, s63, 14
s_add_u32 s70, s63, s68
s_add_u32 s77, s70, 5760
s_add_u32 s72, s70, 1048576
s_lshr_b32 s63, %[w], 1
s_xor_b32 s63, s63, s69
s_lshl_b32 s63, s63, 13
s_and_b32 s64, %[w], 1
s_lshl_b32 s64, s64, 3
s_and_b32 s90, %[j], 3
s_add_u32 s64, s64, s90
s_lshl_b32 s64, s64, 14
s_add_u32 s63, s63, s64
s_lshr_b32 s64, %[j], 2
s_mul_i32 s64, s64, 640
s_add_u32 s63, s63, s64
s_add_u32 s71, s63, 1048576
s_lshl_b32 s63, %[j], 7
s_mov_b32 s64, 1053696
s_cmp_lt_u32 %[j], 24
s_cselect_b32 s64, s64, 2304
s_add_u32 s73, s63, s64
s_mul_i32 s90, %[w], 5
s_add_u32 s63, s90, 4
s_lshr_b32 s64, s63, 4
s_xor_b32 s64, s64, s69
s_lshl_b32 s64, s64, 13
s_and_b32 s63, s63, 15
s_lshl_b32 s63, s63, 14
s_add_u32 s63, s63, s64
s_add_u32 s74, s73, s63
s_mul_i32 s63, %[w], 640
s_sub_u32 s74, s74, s63
v_lshrrev_b32 v36, 2, v124
v_add_u32 v36, s90, v36
v_lshrrev_b32 v37, 4, v36
v_xor_b32 v37, s69, v37
v_and_b32 v36, 15, v36
v_lshl_add_u32 v36, v36, 1, v37
v_and_b32 v37, 3, v124
v_lshlrev_b32 v37, 5, v37
v_lshl_add_u32 v37, v123, 4, v37
v_lshl_add_u32 v4, v36, 13, v37
v_lshrrev_b32 v36, 5, v122
v_xor_b32 v36, s69, v36
v_bfe_u32 v37, v122, 3, 2
v_lshl_add_u32 v37, v37, 2, s86
v_lshl_add_u32 v36, v37, 1, v36
s_lshl_b32 s63, s87, 7
s_add_u32 s63, s63, 5760
v_and_b32 v37, 56, v12
v_add_u32 v37, s63, v37
v_lshl_add_u32 v6, v36, 13, v37
v_mov_b32 v110, -1
v_mov_b32 v111, -1
v_mov_b32 v112, -1
v_mov_b32 v113, -1
s_add_u32 s89, s71, 0
s_add_u32 s63, s71, 65536
s_bfm_b64 exec, 40, 0
buffer_store_dwordx4 v[110:113], v0, %[rw], s89 offen sc1
buffer_store_dwordx4 v[110:113], v0, %[rw], s63 offen sc1
s_add_u32 s89, s71, 262144
s_add_u32 s63, s71, 327680
s_bfm_b64 exec, 40, 0
buffer_store_dwordx4 v[110:113], v0, %[rw], s89 offen sc1
buffer_store_dwordx4 v[110:113], v0, %[rw], s63 offen sc1
s_add_u32 s89, s71, 524288
s_add_u32 s63, s71, 589824
s_bfm_b64 exec, 40, 0
buffer_store_dwordx4 v[110:113], v0, %[rw], s89 offen sc1
buffer_store_dwordx4 v[110:113], v0, %[rw], s63 offen sc1
s_add_u32 s89, s71, 786432
s_add_u32 s63, s71, 851968
s_bfm_b64 exec, 40, 0
buffer_store_dwordx4 v[110:113], v0, %[rw], s89 offen sc1
buffer_store_dwordx4 v[110:113], v0, %[rw], s63 offen sc1
s_bfm_b64 exec, 32, 0
s_cmp_gt_u32 %[j], 26
s_cbranch_scc1 Cnozc_%=
s_add_u32 s89, s73, 0
s_add_u32 s63, s74, 0
buffer_store_dwordx4 v[110:113], v4, %[rw], s89 offen sc1
buffer_store_dword v110, v9, %[rw], s63 offen sc1
s_add_u32 s89, s73, 262144
s_add_u32 s63, s74, 262144
buffer_store_dwordx4 v[110:113], v4, %[rw], s89 offen sc1
buffer_store_dword v110, v9, %[rw], s63 offen sc1
s_add_u32 s89, s73, 524288
s_add_u32 s63, s74, 524288
buffer_store_dwordx4 v[110:113], v4, %[rw], s89 offen sc1
buffer_store_dword v110, v9, %[rw], s63 offen sc1
s_add_u32 s89, s73, 786432
s_add_u32 s63, s74, 786432
buffer_store_dwordx4 v[110:113], v4, %[rw], s89 offen sc1
buffer_store_dword v110, v9, %[rw], s63 offen sc1
Cnozc_%=:
s_mov_b64 exec, -1
s_cmp_lg_u32 %[w], s86
s_cbranch_scc1 Cnoxc_%=
buffer_store_dwordx2 v[110:111], v6, %[rw], 0 offen sc1
s_mov_b32 s89, 524288
buffer_store_dwordx2 v[110:111], v6, %[rw], s89 offen sc1
Cnoxc_%=:
s_cmp_lg_u32 %[w], 3
s_cbranch_scc1 Cnoqc_%=
s_add_u32 s89, s78, 0
s_add_u32 s63, s79, 262144
s_bfm_b64 exec, 16, 0
buffer_store_dwordx4 v[110:113], v0, %[rw], s89 offen sc1
s_bfm_b64 exec, 33, 0
buffer_store_dwordx4 v[110:113], v0, %[rw], s63 offen sc1
s_add_u32 s89, s78, 524288
s_add_u32 s63, s79, 786432
s_bfm_b64 exec, 16, 0
buffer_store_dwordx4 v[110:113], v0, %[rw], s89 offen sc1
s_bfm_b64 exec, 33, 0
buffer_store_dwordx4 v[110:113], v0, %[rw], s63 offen sc1
s_mov_b64 exec, -1
Cnoqc_%=:
s_cmp_lt_u32 %[w], 2
s_cbranch_scc1 Cnolo_%=
s_and_b32 s63, %[w], 1
s_xor_b32 s63, s63, 1
s_mul_i32 s63, s63, 524288
s_add_u32 s83, s79, s63
v_mov_b32 v70, v13
s_call_b64 s[80:81], Clo_%=
Cnolo_%=:
s_nop 4
s_waitcnt vmcnt(0)
v_mov_b32 v32, s60
v_mov_b32 v33, s61
s_lshl_b32 s89, %[j], 2
s_add_u32 s89, s89, %[w]
s_lshl_b32 s89, s89, 3
s_add_u32 s89, s89, 2105856
s_mov_b64 exec, 0x1
buffer_store_dwordx2 v[32:33], off, %[rw], s89 sc1
s_mov_b64 exec, -1
s_lshl_b32 s63, %[w], 9
v_add_u32 v26, s63, v12
s_lshr_b32 s63, %[j], 2
s_mul_i32 s63, s63, 896
v_add_u32 v27, s63, v26
v_subrev_u32 v28, 0x380, v26
v_and_b32 v28, 0x3f8, v28
v_add_u32 v28, 0x2200, v28
v_mov_b32 v29, 0x380
v_cmp_lt_u32 vcc, v26, v29
s_nop 1
v_cndmask_b32 v26, v28, v27, vcc
s_mov_b32 s89, 2097152
Cfl_%=:
buffer_load_dwordx2 v[28:29], v26, %[rw], s89 offen sc1
s_waitcnt vmcnt(0)
v_cmp_ne_u64 vcc, s[60:61], v[28:29]
s_cbranch_vccz Cflx_%=
s_sleep 8
s_sub_u32 s62, s62, 1
s_cmp_gt_i32 s62, 0
s_cbranch_scc1 Cfl_%=
Cflx_%=:
v_add_u32 v26, 0x1e00, v12
s_waitcnt lgkmcnt(0)
s_barrier
ds_read2_b64 v[28:31], v26 offset1:80
ds_read2_b64 v[32:35], v26 offset0:160 offset1:240
ds_read2st64_b32 v[36:37], v11 offset0:44 offset1:45
ds_read2st64_b32 v[38:39], v11 offset0:46 offset1:47
s_waitcnt lgkmcnt(0)
v_pk_add_f32 v[28:29], v[28:29], v[30:31]
v_pk_add_f32 v[32:33], v[32:33], v[34:35]
v_pk_add_f32 v[14:15], v[28:29], v[32:33]
v_pk_add_f32 v[36:37], v[36:37], v[38:39]
v_add_f32 v13, v36, v37
s_mov_b32 s66, 0
s_mov_b32 s67, 0
Cjob_%=:
s_nop 4
s_add_u32 s89, s70, s66
s_add_u32 s90, s71, s66
s_add_u32 s91, s90, 65536
ds_read_b128 v[96:99], v20 offset:16384
ds_read_b128 v[100:103], v20 offset:17408
ds_read_b128 v[104:107], v20 offset:18432
ds_read_b128 v[108:111], v20 offset:19456
ds_read_b128 v[112:115], v20 offset:20480
ds_read_b128 v[116:119], v20 offset:21504
ds_read_b128 v[120:123], v20 offset:22528
ds_read_b128 v[124:127], v20 offset:23552
v_mov_b32 v56, 0
v_mov_b32 v60, 0
v_mov_b32 v64, 0
v_mov_b32 v68, 0
v_mov_b32 v72, 0
v_mov_b32 v76, 0
v_mov_b32 v80, 0
v_mov_b32 v84, 0
v_mov_b32 v88, 0
v_mov_b32 v92, 0
s_cmp_eq_u32 s67, 3
s_cbranch_scc1 Cpgx_%=
s_mov_b64 exec, 0x7f
Cpg_%=:
buffer_load_dword v38, v2, %[rw], s89 offen sc1
s_waitcnt vmcnt(0)
v_cmp_ne_u32 vcc, -1, v38
s_cbranch_vccnz Cpgy_%=
s_sleep 8
s_sub_u32 s62, s62, 1
s_cmp_gt_i32 s62, 0
s_cbranch_scc1 Cpg_%=
Cpgy_%=:
s_mov_b64 exec, -1
Cpgx_%=:
Cpa_%=:
buffer_load_dwordx2 v[38:39], v2, %[rw], s89 offen sc1
buffer_load_dwordx4 v[22:25], v1, %[rw], s89 offen sc1
buffer_load_dwordx4 v[26:29], v1, %[rw], s89 offen offset:1280 sc1
buffer_load_dwordx4 v[30:33], v1, %[rw], s89 offen offset:2560 sc1
buffer_load_dwordx4 v[34:37], v1, %[rw], s89 offen offset:3840 sc1
s_waitcnt vmcnt(0)
v_max_f32_dpp v40, v38, v38 row_ror:4 row_mask:0xf bank_mask:0xf
v_max3_u32 v43, v22, v23, v24
v_max3_u32 v43, v43, v25, v26
)ASM"
R"ASM(v_max_f32_dpp v40, v40, v40 row_ror:2 row_mask:0xf bank_mask:0xf
v_max3_u32 v43, v43, v27, v28
v_max3_u32 v43, v43, v29, v30
v_max_f32_dpp v40, v40, v40 row_ror:1 row_mask:0xf bank_mask:0xf
v_sub_f32 v41, v38, v40
v_exp_f32 v41, v41
v_max3_u32 v43, v43, v31, v32
v_mul_f32 v42, v41, v39
v_max3_u32 v43, v43, v33, v34
v_max3_u32 v43, v43, v35, v36
v_max3_u32 v43, v43, v37, v38
v_max_u32 v43, v43, v39
v_cmp_eq_u32 vcc, -1, v43
s_cbranch_vccz Cpax_%=
s_sub_u32 s62, s62, 1
s_cmp_gt_i32 s62, 0
s_cbranch_scc1 Cpa_%=
Cpax_%=:
v_mul_f32_dpp v52, v41, v22 row_newbcast:0 row_mask:0xf bank_mask:0xf
v_mul_f32_dpp v54, v41, v24 row_newbcast:0 row_mask:0xf bank_mask:0xf
v_mul_f32_dpp v53, v41, v23 row_newbcast:0 row_mask:0xf bank_mask:0xf
v_mul_f32_dpp v55, v41, v25 row_newbcast:0 row_mask:0xf bank_mask:0xf
v_add_f32_dpp v42, v42, v42 row_ror:4 row_mask:0xf bank_mask:0xf
v_fmac_f32_dpp v52, v41, v26 row_newbcast:2 row_mask:0xf bank_mask:0xf
v_fmac_f32_dpp v54, v41, v28 row_newbcast:2 row_mask:0xf bank_mask:0xf
v_fmac_f32_dpp v53, v41, v27 row_newbcast:2 row_mask:0xf bank_mask:0xf
v_fmac_f32_dpp v55, v41, v29 row_newbcast:2 row_mask:0xf bank_mask:0xf
v_add_f32_dpp v42, v42, v42 row_ror:2 row_mask:0xf bank_mask:0xf
v_fmac_f32_dpp v52, v41, v30 row_newbcast:4 row_mask:0xf bank_mask:0xf
v_fmac_f32_dpp v54, v41, v32 row_newbcast:4 row_mask:0xf bank_mask:0xf
v_fmac_f32_dpp v53, v41, v31 row_newbcast:4 row_mask:0xf bank_mask:0xf
v_fmac_f32_dpp v55, v41, v33 row_newbcast:4 row_mask:0xf bank_mask:0xf
v_add_f32_dpp v42, v42, v42 row_ror:1 row_mask:0xf bank_mask:0xf
v_fmac_f32_dpp v52, v41, v34 row_newbcast:6 row_mask:0xf bank_mask:0xf
v_fmac_f32_dpp v54, v41, v36 row_newbcast:6 row_mask:0xf bank_mask:0xf
v_fmac_f32_dpp v53, v41, v35 row_newbcast:6 row_mask:0xf bank_mask:0xf
v_fmac_f32_dpp v55, v41, v37 row_newbcast:6 row_mask:0xf bank_mask:0xf
v_rcp_f32 v42, v42
v_permlane32_swap_b32 v52, v54
v_permlane32_swap_b32 v53, v55
v_add_f32 v52, v52, v54
v_add_f32 v53, v53, v55
v_mul_f32 v52, v52, v42
v_mul_f32 v53, v53, v42
v_cvt_pk_bf16_f32 v44, v52, v53
s_nop 1
v_mov_b32_dpp v45, v44 row_shl:1 row_mask:0xf bank_mask:0xf bound_ctrl:1
v_mov_b32_dpp v46, v44 row_shl:2 row_mask:0xf bank_mask:0xf bound_ctrl:1
v_mov_b32_dpp v47, v44 row_shl:3 row_mask:0xf bank_mask:0xf bound_ctrl:1
s_waitcnt lgkmcnt(0)
s_nop 0
v_smfmac_f32_16x16x64_bf16 v[56:59], v[44:47], v[128:135], v17
v_mov_b32_dpp v48, v44 row_shl:8 row_mask:0xf bank_mask:0xf bound_ctrl:1
v_smfmac_f32_16x16x64_bf16 v[72:75], v[44:47], v[160:167], v17
v_mov_b32_dpp v49, v44 row_shl:9 row_mask:0xf bank_mask:0xf bound_ctrl:1
v_smfmac_f32_16x16x64_bf16 v[60:63], v[44:47], v[136:143], v17
v_mov_b32_dpp v50, v44 row_shl:10 row_mask:0xf bank_mask:0xf bound_ctrl:1
v_smfmac_f32_16x16x64_bf16 v[76:79], v[44:47], v[168:175], v17
v_mov_b32_dpp v51, v44 row_shl:11 row_mask:0xf bank_mask:0xf bound_ctrl:1
v_smfmac_f32_16x16x64_bf16 v[64:67], v[44:47], v[144:151], v17
v_smfmac_f32_16x16x64_bf16 v[80:83], v[44:47], v[176:183], v17
v_smfmac_f32_16x16x64_bf16 v[68:71], v[44:47], v[152:159], v17
v_smfmac_f32_16x16x64_bf16 v[84:87], v[44:47], v[184:191], v17
v_smfmac_f32_16x16x64_bf16 v[88:91], v[44:47], v[96:103], v17
v_smfmac_f32_16x16x64_bf16 v[92:95], v[44:47], v[104:111], v17
v_smfmac_f32_16x16x64_bf16 v[56:59], v[48:51], v[192:199], v17
v_smfmac_f32_16x16x64_bf16 v[72:75], v[48:51], v[224:231], v17
v_smfmac_f32_16x16x64_bf16 v[60:63], v[48:51], v[200:207], v17
v_smfmac_f32_16x16x64_bf16 v[76:79], v[48:51], v[232:239], v17
v_smfmac_f32_16x16x64_bf16 v[64:67], v[48:51], v[208:215], v17
v_smfmac_f32_16x16x64_bf16 v[80:83], v[48:51], v[240:247], v17
v_smfmac_f32_16x16x64_bf16 v[68:71], v[48:51], v[216:223], v17
v_smfmac_f32_16x16x64_bf16 v[84:87], v[48:51], v[248:255], v17
v_smfmac_f32_16x16x64_bf16 v[88:91], v[48:51], v[112:119], v17
v_smfmac_f32_16x16x64_bf16 v[92:95], v[48:51], v[120:127], v17
v_permlane16_swap_b32 v56, v72
v_add_f32 v52, v56, v72
v_permlane16_swap_b32 v60, v76
v_add_f32 v53, v60, v76
v_permlane16_swap_b32 v64, v80
v_add_f32 v54, v64, v80
v_permlane16_swap_b32 v68, v84
v_add_f32 v55, v68, v84
s_bfm_b64 exec, 32, 0
ds_write_b128 v8, v[52:55] offset:0
v_permlane16_swap_b32 v88, v92
v_add_f32 v40, v88, v92
ds_write_b32 v9, v40 offset:512
s_bfm_b64 exec, 40, 0
s_waitcnt lgkmcnt(0)
s_barrier
ds_read_b128 v[56:59], v0 offset:0
ds_read_b128 v[60:63], v0 offset:640
ds_read_b128 v[64:67], v0 offset:1280
ds_read_b128 v[68:71], v0 offset:1920
s_waitcnt lgkmcnt(2)
v_pk_add_f32 v[56:57], v[56:57], v[60:61]
v_pk_add_f32 v[58:59], v[58:59], v[62:63]
s_waitcnt lgkmcnt(0)
v_pk_add_f32 v[64:65], v[64:65], v[68:69]
v_pk_add_f32 v[66:67], v[66:67], v[70:71]
v_pk_add_f32 v[56:57], v[56:57], v[64:65]
v_pk_add_f32 v[58:59], v[58:59], v[66:67]
v_min_u32 v44, -2, v56
v_min_u32 v45, -2, v57
v_min_u32 v46, -2, v58
v_min_u32 v47, -2, v59
buffer_store_dwordx4 v[44:47], v0, %[rw], s90 offen sc1
buffer_store_dwordx4 v[44:47], v0, %[rw], s91 offen sc1
s_mov_b64 exec, -1
s_cmp_lt_u32 %[j], 27
s_cbranch_scc1 Cb_%=
s_barrier
s_branch Cbx_%=
Cb_%=:
s_add_u32 s89, s72, s66
s_add_u32 s90, s73, s66
s_add_u32 s91, s74, s66
v_mov_b32 v42, 0
v_mov_b32 v43, 0
v_mov_b32 v44, 0
v_mov_b32 v45, 0
v_mov_b32 v46, 0
ds_read_u16_d16_hi v42, v16 offset:0
ds_read_u16_d16_hi v43, v16 offset:64
ds_read_u16_d16_hi v44, v16 offset:128
ds_read_u16_d16_hi v45, v16 offset:192
ds_read_u16_d16_hi v46, v16 offset:256
ds_read_b128 v[80:83], v20 offset:0
ds_read_b128 v[84:87], v20 offset:1024
ds_read_b128 v[88:91], v20 offset:8192
ds_read_b128 v[92:95], v20 offset:9216
ds_read_b128 v[96:99], v20 offset:2048
ds_read_b128 v[100:103], v20 offset:3072
ds_read_b128 v[104:107], v20 offset:10240
ds_read_b128 v[108:111], v20 offset:11264
ds_read_b128 v[112:115], v20 offset:4096
ds_read_b128 v[116:119], v20 offset:5120
s_waitcnt lgkmcnt(14)
ds_read_b128 v[120:123], v20 offset:12288
s_waitcnt lgkmcnt(14)
ds_read_b128 v[124:127], v20 offset:13312
v_mov_b32 v58, 0
v_mov_b32 v62, 0
v_mov_b32 v66, 0
v_mov_b32 v70, 0
v_add_u32 v16, 0x500, v16
Cpb_%=:
buffer_load_dwordx4 v[22:25], v3, %[rw], s89 offen sc1
buffer_load_dwordx4 v[26:29], v3, %[rw], s89 offen offset:128 sc1
buffer_load_dwordx4 v[30:33], v3, %[rw], s89 offen offset:256 sc1
buffer_load_dwordx4 v[34:37], v3, %[rw], s89 offen offset:384 sc1
buffer_load_dwordx4 v[38:41], v3, %[rw], s89 offen offset:512 sc1
s_waitcnt vmcnt(0)
v_max3_u32 v47, v22, v23, v24
v_max3_u32 v47, v47, v25, v26
v_max3_u32 v47, v47, v27, v28
v_max3_u32 v47, v47, v29, v30
v_max3_u32 v47, v47, v31, v32
v_max3_u32 v47, v47, v33, v34
v_max3_u32 v47, v47, v35, v36
v_max3_u32 v47, v47, v37, v38
v_max3_u32 v47, v47, v39, v40
v_max_u32 v47, v47, v41
v_cmp_eq_u32 vcc, -1, v47
s_cbranch_vccz Cpbx_%=
s_sub_u32 s62, s62, 1
s_cmp_gt_i32 s62, 0
s_cbranch_scc1 Cpb_%=
Cpbx_%=:
v_permlane32_swap_b32 v22, v24
v_permlane32_swap_b32 v23, v25
v_permlane32_swap_b32 v26, v28
v_permlane32_swap_b32 v27, v29
v_permlane32_swap_b32 v30, v32
v_permlane32_swap_b32 v31, v33
v_permlane32_swap_b32 v34, v36
v_permlane32_swap_b32 v35, v37
v_permlane32_swap_b32 v38, v40
v_permlane32_swap_b32 v39, v41
v_add_f32 v22, v22, v24
v_add_f32 v23, v23, v25
v_add_f32 v26, v26, v28
v_add_f32 v27, v27, v29
v_add_f32 v30, v30, v32
v_add_f32 v31, v31, v33
v_add_f32 v34, v34, v36
v_add_f32 v35, v35, v37
v_add_f32 v38, v38, v40
v_add_f32 v39, v39, v41
v_permlane16_swap_b32 v22, v23
v_permlane16_swap_b32 v26, v27
v_permlane16_swap_b32 v30, v31
v_permlane16_swap_b32 v34, v35
v_permlane16_swap_b32 v38, v39
v_add_f32 v22, v22, v23
v_add_f32 v26, v26, v27
v_add_f32 v30, v30, v31
v_add_f32 v34, v34, v35
v_add_f32 v38, v38, v39
v_add_f32_dpp v22, v22, v22 row_ror:8 row_mask:0xf bank_mask:0xf
v_add_f32_dpp v26, v26, v26 row_ror:8 row_mask:0xf bank_mask:0xf
v_add_f32_dpp v30, v30, v30 row_ror:8 row_mask:0xf bank_mask:0xf
v_add_f32_dpp v34, v34, v34 row_ror:8 row_mask:0xf bank_mask:0xf
v_add_f32_dpp v38, v38, v38 row_ror:8 row_mask:0xf bank_mask:0xf
v_cvt_pk_bf16_f32 v22, 0, v22
v_cvt_pk_bf16_f32 v26, 0, v26
v_cvt_pk_bf16_f32 v30, 0, v30
v_cvt_pk_bf16_f32 v34, 0, v34
v_cvt_pk_bf16_f32 v38, 0, v38
s_waitcnt lgkmcnt(12)
v_add_f32 v22, v22, v42
v_add_f32 v26, v26, v43
v_add_f32 v30, v30, v44
v_add_f32 v34, v34, v45
v_add_f32 v38, v38, v46
v_mov_b32_dpp v23, v22 row_shl:1 row_mask:0xf bank_mask:0xf bound_ctrl:1
v_mov_b32_dpp v27, v26 row_shl:1 row_mask:0xf bank_mask:0xf bound_ctrl:1
v_mov_b32_dpp v31, v30 row_shl:1 row_mask:0xf bank_mask:0xf bound_ctrl:1
v_mov_b32_dpp v35, v34 row_shl:1 row_mask:0xf bank_mask:0xf bound_ctrl:1
v_mov_b32_dpp v39, v38 row_shl:1 row_mask:0xf bank_mask:0xf bound_ctrl:1
v_cvt_pk_bf16_f32 v48, v22, v23
v_cvt_pk_bf16_f32 v50, v26, v27
v_cvt_pk_bf16_f32 v52, v30, v31
v_cvt_pk_bf16_f32 v54, v34, v35
v_cvt_pk_bf16_f32 v56, v38, v39
v_mov_b32_dpp v49, v48 row_shl:2 row_mask:0xf bank_mask:0xf bound_ctrl:1
v_mov_b32_dpp v51, v50 row_shl:2 row_mask:0xf bank_mask:0xf bound_ctrl:1
s_nop 1
v_smfmac_f32_16x16x64_bf16 v[58:61], v[48:51], a[160:167], v17
v_mov_b32_dpp v53, v52 row_shl:2 row_mask:0xf bank_mask:0xf bound_ctrl:1
v_mov_b32_dpp v55, v54 row_shl:2 row_mask:0xf bank_mask:0xf bound_ctrl:1
v_smfmac_f32_16x16x64_bf16 v[66:69], v[48:51], a[176:183], v17
v_mov_b32_dpp v57, v56 row_shl:2 row_mask:0xf bank_mask:0xf bound_ctrl:1
v_lshlrev_b32 v42, 16, v48
v_smfmac_f32_16x16x64_bf16 v[62:65], v[48:51], a[168:175], v17
v_lshlrev_b32 v43, 16, v50
v_lshlrev_b32 v44, 16, v52
v_smfmac_f32_16x16x64_bf16 v[70:73], v[48:51], a[184:191], v17
v_lshlrev_b32 v45, 16, v54
v_lshlrev_b32 v46, 16, v56
ds_read_b128 v[22:25], v20 offset:6144
ds_read_b128 v[26:29], v20 offset:7168
ds_read_b128 v[30:33], v20 offset:14336
s_waitcnt lgkmcnt(14)
ds_read_b128 v[34:37], v20 offset:15360
v_smfmac_f32_16x16x64_bf16 v[58:61], v[52:55], a[192:199], v17
v_add_f32 v74, v42, v43
v_mul_f32 v75, v42, v42
v_add_f32 v74, v74, v44
v_smfmac_f32_16x16x64_bf16 v[66:69], v[52:55], a[208:215], v17
v_fma_f32 v75, v43, v43, v75
v_add_f32 v74, v74, v45
v_fma_f32 v75, v44, v44, v75
v_smfmac_f32_16x16x64_bf16 v[62:65], v[52:55], a[200:207], v17
v_add_f32 v74, v74, v46
v_fma_f32 v75, v45, v45, v75
v_fma_f32 v75, v46, v46, v75
v_smfmac_f32_16x16x64_bf16 v[70:73], v[52:55], a[216:223], v17
v_smfmac_f32_16x16x32_bf16 v[58:61], v[56:57], a[224:227], v17
v_permlane32_swap_b32 v74, v75
v_add_f32 v74, v74, v75
v_mov_b32 v47, v74
v_smfmac_f32_16x16x32_bf16 v[66:69], v[56:57], a[232:235], v17
v_smfmac_f32_16x16x32_bf16 v[62:65], v[56:57], a[228:231], v17
v_permlane16_swap_b32 v74, v47
v_add_f32 v74, v74, v47
v_smfmac_f32_16x16x32_bf16 v[70:73], v[56:57], a[236:239], v17
s_nop 0
v_add_f32_dpp v74, v74, v74 quad_perm:[1,0,3,2] row_mask:0xf bank_mask:0xf
s_nop 1
v_add_f32_dpp v74, v74, v74 quad_perm:[2,3,0,1] row_mask:0xf bank_mask:0xf
s_nop 1
v_add_f32_dpp v74, v74, v74 row_half_mirror row_mask:0xf bank_mask:0xf
s_mov_b32 exec_lo, 0x1
s_mov_b32 exec_hi, 0x1
s_waitcnt lgkmcnt(14)
ds_write_b32 v19, v74 offset:12288
s_mov_b64 exec, -1
v_permlane16_swap_b32 v58, v66
v_permlane16_swap_b32 v62, v70
v_add_f32 v76, v58, v66
v_add_f32 v77, v62, v70
s_bfm_b64 exec, 32, 0
s_waitcnt lgkmcnt(14)
ds_write_b64 v10, v[76:77] offset:10240
s_mov_b64 exec, -1
v_mov_b32 v47, 0
v_mov_b32 v58, 0
v_mov_b32 v62, 0
v_mov_b32 v66, 0
v_mov_b32 v70, 0
v_mov_b32 v79, v42
s_cmp_eq_u32 s87, 1
s_cselect_b64 vcc, -1, 0
v_cndmask_b32 v79, v79, v43, vcc
s_cmp_eq_u32 s87, 2
s_cselect_b64 vcc, -1, 0
v_cndmask_b32 v79, v79, v44, vcc
s_cmp_eq_u32 s87, 3
s_cselect_b64 vcc, -1, 0
v_cndmask_b32 v79, v79, v45, vcc
s_cmp_eq_u32 s87, 4
s_cselect_b64 vcc, -1, 0
v_cndmask_b32 v79, v79, v46, vcc
s_waitcnt lgkmcnt(0)
s_barrier
ds_read_b128 v[38:41], v47 offset:12288
ds_read_b128 v[42:45], v47 offset:12304
ds_read2st64_b32 v[48:49], v11 offset0:40 offset1:41
ds_read2st64_b32 v[50:51], v11 offset0:42 offset1:43
s_waitcnt lgkmcnt(2)
v_pk_add_f32 v[38:39], v[38:39], v[40:41]
v_pk_add_f32 v[42:43], v[42:43], v[44:45]
v_pk_add_f32 v[38:39], v[38:39], v[42:43]
v_mul_f32 v38, 0x3acccccd, v38
v_mul_f32 v39, 0x3acccccd, v39
v_fma_f32 v39, -v38, v38, v39
v_cvt_pk_bf16_f32 v38, 0, v38
v_max_f32 v39, 0, v39
v_cvt_pk_bf16_f32 v39, 0, v39
v_add_f32 v39, 0x3727c5ac, v39
v_cvt_pk_bf16_f32 v39, 0, v39
v_sqrt_f32 v39, v39
s_waitcnt lgkmcnt(0)
v_pk_add_f32 v[48:49], v[48:49], v[50:51]
v_cvt_pk_bf16_f32 v39, 0, v39
v_rcp_f32 v39, v39
v_add_f32 v48, v48, v49
v_fma_f32 v48, -v38, v13, v48
v_mul_f32 v48, v48, v39
v_cvt_pk_bf16_f32 v48, 0, v48
v_mul_f32 v49, 0xbfb8aa3b, v48
v_exp_f32 v49, v49
s_nop 0
v_add_f32 v49, 1.0, v49
v_rcp_f32 v49, v49
s_nop 0
v_mul_f32 v48, v48, v49
s_nop 1
v_mov_b32_dpp v49, v48 row_shl:1 row_mask:0xf bank_mask:0xf bound_ctrl:1
v_cvt_pk_bf16_f32 v52, v48, v49
s_nop 1
v_mov_b32_dpp v53, v52 row_shl:2 row_mask:0xf bank_mask:0xf bound_ctrl:1
v_mov_b32_dpp v54, v52 row_shl:8 row_mask:0xf bank_mask:0xf bound_ctrl:1
v_mov_b32_dpp v55, v52 row_shl:10 row_mask:0xf bank_mask:0xf bound_ctrl:1
s_nop 1
v_smfmac_f32_16x16x64_bf16 v[58:61], v[52:55], v[80:87], v17
v_smfmac_f32_16x16x64_bf16 v[62:65], v[52:55], v[88:95], v17
v_mov_b32 v80, 0
v_mov_b32 v84, 0
v_smfmac_f32_16x16x64_bf16 v[66:69], v[52:55], v[96:103], v17
v_mov_b32 v88, 0
v_mov_b32 v92, 0
v_smfmac_f32_16x16x64_bf16 v[70:73], v[52:55], v[104:111], v17
v_mov_b32 v96, 0
v_mov_b32 v100, 0
v_smfmac_f32_16x16x64_bf16 v[80:83], v[52:55], v[112:119], v17
v_permlane16_swap_b32 v58, v62
v_add_f32 v38, v58, v62
v_smfmac_f32_16x16x64_bf16 v[84:87], v[52:55], v[120:127], v17
v_min_u32 v38, -2, v38
v_smfmac_f32_16x16x64_bf16 v[88:91], v[52:55], v[22:29], v17
v_permlane16_swap_b32 v66, v70
v_add_f32 v39, v66, v70
v_smfmac_f32_16x16x64_bf16 v[92:95], v[52:55], v[30:37], v17
v_min_u32 v39, -2, v39
v_smfmac_f32_16x16x64_bf16 v[96:99], v[52:55], a[240:247], v17
v_smfmac_f32_16x16x64_bf16 v[100:103], v[52:55], a[248:255], v17
v_permlane16_swap_b32 v80, v84
v_add_f32 v40, v80, v84
v_min_u32 v40, -2, v40
s_nop 1
v_permlane16_swap_b32 v88, v92
v_add_f32 v41, v88, v92
v_min_u32 v41, -2, v41
s_bfm_b64 exec, 32, 0
buffer_store_dwordx4 v[38:41], v4, %[rw], s90 offen sc1
v_permlane16_swap_b32 v96, v100
v_add_f32 v42, v96, v100
v_min_u32 v42, -2, v42
buffer_store_dword v42, v9, %[rw], s91 offen sc1
s_mov_b64 exec, -1
Cbx_%=:
s_waitcnt lgkmcnt(0)
s_cmp_lg_u32 %[w], s86
s_cbranch_scc1 Ccx_%=
s_add_u32 s89, s75, s66
s_add_u32 s63, s76, s66
s_lshr_b32 s90, s67, 1
s_mul_i32 s90, s90, 1280
s_lshl_b32 s91, %[j], 6
s_add_u32 s90, s90, s91
Cpc_%=:
buffer_load_dwordx4 v[22:25], v0, %[rw], s89 offen sc1
buffer_load_dwordx4 v[26:29], v0, %[rw], s89 offen offset:1024 sc1
buffer_load_dwordx4 v[30:33], v0, %[rw], s89 offen offset:2048 sc1
buffer_load_dwordx4 v[34:37], v5, %[rw], s63 offen sc1
s_waitcnt vmcnt(0)
v_max3_u32 v46, v22, v23, v24
)ASM"
R"ASM(v_max3_u32 v46, v46, v25, v26
v_max3_u32 v46, v46, v27, v28
v_max3_u32 v46, v46, v29, v30
v_max3_u32 v46, v46, v31, v32
v_max3_u32 v46, v46, v33, v34
v_max3_u32 v46, v46, v35, v36
v_max_u32 v46, v46, v37
v_cmp_eq_u32 vcc, -1, v46
s_cbranch_vccz Cpcx_%=
s_sub_u32 s62, s62, 1
s_cmp_gt_i32 s62, 0
s_cbranch_scc1 Cpc_%=
Cpcx_%=:
v_pk_add_f32 v[38:39], v[22:23], v[26:27]
v_pk_add_f32 v[40:41], v[24:25], v[28:29]
v_pk_add_f32 v[42:43], v[30:31], v[34:35]
v_pk_add_f32 v[44:45], v[32:33], v[36:37]
v_pk_add_f32 v[38:39], v[38:39], v[42:43]
v_pk_add_f32 v[40:41], v[40:41], v[44:45]
s_nop 1
v_permlane32_swap_b32 v38, v40
v_permlane32_swap_b32 v39, v41
v_pk_add_f32 v[38:39], v[38:39], v[40:41]
s_nop 1
v_permlane16_swap_b32 v38, v39
v_add_f32 v38, v38, v39
s_nop 1
v_add_f32_dpp v38, v38, v38 row_ror:8 row_mask:0xf bank_mask:0xf
v_cvt_pk_bf16_f32 v38, 0, v38
v_add_f32 v39, v38, v79
v_add_f32 v38, v38, v79
s_nop 1
v_permlane16_swap_b32 v38, v39
v_cvt_pk_bf16_f32 v41, v38, v39
v_cvt_pk_bf16_f32 v40, v38, v39
s_nop 1
v_permlane32_swap_b32 v40, v41
s_bitcmp1_b32 s67, 0
s_cbranch_scc1 Cco_%=
v_min_u32 v40, -2, v40
v_min_u32 v41, -2, v41
buffer_store_dwordx2 v[40:41], v6, %[rw], s66 offen sc1
s_branch Cct_%=
Cco_%=:
s_bfm_b64 exec, 8, 0
buffer_store_dwordx2 v[40:41], v12, %[rout], s90 offen
s_lshl_b32 s63, %[j], 2
s_add_u32 s63, s63, s67
s_cmp_lg_u32 s63, 3
s_cbranch_scc1 Cce_%=
v_mov_b32 v42, s60
v_mov_b32 v43, s61
s_mov_b32 s89, 2113536
s_mov_b64 exec, 0x1
buffer_store_dwordx2 v[42:43], off, %[rw], s89 sc1
Cce_%=:
s_mov_b64 exec, -1
Cct_%=:
s_nop 4
Ccx_%=:
s_bitcmp1_b32 s67, 0
s_cbranch_scc1 Cdx_%=
s_add_u32 s89, s77, s66
s_add_u32 s90, s78, s66
s_add_u32 s83, s79, s66
s_add_u32 s83, s83, 262144
s_lshl_b32 s82, s67, 12
s_add_u32 s82, s82, 118784
v_mov_b32 v44, 0
v_mov_b32 v48, 0
v_mov_b32 v52, 0
v_mov_b32 v56, 0
v_mov_b32 v60, 0
v_add_u32 v68, 0xa00, v12
v_mov_b32 v69, 0
Cpd_%=:
buffer_load_dwordx4 v[22:25], v7, %[rw], s89 offen sc1
s_waitcnt vmcnt(0)
v_mov_b32 v32, 0
v_mov_b32 v36, 0
v_mov_b32 v40, 0
v_max3_u32 v66, v22, v23, v24
v_max_u32 v66, v66, v25
v_cmp_eq_u32 vcc, -1, v66
s_cbranch_vccz Cpdx_%=
s_sub_u32 s62, s62, 1
s_cmp_gt_i32 s62, 0
s_cbranch_scc1 Cpd_%=
Cpdx_%=:
v_smfmac_f32_16x16x64_bf16 v[32:35], v[22:25], a[0:7], v17
v_mov_b32_dpp v26, v22 row_shl:1 row_mask:0xf bank_mask:0xf bound_ctrl:1
v_smfmac_f32_16x16x64_bf16 v[48:51], v[22:25], a[32:39], v17
v_mov_b32_dpp v27, v23 row_shl:1 row_mask:0xf bank_mask:0xf bound_ctrl:1
v_smfmac_f32_16x16x64_bf16 v[36:39], v[22:25], a[8:15], v17
v_mov_b32_dpp v28, v24 row_shl:1 row_mask:0xf bank_mask:0xf bound_ctrl:1
v_smfmac_f32_16x16x64_bf16 v[52:55], v[22:25], a[40:47], v17
v_mov_b32_dpp v29, v25 row_shl:1 row_mask:0xf bank_mask:0xf bound_ctrl:1
v_smfmac_f32_16x16x64_bf16 v[40:43], v[22:25], a[16:23], v17
v_mov_b32_dpp v30, v22 row_shl:2 row_mask:0xf bank_mask:0x5 bound_ctrl:1
v_smfmac_f32_16x16x64_bf16 v[56:59], v[22:25], a[48:55], v17
v_mov_b32_dpp v31, v23 row_shl:2 row_mask:0xf bank_mask:0x5 bound_ctrl:1
v_smfmac_f32_16x16x64_bf16 v[44:47], v[22:25], a[24:31], v17
v_mov_b32_dpp v30, v24 row_shr:2 row_mask:0xf bank_mask:0xa bound_ctrl:1
v_smfmac_f32_16x16x64_bf16 v[60:63], v[22:25], a[56:63], v17
v_mov_b32_dpp v31, v25 row_shr:2 row_mask:0xf bank_mask:0xa bound_ctrl:1
v_smfmac_f32_16x16x64_bf16 v[32:35], v[26:29], a[64:71], v17
v_dot2_f32_bf16 v64, v22, s88, 0
v_dot2_f32_bf16 v65, v22, v22, 0
v_smfmac_f32_16x16x64_bf16 v[48:51], v[26:29], a[96:103], v17
v_dot2_f32_bf16 v64, v23, s88, v64
v_dot2_f32_bf16 v65, v23, v23, v65
v_smfmac_f32_16x16x64_bf16 v[36:39], v[26:29], a[72:79], v17
v_dot2_f32_bf16 v64, v24, s88, v64
v_dot2_f32_bf16 v65, v24, v24, v65
v_smfmac_f32_16x16x64_bf16 v[52:55], v[26:29], a[104:111], v17
v_dot2_f32_bf16 v64, v25, s88, v64
v_dot2_f32_bf16 v65, v25, v25, v65
v_smfmac_f32_16x16x64_bf16 v[40:43], v[26:29], a[80:87], v17
v_smfmac_f32_16x16x64_bf16 v[56:59], v[26:29], a[112:119], v17
v_cndmask_b32 v64, 0, v64, s[84:85]
v_cndmask_b32 v65, 0, v65, s[84:85]
v_smfmac_f32_16x16x64_bf16 v[44:47], v[26:29], a[88:95], v17
v_smfmac_f32_16x16x64_bf16 v[60:63], v[26:29], a[120:127], v17
v_permlane32_swap_b32 v64, v65
v_add_f32 v64, v64, v65
v_smfmac_f32_16x16x32_bf16 v[32:35], v[30:31], a[128:131], v17
v_mov_b32 v66, v64
v_smfmac_f32_16x16x32_bf16 v[48:51], v[30:31], a[144:147], v17
v_smfmac_f32_16x16x32_bf16 v[36:39], v[30:31], a[132:135], v17
v_permlane16_swap_b32 v64, v66
v_add_f32 v64, v64, v66
v_smfmac_f32_16x16x32_bf16 v[52:55], v[30:31], a[148:151], v17
v_smfmac_f32_16x16x32_bf16 v[40:43], v[30:31], a[136:139], v17
v_add_f32_dpp v64, v64, v64 quad_perm:[1,0,3,2] row_mask:0xf bank_mask:0xf
v_smfmac_f32_16x16x32_bf16 v[56:59], v[30:31], a[152:155], v17
v_smfmac_f32_16x16x32_bf16 v[44:47], v[30:31], a[140:143], v17
v_add_f32_dpp v64, v64, v64 quad_perm:[2,3,0,1] row_mask:0xf bank_mask:0xf
v_smfmac_f32_16x16x32_bf16 v[60:63], v[30:31], a[156:159], v17
s_nop 0
v_add_f32_dpp v64, v64, v64 row_half_mirror row_mask:0xf bank_mask:0xf
v_permlane16_swap_b32 v32, v48
v_add_f32 v72, v32, v48
v_permlane16_swap_b32 v36, v52
v_add_f32 v73, v36, v52
v_permlane16_swap_b32 v40, v56
v_add_f32 v74, v40, v56
v_permlane16_swap_b32 v44, v60
v_add_f32 v75, v44, v60
s_bfm_b64 exec, 32, 0
ds_write_b128 v8, v[72:75] offset:2560
s_mov_b32 exec_lo, 0x1
s_mov_b32 exec_hi, 0x1
ds_write_b32 v19, v64 offset:12352
s_mov_b64 exec, -1
s_waitcnt lgkmcnt(0)
s_barrier
ds_write_b128 v18, v[22:25]
s_cmp_lg_u32 %[w], 3
s_cbranch_scc1 Cdq_%=
ds_read_b128 v[84:87], v69 offset:12352
ds_read_b128 v[88:91], v69 offset:12368
ds_read2_b64 v[76:79], v68 offset1:80
ds_read2_b64 v[80:83], v68 offset0:160 offset1:240
s_waitcnt lgkmcnt(2)
v_pk_add_f32 v[84:85], v[84:85], v[86:87]
v_pk_add_f32 v[88:89], v[88:89], v[90:91]
v_pk_add_f32 v[84:85], v[84:85], v[88:89]
v_mul_f32 v84, 0x3acccccd, v84
v_mul_f32 v85, 0x3acccccd, v85
v_fma_f32 v85, -v84, v84, v85
v_cvt_pk_bf16_f32 v84, 0, v84
v_max_f32 v85, 0, v85
s_waitcnt lgkmcnt(0)
v_pk_add_f32 v[76:77], v[76:77], v[78:79]
v_cvt_pk_bf16_f32 v85, 0, v85
v_pk_add_f32 v[80:81], v[80:81], v[82:83]
v_add_f32 v85, 0x3727c5ac, v85
v_cvt_pk_bf16_f32 v85, 0, v85
v_sqrt_f32 v85, v85
v_pk_add_f32 v[76:77], v[76:77], v[80:81]
v_cvt_pk_bf16_f32 v85, 0, v85
v_rcp_f32 v85, v85
v_fma_f32 v76, -v84, v14, v76
v_fma_f32 v77, -v84, v15, v77
v_mul_f32 v76, v76, v85
v_mul_f32 v77, v77, v85
v_cvt_pk_bf16_f32 v70, v76, v77
v_min_u32 v76, -2, v70
buffer_store_dword v76, v11, %[rw], s90 offen sc1
s_call_b64 s[80:81], Clo_%=
Cdq_%=:
v_add_u32 v18, 0xa00, v18
Cdx_%=:
s_waitcnt lgkmcnt(0)
s_add_u32 s66, s66, 262144
s_add_u32 s67, s67, 1
s_cmp_lt_u32 s67, 4
s_cbranch_scc1 Cjob_%=
s_branch Cend_%=
Clo_%=:
s_nop 4
s_waitcnt vmcnt(1)
s_and_b32 s63, %[w], 1
s_lshl_b32 s63, s63, 8
v_add_u32 v84, s63, v11
v_add_u32 v85, s82, v0
v_lshrrev_b32 v86, 4, v0
v_and_b32 v86, 0x30, v86
v_add_u32 v86, s63, v86
ds_write_b32 v84, v70 offset:12416
ds_read_b128 v[22:25], v85 offset:0
ds_read_b128 v[26:29], v85 offset:1024
ds_read_b128 v[30:33], v85 offset:2048
ds_read_b128 v[34:37], v85 offset:3072
ds_read_b128 v[38:41], v86 offset:12416
ds_read_b128 v[42:45], v86 offset:12480
ds_read_b128 v[46:49], v86 offset:12544
ds_read_b128 v[50:53], v86 offset:12608
ds_read_b128 v[54:57], v85 offset:4096
ds_read_b128 v[58:61], v85 offset:5120
ds_read_b128 v[62:65], v85 offset:6144
ds_read_b128 v[66:69], v85 offset:7168
s_waitcnt lgkmcnt(4)
v_mfma_f32_16x16x32_bf16 v[72:75], v[22:25], v[38:41], 0
v_mfma_f32_16x16x32_bf16 v[72:75], v[26:29], v[42:45], v[72:75]
v_mfma_f32_16x16x32_bf16 v[72:75], v[30:33], v[46:49], v[72:75]
v_mfma_f32_16x16x32_bf16 v[72:75], v[34:37], v[50:53], v[72:75]
s_nop 7
v_cvt_pk_bf16_f32 v72, 0, v72
v_cvt_pk_bf16_f32 v73, 0, v73
v_cvt_pk_bf16_f32 v74, 0, v74
v_cvt_pk_bf16_f32 v75, 0, v75
v_mul_f32 v72, 0x3db504f3, v72
v_mul_f32 v73, 0x3db504f3, v73
v_mul_f32 v74, 0x3db504f3, v74
v_mul_f32 v75, 0x3db504f3, v75
v_cvt_pk_bf16_f32 v72, 0, v72
v_cvt_pk_bf16_f32 v73, 0, v73
v_cvt_pk_bf16_f32 v74, 0, v74
v_cvt_pk_bf16_f32 v75, 0, v75
v_max3_f32 v76, v72, v73, v74
v_max_f32 v76, v76, v75
v_mov_b32 v78, v76
s_nop 1
v_permlane16_swap_b32 v76, v78
v_max_f32 v76, v76, v78
v_mov_b32 v78, v76
s_nop 1
v_permlane32_swap_b32 v76, v78
v_max_f32 v76, v76, v78
v_mul_f32 v76, s99, v76
v_fma_f32 v72, v72, s99, -v76
v_fma_f32 v73, v73, s99, -v76
v_fma_f32 v74, v74, s99, -v76
v_fma_f32 v75, v75, s99, -v76
v_exp_f32 v72, v72
v_exp_f32 v73, v73
v_exp_f32 v74, v74
v_exp_f32 v75, v75
v_add_f32 v77, v72, v73
v_add_f32 v78, v74, v75
v_add_f32 v77, v77, v78
v_cvt_pk_bf16_f32 v72, v72, v73
v_cvt_pk_bf16_f32 v73, v74, v75
s_waitcnt lgkmcnt(0)
s_nop 0
v_mfma_f32_16x16x16_bf16 v[22:25], v[72:73], v[54:55], 0
v_mfma_f32_16x16x16_bf16 v[26:29], v[72:73], v[56:57], 0
v_mfma_f32_16x16x16_bf16 v[30:33], v[72:73], v[58:59], 0
v_mfma_f32_16x16x16_bf16 v[34:37], v[72:73], v[60:61], 0
v_mfma_f32_16x16x16_bf16 v[38:41], v[72:73], v[62:63], 0
v_mfma_f32_16x16x16_bf16 v[42:45], v[72:73], v[64:65], 0
v_mfma_f32_16x16x16_bf16 v[46:49], v[72:73], v[66:67], 0
v_mfma_f32_16x16x16_bf16 v[50:53], v[72:73], v[68:69], 0
v_mov_b32 v78, v77
s_nop 1
v_permlane16_swap_b32 v77, v78
v_add_f32 v77, v77, v78
v_mov_b32 v78, v77
s_nop 1
v_permlane32_swap_b32 v77, v78
v_add_f32 v77, v77, v78
v_and_b32 v84, 0xf0, v0
v_lshrrev_b32 v85, 4, v0
v_and_b32 v85, 0x10, v85
v_lshl_add_u32 v84, v84, 1, v85
v_mov_b32 v85, 0x200
s_nop 1
v_mov_b32_dpp v84, v85 quad_perm:[0,1,2,3] row_mask:0xc bank_mask:0xf
v_mov_b32 v80, v22
v_mov_b32 v81, v26
v_mov_b32 v82, v30
v_mov_b32 v83, v34
v_mov_b32_dpp v80, v38 quad_perm:[0,1,2,3] row_mask:0x2 bank_mask:0xf
v_mov_b32_dpp v81, v42 quad_perm:[0,1,2,3] row_mask:0x2 bank_mask:0xf
v_mov_b32_dpp v82, v46 quad_perm:[0,1,2,3] row_mask:0x2 bank_mask:0xf
v_mov_b32_dpp v83, v50 quad_perm:[0,1,2,3] row_mask:0x2 bank_mask:0xf
v_mov_b32_dpp v80, v76 quad_perm:[0,1,2,3] row_mask:0x4 bank_mask:0xf
v_mov_b32_dpp v81, v77 quad_perm:[0,1,2,3] row_mask:0x4 bank_mask:0xf
v_min_u32 v80, -2, v80
v_min_u32 v81, -2, v81
v_min_u32 v82, -2, v82
v_min_u32 v83, -2, v83
s_bfm_b64 exec, 33, 0
buffer_store_dwordx4 v[80:83], v84, %[rw], s83 offen sc1
s_mov_b64 exec, -1
s_nop 4
s_setpc_b64 s[80:81]
Cend_%=:
s_waitcnt vmcnt(0)
.Lendc:
)ASM"
        :
        : [rw] "s"(rw), [rq] "s"(rq), [ro] "s"(ro), [r1] "s"(r1), [r2] "s"(r2), [rx] "s"(rx), [rk] "s"(rk), [rv] "s"(rv), [rout] "s"(rout), [ep] "s"(ep), [w] "s"(w), [j] "s"(j), [lb] "s"(lb)
        : "v0","v1","v2","v3","v4","v5","v6","v7","v8","v9","v10","v11","v12","v13","v14","v15","v16","v17","v18","v19","v20","v21","v22","v23","v24","v25","v26","v27","v28","v29","v30","v31","v32","v33","v34","v35","v36","v37","v38","v39","v40","v41","v42","v43","v44","v45","v46","v47","v48","v49","v50","v51","v52","v53","v54","v55","v56","v57","v58","v59","v60","v61","v62","v63","v64","v65","v66","v67","v68","v69","v70","v71","v72","v73","v74","v75","v76","v77","v78","v79","v80","v81","v82","v83","v84","v85","v86","v87","v88","v89","v90","v91","v92","v93","v94","v95","v96","v97","v98","v99","v100","v101","v102","v103","v104","v105","v106","v107","v108","v109","v110","v111","v112","v113","v114","v115","v116","v117","v118","v119","v120","v121","v122","v123","v124","v125","v126","v127","v128","v129","v130","v131","v132","v133","v134","v135","v136","v137","v138","v139","v140","v141","v142","v143","v144","v145","v146","v147","v148","v149","v150","v151","v152","v153","v154","v155","v156","v157","v158","v159","v160","v161","v162","v163","v164","v165","v166","v167","v168","v169","v170","v171","v172","v173","v174","v175","v176","v177","v178","v179","v180","v181","v182","v183","v184","v185","v186","v187","v188","v189","v190","v191","v192","v193","v194","v195","v196","v197","v198","v199","v200","v201","v202","v203","v204","v205","v206","v207","v208","v209","v210","v211","v212","v213","v214","v215","v216","v217","v218","v219","v220","v221","v222","v223","v224","v225","v226","v227","v228","v229","v230","v231","v232","v233","v234","v235","v236","v237","v238","v239","v240","v241","v242","v243","v244","v245","v246","v247","v248","v249","v250","v251","v252","v253","v254","v255","a0","a1","a2","a3","a4","a5","a6","a7","a8","a9","a10","a11","a12","a13","a14","a15","a16","a17","a18","a19","a20","a21","a22","a23","a24","a25","a26","a27","a28","a29","a30","a31","a32","a33","a34","a35","a36","a37","a38","a39","a40","a41","a42","a43","a44","a45","a46","a47","a48","a49","a50","a51","a52","a53","a54","a55","a56","a57","a58","a59","a60","a61","a62","a63","a64","a65","a66","a67","a68","a69","a70","a71","a72","a73","a74","a75","a76","a77","a78","a79","a80","a81","a82","a83","a84","a85","a86","a87","a88","a89","a90","a91","a92","a93","a94","a95","a96","a97","a98","a99","a100","a101","a102","a103","a104","a105","a106","a107","a108","a109","a110","a111","a112","a113","a114","a115","a116","a117","a118","a119","a120","a121","a122","a123","a124","a125","a126","a127","a128","a129","a130","a131","a132","a133","a134","a135","a136","a137","a138","a139","a140","a141","a142","a143","a144","a145","a146","a147","a148","a149","a150","a151","a152","a153","a154","a155","a156","a157","a158","a159","a160","a161","a162","a163","a164","a165","a166","a167","a168","a169","a170","a171","a172","a173","a174","a175","a176","a177","a178","a179","a180","a181","a182","a183","a184","a185","a186","a187","a188","a189","a190","a191","a192","a193","a194","a195","a196","a197","a198","a199","a200","a201","a202","a203","a204","a205","a206","a207","a208","a209","a210","a211","a212","a213","a214","a215","a216","a217","a218","a219","a220","a221","a222","a223","a224","a225","a226","a227","a228","a229","a230","a231","a232","a233","a234","a235","a236","a237","a238","a239","a240","a241","a242","a243","a244","a245","a246","a247","a248","a249","a250","a251","a252","a253","a254","a255","s60","s61","s62","s63","s64","s65","s66","s67","s68","s69","s70","s71","s72","s73","s74","s75","s76","s77","s78","s79","s80","s81","s82","s83","s84","s85","s86","s87","s88","s89","s90","s91","s92","s93","s94","s95","s96","s97","s98","s99", "vcc", "scc", "memory");
  } else {
    const i32x4 rk = srd(kc, off, 134217728);
    u64 t0;
    asm volatile(
R"ASM(s_memrealtime %[t0]
v_mbcnt_lo_u32_b32 v38, -1, 0
s_lshl_b32 s41, %[w], 10
v_mbcnt_hi_u32_b32 v38, -1, v38
s_mul_i32 s53, %[w], 39936
s_add_u32 s53, s53, %[lb]
v_lshl_add_u32 v0, v38, 4, s41
s_sub_u32 s35, %[rot], 2064384
s_add_u32 m0, s53, 0
s_add_u32 s40, %[rot], 0
s_and_b32 s40, s40, 28672
s_add_u32 s40, s40, 0
buffer_load_dwordx4 v0, %[rk], s40 offen nt lds
s_add_u32 m0, s53, 1024
s_add_u32 s40, %[rot], 4096
s_and_b32 s40, s40, 28672
s_add_u32 s40, s40, 0
buffer_load_dwordx4 v0, %[rk], s40 offen nt lds
s_add_u32 m0, s53, 2048
s_add_u32 s40, %[rot], 8192
s_and_b32 s40, s40, 28672
s_add_u32 s40, s40, 0
buffer_load_dwordx4 v0, %[rk], s40 offen nt lds
s_add_u32 m0, s53, 3072
s_add_u32 s40, %[rot], 12288
s_and_b32 s40, s40, 28672
s_add_u32 s40, s40, 0
buffer_load_dwordx4 v0, %[rk], s40 offen nt lds
s_add_u32 m0, s53, 4096
s_add_u32 s40, %[rot], 16384
s_and_b32 s40, s40, 28672
s_add_u32 s40, s40, 0
buffer_load_dwordx4 v0, %[rk], s40 offen nt lds
s_add_u32 m0, s53, 5120
s_add_u32 s40, %[rot], 20480
s_and_b32 s40, s40, 28672
s_add_u32 s40, s40, 0
buffer_load_dwordx4 v0, %[rk], s40 offen nt lds
s_add_u32 m0, s53, 6144
s_add_u32 s40, %[rot], 24576
s_and_b32 s40, s40, 28672
s_add_u32 s40, s40, 0
buffer_load_dwordx4 v0, %[rk], s40 offen nt lds
s_add_u32 m0, s53, 7168
s_add_u32 s40, %[rot], 28672
s_and_b32 s40, s40, 28672
s_add_u32 s40, s40, 0
buffer_load_dwordx4 v0, %[rk], s40 offen nt lds
s_waitcnt lgkmcnt(0)
)ASM"
        : [t0] "=&s"(t0)
        : [rk] "s"(rk), [w] "s"(w), [lb] "s"(lb), [rot] "s"(rot)
        : "v0", "v38", "s35", "s40", "s41", "s43", "s53", "scc", "memory");
    __builtin_amdgcn_sched_barrier(0);
    const u64 wa = ((u64)ws + 2097151ull) & ~2097151ull, ep = wa + W_EP;
    const i32x4 rw = srd((const void*)wa, 0, W_END), rv = srd(vc, off, 134217728);
    const u32 po = (h & 15) * W_CH + c * 640, q2 = (h & 15) * W_CH + 5120, xh = (h >> 4) << 13, fl = W_FA + ((h * 7 + c) * 4 + w) * 8;
    const u32 cf = W_FC1 + h * 8, cf2 = W_FC2 + h * 32 + 24, qo = W_Q1 + h * 512, pf = (sp * 4 + w) * 1024;
    const u32 dbg = W_DBG + ((h * 7 + c) * 4 + w) * 128;
    const u32 x7 = xcd < 7 ? xcd : 0, n2 = xcd < 6 ? 327680 - 128 : xcd == 6 ? 245760 - 128 : 0;
    const u64 pq = (u64)wq + 1024 * xcd, po2 = (u64)wo + 655360 * xcd, p1 = (u64)w1 + 512 * x7, p2 = (u64)w2 + 327680 * x7;
    asm volatile(
R"ASM(s_load_dwordx2 s[58:59], %[ep], 0x0
v_mbcnt_lo_u32_b32 v38, -1, 0
s_lshl_b32 s41, %[w], 10
v_mbcnt_hi_u32_b32 v38, -1, v38
s_mul_i32 s53, %[w], 39936
s_add_u32 s53, s53, %[lb]
v_lshl_add_u32 v0, v38, 4, s41
s_sub_u32 s35, %[rot], 2064384
s_lshr_b32 s42, %[pf], 10
s_mul_i32 s42, s42, 46
v_lshrrev_b32 v31, 4, v0
v_and_b32 v31, 63, v31
v_add_u32 v31, s42, v31
v_lshrrev_b32 v35, 3, v31
v_and_b32 v31, 7, v31
v_mul_u32_u24 v35, 64, v35
v_add_u32 v31, v35, v31
v_lshlrev_b32 v31, 7, v31
s_mov_b32 s42, 5235584
v_min_u32 v31, s42, v31
s_bfm_b64 exec, 46, 0
global_load_dword v36, v31, %[pq] nt
global_load_dword v36, v31, %[pq] sc1
s_mov_b64 exec, -1
s_mov_b32 s34, %[t0]
v_lshl_add_u32 v1, v38, 4, s53
s_mov_b64 exec, 1
v_mov_b32 v12, 0
v_mov_b32 v13, 0
v_mov_b32 v14, 0
v_mov_b32 v15, 0
s_add_u32 s41, %[lb], 161824
v_mov_b32 v29, s41
ds_write_b128 v29, v[12:15]
s_mov_b64 exec, -1
v_and_b32 v29, 3, v38
v_mov_b32 v30, 0xffff
v_mov_b32 v26, 0xffff0000
v_cmp_eq_u32 vcc, 0, v29
s_nop 1
v_cndmask_b32 v2, 0, v30, vcc
v_cmp_eq_u32 vcc, 1, v29
s_nop 1
v_cndmask_b32 v2, v2, v26, vcc
v_cmp_eq_u32 vcc, 2, v29
s_nop 1
v_cndmask_b32 v3, 0, v30, vcc
v_cmp_eq_u32 vcc, 3, v29
s_nop 1
v_cndmask_b32 v3, v3, v26, vcc
v_and_b32 v39, 15, v38
v_lshrrev_b32 v29, 4, v38
v_lshlrev_b32 v29, 3, v29
v_lshl_add_u32 v38, v39, 5, v29
v_lshlrev_b32 v39, 4, v39
s_mov_b32 s44, 0xcccccccc
s_mov_b32 s45, 0xcccccccc
s_mov_b32 s46, 0xaaaaaaaa
s_mov_b32 s47, 0xaaaaaaaa
s_mov_b32 s54, 0x3fb8aa3b
s_mov_b32 s55, 0x3db504f3
s_mov_b32 s52, 0x9c40
s_mov_b32 s51, 0x0
s_waitcnt lgkmcnt(0)
s_add_u32 s58, s58, 1
s_addc_u32 s59, s59, 0
s_barrier
s_waitcnt vmcnt(9)
Lpc0_%=:
s_memrealtime s[56:57]
s_waitcnt lgkmcnt(0)
s_sub_u32 s56, s56, s34
s_cmp_lt_u32 s56, 115
s_cbranch_scc1 Lpc0_%=
s_add_u32 m0, s53, 8192
s_add_u32 s40, %[rot], 0
s_and_b32 s40, s40, 28672
s_add_u32 s40, s40, 229376
buffer_load_dwordx4 v0, %[rk], s40 offen nt lds
s_waitcnt vmcnt(9)
s_add_u32 m0, s53, 9216
s_add_u32 s40, %[rot], 4096
s_and_b32 s40, s40, 28672
s_add_u32 s40, s40, 229376
buffer_load_dwordx4 v0, %[rk], s40 offen nt lds
s_lshr_b32 s42, %[pf], 10
s_mul_i32 s42, s42, 46
v_lshrrev_b32 v32, 4, v0
v_and_b32 v32, 63, v32
v_add_u32 v32, s42, v32
v_lshlrev_b32 v32, 7, v32
s_mov_b32 s42, 655232
v_min_u32 v32, s42, v32
s_bfm_b64 exec, 46, 0
global_load_dword v36, v32, %[po2] nt
global_load_dword v36, v32, %[po2] sc1
s_mov_b64 exec, -1
s_waitcnt vmcnt(11)
s_add_u32 m0, s53, 10240
s_add_u32 s40, %[rot], 8192
s_and_b32 s40, s40, 28672
s_add_u32 s40, s40, 229376
buffer_load_dwordx4 v0, %[rk], s40 offen nt lds
s_waitcnt vmcnt(11)
s_add_u32 m0, s53, 11264
s_add_u32 s40, %[rot], 12288
s_and_b32 s40, s40, 28672
s_add_u32 s40, s40, 229376
buffer_load_dwordx4 v0, %[rk], s40 offen nt lds
s_waitcnt vmcnt(11)
Lpc1_%=:
s_memrealtime s[56:57]
s_waitcnt lgkmcnt(0)
s_sub_u32 s56, s56, s34
s_cmp_lt_u32 s56, 172
s_cbranch_scc1 Lpc1_%=
s_add_u32 m0, s53, 12288
s_add_u32 s40, %[rot], 16384
s_and_b32 s40, s40, 28672
s_add_u32 s40, s40, 229376
buffer_load_dwordx4 v0, %[rk], s40 offen nt lds
s_waitcnt vmcnt(11)
s_add_u32 m0, s53, 13312
s_add_u32 s40, %[rot], 20480
s_and_b32 s40, s40, 28672
s_add_u32 s40, s40, 229376
buffer_load_dwordx4 v0, %[rk], s40 offen nt lds
s_lshr_b32 s42, %[pf], 10
s_mul_i32 s42, s42, 23
v_lshrrev_b32 v33, 4, v0
v_and_b32 v33, 63, v33
v_add_u32 v33, s42, v33
v_lshrrev_b32 v35, 2, v33
v_and_b32 v33, 3, v33
v_mul_u32_u24 v35, 27, v35
v_add_u32 v33, v35, v33
v_lshlrev_b32 v33, 7, v33
s_cmp_lg_u32 %[n2], 0
s_cselect_b32 s42, 2208640, 0
v_min_u32 v33, s42, v33
s_bfm_b64 exec, 23, 0
global_load_dword v36, v33, %[p1] nt
global_load_dword v36, v33, %[p1] sc1
s_mov_b64 exec, -1
s_waitcnt vmcnt(13)
s_add_u32 m0, s53, 14336
s_add_u32 s40, %[rot], 24576
s_and_b32 s40, s40, 28672
s_add_u32 s40, s40, 229376
buffer_load_dwordx4 v0, %[rk], s40 offen nt lds
s_waitcnt vmcnt(13)
s_add_u32 m0, s53, 15360
s_add_u32 s40, %[rot], 28672
s_and_b32 s40, s40, 28672
s_add_u32 s40, s40, 229376
buffer_load_dwordx4 v0, %[rk], s40 offen nt lds
s_lshr_b32 s42, %[pf], 10
s_mul_i32 s42, s42, 23
v_lshrrev_b32 v34, 4, v0
v_and_b32 v34, 63, v34
v_add_u32 v34, s42, v34
v_lshlrev_b32 v34, 7, v34
s_mov_b32 s42, %[n2]
v_min_u32 v34, s42, v34
s_bfm_b64 exec, 23, 0
global_load_dword v36, v34, %[p2] nt
global_load_dword v36, v34, %[p2] sc1
s_mov_b64 exec, -1
s_waitcnt vmcnt(13)
Lpc2_%=:
s_memrealtime s[56:57]
s_waitcnt lgkmcnt(0)
s_sub_u32 s56, s56, s34
s_cmp_lt_u32 s56, 230
s_cbranch_scc1 Lpc2_%=
s_add_u32 s40, %[rot], 0
s_and_b32 s40, s40, 28672
s_add_u32 s40, s40, 0
buffer_load_dwordx4 a[0:3], v0, %[rv], s40 offen nt
s_waitcnt vmcnt(13)
s_add_u32 s40, %[rot], 4096
s_and_b32 s40, s40, 28672
s_add_u32 s40, s40, 0
buffer_load_dwordx4 a[4:7], v0, %[rv], s40 offen nt
s_waitcnt vmcnt(11)
s_add_u32 s40, %[rot], 8192
s_and_b32 s40, s40, 28672
s_add_u32 s40, s40, 0
buffer_load_dwordx4 a[8:11], v0, %[rv], s40 offen nt
s_waitcnt vmcnt(11)
s_add_u32 s40, %[rot], 12288
s_and_b32 s40, s40, 28672
s_add_u32 s40, s40, 0
buffer_load_dwordx4 a[12:15], v0, %[rv], s40 offen nt
s_waitcnt vmcnt(11)
Lpc3_%=:
s_memrealtime s[56:57]
s_waitcnt lgkmcnt(0)
s_sub_u32 s56, s56, s34
s_cmp_lt_u32 s56, 288
s_cbranch_scc1 Lpc3_%=
s_add_u32 s40, %[rot], 16384
s_and_b32 s40, s40, 28672
s_add_u32 s40, s40, 0
buffer_load_dwordx4 a[16:19], v0, %[rv], s40 offen nt
s_waitcnt vmcnt(11)
s_add_u32 s40, %[rot], 20480
s_and_b32 s40, s40, 28672
s_add_u32 s40, s40, 0
buffer_load_dwordx4 a[20:23], v0, %[rv], s40 offen nt
s_waitcnt vmcnt(9)
s_add_u32 s40, %[rot], 24576
s_and_b32 s40, s40, 28672
s_add_u32 s40, s40, 0
buffer_load_dwordx4 a[24:27], v0, %[rv], s40 offen nt
s_waitcnt vmcnt(9)
s_add_u32 s40, %[rot], 28672
s_and_b32 s40, s40, 28672
s_add_u32 s40, s40, 0
buffer_load_dwordx4 a[28:31], v0, %[rv], s40 offen nt
s_waitcnt vmcnt(7)
Lpc4_%=:
s_memrealtime s[56:57]
s_waitcnt lgkmcnt(0)
s_sub_u32 s56, s56, s34
s_cmp_lt_u32 s56, 345
s_cbranch_scc1 Lpc4_%=
s_add_u32 s40, %[rot], 0
s_and_b32 s40, s40, 28672
s_add_u32 s40, s40, 229376
buffer_load_dwordx4 a[32:35], v0, %[rv], s40 offen nt
s_waitcnt vmcnt(7)
s_add_u32 s40, %[rot], 4096
s_and_b32 s40, s40, 28672
s_add_u32 s40, s40, 229376
buffer_load_dwordx4 a[36:39], v0, %[rv], s40 offen nt
s_waitcnt vmcnt(7)
s_add_u32 s40, %[rot], 8192
s_and_b32 s40, s40, 28672
s_add_u32 s40, s40, 229376
buffer_load_dwordx4 a[40:43], v0, %[rv], s40 offen nt
s_waitcnt vmcnt(7)
s_add_u32 s40, %[rot], 12288
s_and_b32 s40, s40, 28672
s_add_u32 s40, s40, 229376
buffer_load_dwordx4 a[44:47], v0, %[rv], s40 offen nt
s_waitcnt vmcnt(7)
Lpc5_%=:
s_memrealtime s[56:57]
s_waitcnt lgkmcnt(0)
s_sub_u32 s56, s56, s34
s_cmp_lt_u32 s56, 403
s_cbranch_scc1 Lpc5_%=
s_add_u32 s40, %[rot], 16384
s_and_b32 s40, s40, 28672
s_add_u32 s40, s40, 229376
buffer_load_dwordx4 a[48:51], v0, %[rv], s40 offen nt
s_waitcnt vmcnt(7)
s_add_u32 s40, %[rot], 20480
s_and_b32 s40, s40, 28672
s_add_u32 s40, s40, 229376
buffer_load_dwordx4 a[52:55], v0, %[rv], s40 offen nt
s_waitcnt vmcnt(7)
s_add_u32 s40, %[rot], 24576
s_and_b32 s40, s40, 28672
s_add_u32 s40, s40, 229376
buffer_load_dwordx4 a[56:59], v0, %[rv], s40 offen nt
s_waitcnt vmcnt(7)
s_add_u32 s40, %[rot], 28672
s_and_b32 s40, s40, 28672
s_add_u32 s40, s40, 229376
buffer_load_dwordx4 a[60:63], v0, %[rv], s40 offen nt
s_waitcnt vmcnt(7)
Lpc6_%=:
s_memrealtime s[56:57]
s_waitcnt lgkmcnt(0)
s_sub_u32 s56, s56, s34
s_cmp_lt_u32 s56, 460
s_cbranch_scc1 Lpc6_%=
s_add_u32 m0, s53, 16384
s_add_u32 s40, %[rot], 0
s_and_b32 s40, s40, 28672
s_add_u32 s40, s40, 458752
buffer_load_dwordx4 v0, %[rk], s40 offen nt lds
s_waitcnt vmcnt(7)
s_add_u32 m0, s53, 17408
s_add_u32 s40, %[rot], 4096
s_and_b32 s40, s40, 28672
s_add_u32 s40, s40, 458752
buffer_load_dwordx4 v0, %[rk], s40 offen nt lds
s_waitcnt vmcnt(7)
s_add_u32 m0, s53, 18432
s_add_u32 s40, %[rot], 8192
s_and_b32 s40, s40, 28672
s_add_u32 s40, s40, 458752
buffer_load_dwordx4 v0, %[rk], s40 offen nt lds
s_waitcnt vmcnt(7)
s_add_u32 m0, s53, 19456
s_add_u32 s40, %[rot], 12288
s_and_b32 s40, s40, 28672
s_add_u32 s40, s40, 458752
buffer_load_dwordx4 v0, %[rk], s40 offen nt lds
s_waitcnt vmcnt(7)
Lpc7_%=:
s_memrealtime s[56:57]
s_waitcnt lgkmcnt(0)
s_sub_u32 s56, s56, s34
s_cmp_lt_u32 s56, 518
s_cbranch_scc1 Lpc7_%=
s_add_u32 m0, s53, 20480
s_add_u32 s40, %[rot], 16384
s_and_b32 s40, s40, 28672
s_add_u32 s40, s40, 458752
buffer_load_dwordx4 v0, %[rk], s40 offen nt lds
s_waitcnt vmcnt(7)
s_add_u32 m0, s53, 21504
s_add_u32 s40, %[rot], 20480
s_and_b32 s40, s40, 28672
s_add_u32 s40, s40, 458752
buffer_load_dwordx4 v0, %[rk], s40 offen nt lds
s_waitcnt vmcnt(7)
s_add_u32 m0, s53, 22528
s_add_u32 s40, %[rot], 24576
s_and_b32 s40, s40, 28672
s_add_u32 s40, s40, 458752
buffer_load_dwordx4 v0, %[rk], s40 offen nt lds
s_waitcnt vmcnt(7)
s_add_u32 m0, s53, 23552
s_add_u32 s40, %[rot], 28672
s_and_b32 s40, s40, 28672
s_add_u32 s40, s40, 458752
buffer_load_dwordx4 v0, %[rk], s40 offen nt lds
s_waitcnt vmcnt(7)
Lpc8_%=:
s_memrealtime s[56:57]
s_waitcnt lgkmcnt(0)
s_sub_u32 s56, s56, s34
s_cmp_lt_u32 s56, 576
s_cbranch_scc1 Lpc8_%=
s_add_u32 m0, s53, 24576
s_add_u32 s40, %[rot], 0
s_and_b32 s40, s40, 28672
s_add_u32 s40, s40, 688128
buffer_load_dwordx4 v0, %[rk], s40 offen nt lds
s_waitcnt vmcnt(7)
s_add_u32 m0, s53, 25600
s_add_u32 s40, %[rot], 4096
s_and_b32 s40, s40, 28672
s_add_u32 s40, s40, 688128
buffer_load_dwordx4 v0, %[rk], s40 offen nt lds
s_waitcnt vmcnt(7)
s_add_u32 m0, s53, 26624
s_add_u32 s40, %[rot], 8192
s_and_b32 s40, s40, 28672
s_add_u32 s40, s40, 688128
buffer_load_dwordx4 v0, %[rk], s40 offen nt lds
s_waitcnt vmcnt(7)
s_add_u32 m0, s53, 27648
s_add_u32 s40, %[rot], 12288
s_and_b32 s40, s40, 28672
s_add_u32 s40, s40, 688128
buffer_load_dwordx4 v0, %[rk], s40 offen nt lds
s_waitcnt vmcnt(7)
Lpc9_%=:
s_memrealtime s[56:57]
s_waitcnt lgkmcnt(0)
s_sub_u32 s56, s56, s34
s_cmp_lt_u32 s56, 633
s_cbranch_scc1 Lpc9_%=
s_add_u32 m0, s53, 28672
s_add_u32 s40, %[rot], 16384
s_and_b32 s40, s40, 28672
s_add_u32 s40, s40, 688128
buffer_load_dwordx4 v0, %[rk], s40 offen nt lds
s_waitcnt vmcnt(7)
s_add_u32 m0, s53, 29696
s_add_u32 s40, %[rot], 20480
s_and_b32 s40, s40, 28672
s_add_u32 s40, s40, 688128
buffer_load_dwordx4 v0, %[rk], s40 offen nt lds
s_waitcnt vmcnt(7)
s_add_u32 m0, s53, 30720
s_add_u32 s40, %[rot], 24576
s_and_b32 s40, s40, 28672
s_add_u32 s40, s40, 688128
buffer_load_dwordx4 v0, %[rk], s40 offen nt lds
s_waitcnt vmcnt(7)
s_add_u32 m0, s53, 31744
s_add_u32 s40, %[rot], 28672
s_and_b32 s40, s40, 28672
s_add_u32 s40, s40, 688128
buffer_load_dwordx4 v0, %[rk], s40 offen nt lds
s_waitcnt vmcnt(7)
Lpc10_%=:
s_memrealtime s[56:57]
s_waitcnt lgkmcnt(0)
s_sub_u32 s56, s56, s34
s_cmp_lt_u32 s56, 691
s_cbranch_scc1 Lpc10_%=
s_add_u32 s40, %[rot], 0
)ASM"
R"ASM(s_and_b32 s40, s40, 28672
s_add_u32 s40, s40, 458752
buffer_load_dwordx4 a[64:67], v0, %[rv], s40 offen nt
s_waitcnt vmcnt(7)
s_add_u32 s40, %[rot], 4096
s_and_b32 s40, s40, 28672
s_add_u32 s40, s40, 458752
buffer_load_dwordx4 a[68:71], v0, %[rv], s40 offen nt
s_waitcnt vmcnt(7)
s_add_u32 s40, %[rot], 8192
s_and_b32 s40, s40, 28672
s_add_u32 s40, s40, 458752
buffer_load_dwordx4 a[72:75], v0, %[rv], s40 offen nt
s_waitcnt vmcnt(7)
s_add_u32 s40, %[rot], 12288
s_and_b32 s40, s40, 28672
s_add_u32 s40, s40, 458752
buffer_load_dwordx4 a[76:79], v0, %[rv], s40 offen nt
s_waitcnt vmcnt(7)
Lpc11_%=:
s_memrealtime s[56:57]
s_waitcnt lgkmcnt(0)
s_sub_u32 s56, s56, s34
s_cmp_lt_u32 s56, 748
s_cbranch_scc1 Lpc11_%=
s_add_u32 s40, %[rot], 16384
s_and_b32 s40, s40, 28672
s_add_u32 s40, s40, 458752
buffer_load_dwordx4 a[80:83], v0, %[rv], s40 offen nt
s_waitcnt vmcnt(7)
s_add_u32 s40, %[rot], 20480
s_and_b32 s40, s40, 28672
s_add_u32 s40, s40, 458752
buffer_load_dwordx4 a[84:87], v0, %[rv], s40 offen nt
s_waitcnt vmcnt(7)
s_add_u32 s40, %[rot], 24576
s_and_b32 s40, s40, 28672
s_add_u32 s40, s40, 458752
buffer_load_dwordx4 a[88:91], v0, %[rv], s40 offen nt
s_waitcnt vmcnt(7)
s_add_u32 s40, %[rot], 28672
s_and_b32 s40, s40, 28672
s_add_u32 s40, s40, 458752
buffer_load_dwordx4 a[92:95], v0, %[rv], s40 offen nt
s_waitcnt vmcnt(7)
Lpc12_%=:
s_memrealtime s[56:57]
s_waitcnt lgkmcnt(0)
s_sub_u32 s56, s56, s34
s_cmp_lt_u32 s56, 806
s_cbranch_scc1 Lpc12_%=
s_add_u32 s40, %[rot], 0
s_and_b32 s40, s40, 28672
s_add_u32 s40, s40, 688128
buffer_load_dwordx4 a[96:99], v0, %[rv], s40 offen nt
s_waitcnt vmcnt(7)
s_add_u32 s40, %[rot], 4096
s_and_b32 s40, s40, 28672
s_add_u32 s40, s40, 688128
buffer_load_dwordx4 a[100:103], v0, %[rv], s40 offen nt
buffer_load_dwordx2 v[4:5], off, %[rw], %[cf] sc1
s_waitcnt vmcnt(8)
s_add_u32 s40, %[rot], 8192
s_and_b32 s40, s40, 28672
s_add_u32 s40, s40, 688128
buffer_load_dwordx4 a[104:107], v0, %[rv], s40 offen nt
s_waitcnt vmcnt(8)
s_add_u32 s40, %[rot], 12288
s_and_b32 s40, s40, 28672
s_add_u32 s40, s40, 688128
buffer_load_dwordx4 a[108:111], v0, %[rv], s40 offen nt
s_waitcnt vmcnt(8)
Lpc13_%=:
s_memrealtime s[56:57]
s_waitcnt lgkmcnt(0)
s_sub_u32 s56, s56, s34
s_cmp_lt_u32 s56, 864
s_cbranch_scc1 Lpc13_%=
s_add_u32 s40, %[rot], 16384
s_and_b32 s40, s40, 28672
s_add_u32 s40, s40, 688128
buffer_load_dwordx4 a[112:115], v0, %[rv], s40 offen nt
s_waitcnt vmcnt(8)
s_add_u32 s40, %[rot], 20480
s_and_b32 s40, s40, 28672
s_add_u32 s40, s40, 688128
buffer_load_dwordx4 a[116:119], v0, %[rv], s40 offen nt
s_waitcnt vmcnt(8)
s_add_u32 s40, %[rot], 24576
s_and_b32 s40, s40, 28672
s_add_u32 s40, s40, 688128
buffer_load_dwordx4 a[120:123], v0, %[rv], s40 offen nt
s_waitcnt vmcnt(8)
s_add_u32 s40, %[rot], 28672
s_and_b32 s40, s40, 28672
s_add_u32 s40, s40, 688128
buffer_load_dwordx4 a[124:127], v0, %[rv], s40 offen nt
s_waitcnt vmcnt(8)
Lpc14_%=:
s_memrealtime s[56:57]
s_waitcnt lgkmcnt(0)
s_sub_u32 s56, s56, s34
s_cmp_lt_u32 s56, 921
s_cbranch_scc1 Lpc14_%=
s_add_u32 m0, s53, 32768
s_add_u32 s40, %[rot], 0
s_and_b32 s40, s40, 28672
s_add_u32 s40, s40, 917504
buffer_load_dwordx4 v0, %[rk], s40 offen nt lds
s_waitcnt vmcnt(8)
s_add_u32 m0, s53, 33792
s_add_u32 s40, %[rot], 4096
s_and_b32 s40, s40, 28672
s_add_u32 s40, s40, 917504
buffer_load_dwordx4 v0, %[rk], s40 offen nt lds
s_waitcnt vmcnt(7)
s_add_u32 m0, s53, 34816
s_add_u32 s40, %[rot], 8192
s_and_b32 s40, s40, 28672
s_add_u32 s40, s40, 917504
buffer_load_dwordx4 v0, %[rk], s40 offen nt lds
s_waitcnt vmcnt(7)
s_add_u32 m0, s53, 35840
s_add_u32 s40, %[rot], 12288
s_and_b32 s40, s40, 28672
s_add_u32 s40, s40, 917504
buffer_load_dwordx4 v0, %[rk], s40 offen nt lds
s_waitcnt vmcnt(7)
Lpc15_%=:
s_memrealtime s[56:57]
s_waitcnt lgkmcnt(0)
s_sub_u32 s56, s56, s34
s_cmp_lt_u32 s56, 979
s_cbranch_scc1 Lpc15_%=
s_add_u32 m0, s53, 36864
s_add_u32 s40, %[rot], 16384
s_and_b32 s40, s40, 28672
s_add_u32 s40, s40, 917504
buffer_load_dwordx4 v0, %[rk], s40 offen nt lds
s_waitcnt vmcnt(7)
s_add_u32 m0, s53, 37888
s_add_u32 s40, %[rot], 20480
s_and_b32 s40, s40, 28672
s_add_u32 s40, s40, 917504
buffer_load_dwordx4 v0, %[rk], s40 offen nt lds
v_xor_b32 v4, s58, v4
v_xor_b32 v5, s59, v5
v_or_b32 v4, v4, v5
v_cmp_ne_u32 vcc, 0, v4
s_cbranch_vccnz Lslow_cf_%=
Lback_cf_%=:
s_add_u32 s41, %[qo], 0
buffer_load_dwordx4 v[4:7], v39, %[rw], s41 offen sc1
s_waitcnt vmcnt(8)
s_add_u32 m0, s53, 38912
s_add_u32 s40, %[rot], 24576
s_and_b32 s40, s40, 28672
s_add_u32 s40, s40, 917504
buffer_load_dwordx4 v0, %[rk], s40 offen nt lds
s_waitcnt vmcnt(8)
s_add_u32 s40, %[rot], 28672
s_and_b32 s40, s40, 28672
s_add_u32 s40, s40, 917504
buffer_load_dwordx4 v[168:171], v0, %[rk], s40 offen nt
s_waitcnt vmcnt(8)
Lpc16_%=:
s_memrealtime s[56:57]
s_waitcnt lgkmcnt(0)
s_sub_u32 s56, s56, s34
s_cmp_lt_u32 s56, 1036
s_cbranch_scc1 Lpc16_%=
s_add_u32 s40, %[rot], 0
s_and_b32 s40, s40, 28672
s_add_u32 s40, s40, 1146880
buffer_load_dwordx4 v[172:175], v0, %[rk], s40 offen nt
s_waitcnt vmcnt(8)
s_add_u32 s40, %[rot], 0
s_and_b32 s40, s40, 28672
s_add_u32 s40, s40, 917504
buffer_load_dwordx4 a[128:131], v0, %[rv], s40 offen nt
s_waitcnt vmcnt(8)
s_add_u32 s40, %[rot], 4096
s_and_b32 s40, s40, 28672
s_add_u32 s40, s40, 917504
buffer_load_dwordx4 a[132:135], v0, %[rv], s40 offen nt
s_waitcnt vmcnt(8)
s_add_u32 s40, %[rot], 8192
s_and_b32 s40, s40, 28672
s_add_u32 s40, s40, 917504
buffer_load_dwordx4 a[136:139], v0, %[rv], s40 offen nt
s_waitcnt vmcnt(8)
Lpc17_%=:
s_memrealtime s[56:57]
s_waitcnt lgkmcnt(0)
s_sub_u32 s56, s56, s34
s_cmp_lt_u32 s56, 1094
s_cbranch_scc1 Lpc17_%=
s_add_u32 s40, %[rot], 12288
s_and_b32 s40, s40, 28672
s_add_u32 s40, s40, 917504
buffer_load_dwordx4 a[140:143], v0, %[rv], s40 offen nt
s_waitcnt vmcnt(8)
s_add_u32 s40, %[rot], 16384
s_and_b32 s40, s40, 28672
s_add_u32 s40, s40, 917504
buffer_load_dwordx4 a[144:147], v0, %[rv], s40 offen nt
s_waitcnt vmcnt(8)
v_max3_u32 v27, v4, v5, v6
v_max_u32 v27, v27, v7
v_cmp_eq_u32 vcc, -1, v27
s_cbranch_vccnz Lslow_q1b0_%=
Lback_q1b0_%=:
ds_read_b128 v[212:215], v1 offset:0
ds_read_b128 v[216:219], v1 offset:1024
ds_read_b128 v[220:223], v1 offset:2048
ds_read_b128 v[224:227], v1 offset:3072
ds_read_b128 v[228:231], v1 offset:4096
ds_read_b128 v[232:235], v1 offset:5120
s_waitcnt lgkmcnt(5)
v_dot2_f32_bf16 v31, v212, v4, 0
v_dot2_f32_bf16 v31, v213, v5, v31
v_dot2_f32_bf16 v31, v214, v6, v31
v_dot2_f32_bf16 v31, v215, v7, v31
s_waitcnt lgkmcnt(4)
v_dot2_f32_bf16 v32, v216, v4, 0
v_dot2_f32_bf16 v32, v217, v5, v32
v_dot2_f32_bf16 v32, v218, v6, v32
v_dot2_f32_bf16 v32, v219, v7, v32
s_waitcnt lgkmcnt(3)
v_dot2_f32_bf16 v33, v220, v4, 0
v_dot2_f32_bf16 v33, v221, v5, v33
v_dot2_f32_bf16 v33, v222, v6, v33
v_dot2_f32_bf16 v33, v223, v7, v33
v_add_f32_dpp v31, v31, v31 row_ror:8 row_mask:0xf bank_mask:0x3
s_waitcnt lgkmcnt(2)
v_dot2_f32_bf16 v34, v224, v4, 0
v_dot2_f32_bf16 v34, v225, v5, v34
v_dot2_f32_bf16 v34, v226, v6, v34
v_dot2_f32_bf16 v34, v227, v7, v34
v_add_f32_dpp v31, v32, v32 row_ror:8 row_mask:0xf bank_mask:0xc
s_waitcnt lgkmcnt(1)
v_dot2_f32_bf16 v35, v228, v4, 0
v_dot2_f32_bf16 v35, v229, v5, v35
v_dot2_f32_bf16 v35, v230, v6, v35
v_dot2_f32_bf16 v35, v231, v7, v35
v_add_f32_dpp v33, v33, v33 row_ror:8 row_mask:0xf bank_mask:0x3
s_waitcnt lgkmcnt(0)
v_dot2_f32_bf16 v36, v232, v4, 0
v_dot2_f32_bf16 v36, v233, v5, v36
v_dot2_f32_bf16 v36, v234, v6, v36
v_dot2_f32_bf16 v36, v235, v7, v36
v_add_f32_dpp v33, v34, v34 row_ror:8 row_mask:0xf bank_mask:0xc
v_add_f32_dpp v31, v31, v31 row_shl:4 row_mask:0xf bank_mask:0x5
s_waitcnt vmcnt(7)
s_add_u32 s40, %[rot], 20480
s_and_b32 s40, s40, 28672
s_add_u32 s40, s40, 917504
buffer_load_dwordx4 a[148:151], v0, %[rv], s40 offen nt
ds_read_b128 v[212:215], v1 offset:6144
ds_read_b128 v[216:219], v1 offset:7168
ds_read_b128 v[220:223], v1 offset:8192
ds_read_b128 v[224:227], v1 offset:9216
ds_read_b128 v[228:231], v1 offset:10240
ds_read_b128 v[232:235], v1 offset:11264
s_waitcnt lgkmcnt(5)
v_dot2_f32_bf16 v37, v212, v4, 0
v_dot2_f32_bf16 v37, v213, v5, v37
v_dot2_f32_bf16 v37, v214, v6, v37
v_dot2_f32_bf16 v37, v215, v7, v37
v_add_f32_dpp v31, v33, v33 row_shr:4 row_mask:0xf bank_mask:0xa
v_add_f32_dpp v35, v35, v35 row_ror:8 row_mask:0xf bank_mask:0x3
s_waitcnt lgkmcnt(4)
v_dot2_f32_bf16 v32, v216, v4, 0
v_dot2_f32_bf16 v32, v217, v5, v32
v_dot2_f32_bf16 v32, v218, v6, v32
v_dot2_f32_bf16 v32, v219, v7, v32
v_add_f32_dpp v35, v36, v36 row_ror:8 row_mask:0xf bank_mask:0xc
s_waitcnt lgkmcnt(3)
v_dot2_f32_bf16 v34, v220, v4, 0
v_dot2_f32_bf16 v34, v221, v5, v34
v_dot2_f32_bf16 v34, v222, v6, v34
v_dot2_f32_bf16 v34, v223, v7, v34
v_add_f32_dpp v37, v37, v37 row_ror:8 row_mask:0xf bank_mask:0x3
s_waitcnt lgkmcnt(2)
v_dot2_f32_bf16 v33, v224, v4, 0
v_dot2_f32_bf16 v33, v225, v5, v33
v_dot2_f32_bf16 v33, v226, v6, v33
v_dot2_f32_bf16 v33, v227, v7, v33
v_add_f32_dpp v37, v32, v32 row_ror:8 row_mask:0xf bank_mask:0xc
v_add_f32_dpp v35, v35, v35 row_shl:4 row_mask:0xf bank_mask:0x5
s_waitcnt lgkmcnt(1)
v_dot2_f32_bf16 v36, v228, v4, 0
v_dot2_f32_bf16 v36, v229, v5, v36
v_dot2_f32_bf16 v36, v230, v6, v36
v_dot2_f32_bf16 v36, v231, v7, v36
v_add_f32_dpp v35, v37, v37 row_shr:4 row_mask:0xf bank_mask:0xa
v_cndmask_b32 v30, v35, v31, s[44:45]
v_cndmask_b32 v31, v31, v35, s[44:45]
s_waitcnt lgkmcnt(0)
v_dot2_f32_bf16 v32, v232, v4, 0
v_dot2_f32_bf16 v32, v233, v5, v32
v_dot2_f32_bf16 v32, v234, v6, v32
v_dot2_f32_bf16 v32, v235, v7, v32
v_add_f32_dpp v31, v30, v31 quad_perm:[2,3,0,1] row_mask:0xf bank_mask:0xf
v_add_f32_dpp v34, v34, v34 row_ror:8 row_mask:0xf bank_mask:0x3
s_waitcnt vmcnt(7)
s_add_u32 s40, %[rot], 24576
s_and_b32 s40, s40, 28672
s_add_u32 s40, s40, 917504
buffer_load_dwordx4 a[152:155], v0, %[rv], s40 offen nt
ds_read_b128 v[212:215], v1 offset:12288
ds_read_b128 v[216:219], v1 offset:13312
ds_read_b128 v[220:223], v1 offset:14336
ds_read_b128 v[224:227], v1 offset:15360
s_waitcnt lgkmcnt(3)
v_dot2_f32_bf16 v37, v212, v4, 0
v_dot2_f32_bf16 v37, v213, v5, v37
v_dot2_f32_bf16 v37, v214, v6, v37
v_dot2_f32_bf16 v37, v215, v7, v37
v_add_f32_dpp v34, v33, v33 row_ror:8 row_mask:0xf bank_mask:0xc
v_add_f32_dpp v36, v36, v36 row_ror:8 row_mask:0xf bank_mask:0x3
s_waitcnt lgkmcnt(2)
v_dot2_f32_bf16 v35, v216, v4, 0
v_dot2_f32_bf16 v35, v217, v5, v35
v_dot2_f32_bf16 v35, v218, v6, v35
v_dot2_f32_bf16 v35, v219, v7, v35
v_add_f32_dpp v36, v32, v32 row_ror:8 row_mask:0xf bank_mask:0xc
v_add_f32_dpp v34, v34, v34 row_shl:4 row_mask:0xf bank_mask:0x5
s_waitcnt lgkmcnt(1)
v_dot2_f32_bf16 v33, v220, v4, 0
v_dot2_f32_bf16 v33, v221, v5, v33
v_dot2_f32_bf16 v33, v222, v6, v33
v_dot2_f32_bf16 v33, v223, v7, v33
v_add_f32_dpp v34, v36, v36 row_shr:4 row_mask:0xf bank_mask:0xa
v_add_f32_dpp v37, v37, v37 row_ror:8 row_mask:0xf bank_mask:0x3
s_waitcnt lgkmcnt(0)
v_dot2_f32_bf16 v32, v224, v4, 0
v_dot2_f32_bf16 v32, v225, v5, v32
v_dot2_f32_bf16 v32, v226, v6, v32
v_dot2_f32_bf16 v32, v227, v7, v32
v_add_f32_dpp v37, v35, v35 row_ror:8 row_mask:0xf bank_mask:0xc
v_add_f32_dpp v33, v33, v33 row_ror:8 row_mask:0xf bank_mask:0x3
s_nop 1
v_add_f32_dpp v33, v32, v32 row_ror:8 row_mask:0xf bank_mask:0xc
v_add_f32_dpp v37, v37, v37 row_shl:4 row_mask:0xf bank_mask:0x5
s_nop 1
v_add_f32_dpp v37, v33, v33 row_shr:4 row_mask:0xf bank_mask:0xa
v_cndmask_b32 v30, v37, v34, s[44:45]
v_cndmask_b32 v34, v34, v37, s[44:45]
s_nop 1
v_add_f32_dpp v34, v30, v34 quad_perm:[2,3,0,1] row_mask:0xf bank_mask:0xf
v_cndmask_b32 v30, v34, v31, s[46:47]
v_cndmask_b32 v31, v31, v34, s[46:47]
s_nop 1
v_add_f32_dpp v26, v30, v31 quad_perm:[1,0,3,2] row_mask:0xf bank_mask:0xf
v_cvt_pk_bf16_f32 v27, v26, v26
v_and_b32 v26, 0xffff0000, v27
v_mul_f32 v26, s55, v26
v_cvt_pk_bf16_f32 v27, v26, v26
v_and_b32 v26, 0xffff0000, v27
s_nop 1
v_max_f32_dpp v29, v26, v26 row_ror:8 row_mask:0xf bank_mask:0xf
s_nop 1
v_max_f32_dpp v29, v29, v29 row_ror:4 row_mask:0xf bank_mask:0xf
s_nop 1
v_max_f32_dpp v29, v29, v29 row_ror:2 row_mask:0xf bank_mask:0xf
s_nop 1
v_max_f32_dpp v29, v29, v29 row_ror:1 row_mask:0xf bank_mask:0xf
s_nop 1
v_max_f32_dpp v29, v29, v29 row_bcast:15 row_mask:0xa bank_mask:0xf
s_nop 1
v_max_f32_dpp v29, v29, v29 row_bcast:31 row_mask:0xc bank_mask:0xf
s_nop 0
v_readlane_b32 s50, v29, 63
v_mov_b32 v27, s54
s_nop 0
v_mul_f32 v20, s50, v27
v_fma_f32 v26, v26, s54, -v20
v_exp_f32 v26, v26
s_nop 0
v_cvt_pk_bf16_f32 v28, v26, v26
v_mov_b32 v21, v26
s_nop 0
v_and_b32_dpp v22, v28, v2 quad_perm:[0,0,0,0] row_mask:0xf bank_mask:0xf
v_and_b32_dpp v23, v28, v3 quad_perm:[0,0,0,0] row_mask:0xf bank_mask:0xf
s_nop 1
v_mfma_f32_4x4x4_16b_bf16 v[12:15], v[22:23], a[0:1], 0 cbsz:2 abid:0
v_mfma_f32_4x4x4_16b_bf16 v[16:19], v[22:23], a[2:3], 0 cbsz:2 abid:0
v_and_b32_dpp v24, v28, v2 quad_perm:[2,2,2,2] row_mask:0xf bank_mask:0xf
v_and_b32_dpp v25, v28, v3 quad_perm:[2,2,2,2] row_mask:0xf bank_mask:0xf
v_mfma_f32_4x4x4_16b_bf16 v[12:15], v[22:23], a[4:5], v[12:15] cbsz:2 abid:2
v_mfma_f32_4x4x4_16b_bf16 v[16:19], v[22:23], a[6:7], v[16:19] cbsz:2 abid:2
Lpc18_%=:
s_memrealtime s[56:57]
s_waitcnt lgkmcnt(0)
s_sub_u32 s56, s56, s34
s_cmp_lt_u32 s56, 1152
s_cbranch_scc1 Lpc18_%=
s_add_u32 s40, %[rot], 28672
s_and_b32 s40, s40, 28672
)ASM"
R"ASM(s_add_u32 s40, s40, 917504
buffer_load_dwordx4 v[204:207], v0, %[rv], s40 offen nt
v_mfma_f32_4x4x4_16b_bf16 v[12:15], v[22:23], a[8:9], v[12:15] cbsz:2 abid:1
v_mfma_f32_4x4x4_16b_bf16 v[16:19], v[22:23], a[10:11], v[16:19] cbsz:2 abid:1
s_nop 0
v_mfma_f32_4x4x4_16b_bf16 v[12:15], v[22:23], a[12:13], v[12:15] cbsz:2 abid:3
v_mfma_f32_4x4x4_16b_bf16 v[16:19], v[22:23], a[14:15], v[16:19] cbsz:2 abid:3
s_nop 0
v_mfma_f32_4x4x4_16b_bf16 v[12:15], v[24:25], a[16:17], v[12:15] cbsz:2 abid:0
v_mfma_f32_4x4x4_16b_bf16 v[16:19], v[24:25], a[18:19], v[16:19] cbsz:2 abid:0
v_and_b32_dpp v22, v28, v2 quad_perm:[1,1,1,1] row_mask:0xf bank_mask:0xf
v_and_b32_dpp v23, v28, v3 quad_perm:[1,1,1,1] row_mask:0xf bank_mask:0xf
v_mfma_f32_4x4x4_16b_bf16 v[12:15], v[24:25], a[20:21], v[12:15] cbsz:2 abid:2
v_mfma_f32_4x4x4_16b_bf16 v[16:19], v[24:25], a[22:23], v[16:19] cbsz:2 abid:2
s_nop 0
v_mfma_f32_4x4x4_16b_bf16 v[12:15], v[24:25], a[24:25], v[12:15] cbsz:2 abid:1
v_mfma_f32_4x4x4_16b_bf16 v[16:19], v[24:25], a[26:27], v[16:19] cbsz:2 abid:1
s_nop 0
v_mfma_f32_4x4x4_16b_bf16 v[12:15], v[24:25], a[28:29], v[12:15] cbsz:2 abid:3
v_mfma_f32_4x4x4_16b_bf16 v[16:19], v[24:25], a[30:31], v[16:19] cbsz:2 abid:3
s_add_u32 s40, %[rot], 0
s_and_b32 s40, s40, 28672
s_add_u32 s40, s40, 1146880
buffer_load_dwordx4 v[208:211], v0, %[rv], s40 offen nt
v_mfma_f32_4x4x4_16b_bf16 v[12:15], v[22:23], a[32:33], v[12:15] cbsz:2 abid:0
v_mfma_f32_4x4x4_16b_bf16 v[16:19], v[22:23], a[34:35], v[16:19] cbsz:2 abid:0
v_and_b32_dpp v24, v28, v2 quad_perm:[3,3,3,3] row_mask:0xf bank_mask:0xf
v_and_b32_dpp v25, v28, v3 quad_perm:[3,3,3,3] row_mask:0xf bank_mask:0xf
v_mfma_f32_4x4x4_16b_bf16 v[12:15], v[22:23], a[36:37], v[12:15] cbsz:2 abid:2
v_mfma_f32_4x4x4_16b_bf16 v[16:19], v[22:23], a[38:39], v[16:19] cbsz:2 abid:2
s_nop 0
v_mfma_f32_4x4x4_16b_bf16 v[12:15], v[22:23], a[40:41], v[12:15] cbsz:2 abid:1
v_mfma_f32_4x4x4_16b_bf16 v[16:19], v[22:23], a[42:43], v[16:19] cbsz:2 abid:1
s_nop 0
v_mfma_f32_4x4x4_16b_bf16 v[12:15], v[22:23], a[44:45], v[12:15] cbsz:2 abid:3
v_mfma_f32_4x4x4_16b_bf16 v[16:19], v[22:23], a[46:47], v[16:19] cbsz:2 abid:3
s_nop 0
v_mfma_f32_4x4x4_16b_bf16 v[12:15], v[24:25], a[48:49], v[12:15] cbsz:2 abid:0
v_mfma_f32_4x4x4_16b_bf16 v[16:19], v[24:25], a[50:51], v[16:19] cbsz:2 abid:0
s_nop 0
v_mfma_f32_4x4x4_16b_bf16 v[12:15], v[24:25], a[52:53], v[12:15] cbsz:2 abid:2
v_mfma_f32_4x4x4_16b_bf16 v[16:19], v[24:25], a[54:55], v[16:19] cbsz:2 abid:2
s_waitcnt vmcnt(9)
s_add_u32 s40, %[rot], 4096
s_and_b32 s40, s40, 28672
s_add_u32 s40, s40, 1146880
buffer_load_dwordx4 v[40:43], v0, %[rk], s40 offen nt
ds_read_b128 v[212:215], v1 offset:16384
ds_read_b128 v[216:219], v1 offset:17408
ds_read_b128 v[220:223], v1 offset:18432
ds_read_b128 v[224:227], v1 offset:19456
v_mfma_f32_4x4x4_16b_bf16 v[12:15], v[24:25], a[56:57], v[12:15] cbsz:2 abid:1
v_mfma_f32_4x4x4_16b_bf16 v[16:19], v[24:25], a[58:59], v[16:19] cbsz:2 abid:1
s_nop 0
v_mfma_f32_4x4x4_16b_bf16 v[12:15], v[24:25], a[60:61], v[12:15] cbsz:2 abid:3
v_mfma_f32_4x4x4_16b_bf16 v[16:19], v[24:25], a[62:63], v[16:19] cbsz:2 abid:3
s_waitcnt lgkmcnt(3)
v_dot2_f32_bf16 v31, v212, v4, 0
v_dot2_f32_bf16 v31, v213, v5, v31
v_dot2_f32_bf16 v31, v214, v6, v31
v_dot2_f32_bf16 v31, v215, v7, v31
s_waitcnt lgkmcnt(2)
v_dot2_f32_bf16 v32, v216, v4, 0
v_dot2_f32_bf16 v32, v217, v5, v32
v_dot2_f32_bf16 v32, v218, v6, v32
v_dot2_f32_bf16 v32, v219, v7, v32
s_waitcnt lgkmcnt(1)
v_dot2_f32_bf16 v33, v220, v4, 0
v_dot2_f32_bf16 v33, v221, v5, v33
v_dot2_f32_bf16 v33, v222, v6, v33
v_dot2_f32_bf16 v33, v223, v7, v33
v_add_f32_dpp v31, v31, v31 row_ror:8 row_mask:0xf bank_mask:0x3
s_waitcnt lgkmcnt(0)
v_dot2_f32_bf16 v34, v224, v4, 0
v_dot2_f32_bf16 v34, v225, v5, v34
v_dot2_f32_bf16 v34, v226, v6, v34
v_dot2_f32_bf16 v34, v227, v7, v34
v_add_f32_dpp v31, v32, v32 row_ror:8 row_mask:0xf bank_mask:0xc
s_waitcnt vmcnt(9)
s_add_u32 s40, %[rot], 8192
s_and_b32 s40, s40, 28672
s_add_u32 s40, s40, 1146880
buffer_load_dwordx4 v[44:47], v0, %[rk], s40 offen nt
ds_read_b128 v[212:215], v1 offset:20480
ds_read_b128 v[216:219], v1 offset:21504
ds_read_b128 v[220:223], v1 offset:22528
ds_read_b128 v[224:227], v1 offset:23552
ds_read_b128 v[228:231], v1 offset:24576
ds_read_b128 v[232:235], v1 offset:25600
s_waitcnt lgkmcnt(5)
v_dot2_f32_bf16 v35, v212, v4, 0
v_dot2_f32_bf16 v35, v213, v5, v35
v_dot2_f32_bf16 v35, v214, v6, v35
v_dot2_f32_bf16 v35, v215, v7, v35
v_add_f32_dpp v33, v33, v33 row_ror:8 row_mask:0xf bank_mask:0x3
s_waitcnt lgkmcnt(4)
v_dot2_f32_bf16 v36, v216, v4, 0
v_dot2_f32_bf16 v36, v217, v5, v36
v_dot2_f32_bf16 v36, v218, v6, v36
v_dot2_f32_bf16 v36, v219, v7, v36
v_add_f32_dpp v33, v34, v34 row_ror:8 row_mask:0xf bank_mask:0xc
v_add_f32_dpp v31, v31, v31 row_shl:4 row_mask:0xf bank_mask:0x5
s_waitcnt lgkmcnt(3)
v_dot2_f32_bf16 v37, v220, v4, 0
v_dot2_f32_bf16 v37, v221, v5, v37
v_dot2_f32_bf16 v37, v222, v6, v37
v_dot2_f32_bf16 v37, v223, v7, v37
v_add_f32_dpp v31, v33, v33 row_shr:4 row_mask:0xf bank_mask:0xa
v_add_f32_dpp v35, v35, v35 row_ror:8 row_mask:0xf bank_mask:0x3
s_waitcnt lgkmcnt(2)
v_dot2_f32_bf16 v32, v224, v4, 0
v_dot2_f32_bf16 v32, v225, v5, v32
v_dot2_f32_bf16 v32, v226, v6, v32
v_dot2_f32_bf16 v32, v227, v7, v32
v_add_f32_dpp v35, v36, v36 row_ror:8 row_mask:0xf bank_mask:0xc
s_waitcnt lgkmcnt(1)
v_dot2_f32_bf16 v34, v228, v4, 0
v_dot2_f32_bf16 v34, v229, v5, v34
v_dot2_f32_bf16 v34, v230, v6, v34
v_dot2_f32_bf16 v34, v231, v7, v34
v_add_f32_dpp v37, v37, v37 row_ror:8 row_mask:0xf bank_mask:0x3
s_waitcnt lgkmcnt(0)
v_dot2_f32_bf16 v33, v232, v4, 0
v_dot2_f32_bf16 v33, v233, v5, v33
v_dot2_f32_bf16 v33, v234, v6, v33
v_dot2_f32_bf16 v33, v235, v7, v33
v_add_f32_dpp v37, v32, v32 row_ror:8 row_mask:0xf bank_mask:0xc
v_add_f32_dpp v35, v35, v35 row_shl:4 row_mask:0xf bank_mask:0x5
s_waitcnt vmcnt(9)
Lpc19_%=:
s_memrealtime s[56:57]
s_waitcnt lgkmcnt(0)
s_sub_u32 s56, s56, s34
s_cmp_lt_u32 s56, 1209
s_cbranch_scc1 Lpc19_%=
s_add_u32 s40, %[rot], 12288
s_and_b32 s40, s40, 28672
s_add_u32 s40, s40, 1146880
buffer_load_dwordx4 v[48:51], v0, %[rk], s40 offen nt
ds_read_b128 v[212:215], v1 offset:26624
ds_read_b128 v[216:219], v1 offset:27648
ds_read_b128 v[220:223], v1 offset:28672
ds_read_b128 v[224:227], v1 offset:29696
ds_read_b128 v[228:231], v1 offset:30720
ds_read_b128 v[232:235], v1 offset:31744
s_waitcnt lgkmcnt(5)
v_dot2_f32_bf16 v36, v212, v4, 0
v_dot2_f32_bf16 v36, v213, v5, v36
v_dot2_f32_bf16 v36, v214, v6, v36
v_dot2_f32_bf16 v36, v215, v7, v36
v_add_f32_dpp v35, v37, v37 row_shr:4 row_mask:0xf bank_mask:0xa
v_cndmask_b32 v30, v35, v31, s[44:45]
v_cndmask_b32 v31, v31, v35, s[44:45]
s_waitcnt lgkmcnt(4)
v_dot2_f32_bf16 v32, v216, v4, 0
v_dot2_f32_bf16 v32, v217, v5, v32
v_dot2_f32_bf16 v32, v218, v6, v32
v_dot2_f32_bf16 v32, v219, v7, v32
v_add_f32_dpp v31, v30, v31 quad_perm:[2,3,0,1] row_mask:0xf bank_mask:0xf
v_add_f32_dpp v34, v34, v34 row_ror:8 row_mask:0xf bank_mask:0x3
s_waitcnt lgkmcnt(3)
v_dot2_f32_bf16 v37, v220, v4, 0
v_dot2_f32_bf16 v37, v221, v5, v37
v_dot2_f32_bf16 v37, v222, v6, v37
v_dot2_f32_bf16 v37, v223, v7, v37
v_add_f32_dpp v34, v33, v33 row_ror:8 row_mask:0xf bank_mask:0xc
v_add_f32_dpp v36, v36, v36 row_ror:8 row_mask:0xf bank_mask:0x3
s_waitcnt lgkmcnt(2)
v_dot2_f32_bf16 v35, v224, v4, 0
v_dot2_f32_bf16 v35, v225, v5, v35
v_dot2_f32_bf16 v35, v226, v6, v35
v_dot2_f32_bf16 v35, v227, v7, v35
v_add_f32_dpp v36, v32, v32 row_ror:8 row_mask:0xf bank_mask:0xc
v_add_f32_dpp v34, v34, v34 row_shl:4 row_mask:0xf bank_mask:0x5
s_waitcnt lgkmcnt(1)
v_dot2_f32_bf16 v33, v228, v4, 0
v_dot2_f32_bf16 v33, v229, v5, v33
v_dot2_f32_bf16 v33, v230, v6, v33
v_dot2_f32_bf16 v33, v231, v7, v33
v_add_f32_dpp v34, v36, v36 row_shr:4 row_mask:0xf bank_mask:0xa
v_add_f32_dpp v37, v37, v37 row_ror:8 row_mask:0xf bank_mask:0x3
s_waitcnt lgkmcnt(0)
v_dot2_f32_bf16 v32, v232, v4, 0
v_dot2_f32_bf16 v32, v233, v5, v32
v_dot2_f32_bf16 v32, v234, v6, v32
v_dot2_f32_bf16 v32, v235, v7, v32
v_add_f32_dpp v37, v35, v35 row_ror:8 row_mask:0xf bank_mask:0xc
v_add_f32_dpp v33, v33, v33 row_ror:8 row_mask:0xf bank_mask:0x3
s_nop 1
v_add_f32_dpp v33, v32, v32 row_ror:8 row_mask:0xf bank_mask:0xc
v_add_f32_dpp v37, v37, v37 row_shl:4 row_mask:0xf bank_mask:0x5
s_nop 1
v_add_f32_dpp v37, v33, v33 row_shr:4 row_mask:0xf bank_mask:0xa
v_cndmask_b32 v30, v37, v34, s[44:45]
v_cndmask_b32 v34, v34, v37, s[44:45]
s_nop 1
v_add_f32_dpp v34, v30, v34 quad_perm:[2,3,0,1] row_mask:0xf bank_mask:0xf
v_cndmask_b32 v30, v34, v31, s[46:47]
v_cndmask_b32 v31, v31, v34, s[46:47]
s_nop 1
v_add_f32_dpp v26, v30, v31 quad_perm:[1,0,3,2] row_mask:0xf bank_mask:0xf
v_cvt_pk_bf16_f32 v27, v26, v26
v_and_b32 v26, 0xffff0000, v27
v_mul_f32 v26, s55, v26
v_cvt_pk_bf16_f32 v27, v26, v26
v_and_b32 v26, 0xffff0000, v27
s_nop 1
v_max_f32_dpp v29, v26, v26 row_ror:8 row_mask:0xf bank_mask:0xf
s_nop 1
v_max_f32_dpp v29, v29, v29 row_ror:4 row_mask:0xf bank_mask:0xf
s_nop 1
v_max_f32_dpp v29, v29, v29 row_ror:2 row_mask:0xf bank_mask:0xf
s_nop 1
v_max_f32_dpp v29, v29, v29 row_ror:1 row_mask:0xf bank_mask:0xf
s_nop 1
v_max_f32_dpp v29, v29, v29 row_bcast:15 row_mask:0xa bank_mask:0xf
s_nop 1
v_max_f32_dpp v29, v29, v29 row_bcast:31 row_mask:0xc bank_mask:0xf
s_nop 0
v_readlane_b32 s50, v29, 63
v_mov_b32 v27, s54
s_nop 0
v_mul_f32 v27, s50, v27
v_max_f32 v30, v27, v20
v_sub_f32 v29, v20, v30
v_mov_b32 v20, v30
v_exp_f32 v29, v29
v_fma_f32 v26, v26, s54, -v20
v_exp_f32 v26, v26
v_mul_f32 v12, v29, v12
v_mul_f32 v13, v29, v13
v_mul_f32 v14, v29, v14
v_mul_f32 v15, v29, v15
v_mul_f32 v16, v29, v16
v_mul_f32 v17, v29, v17
v_mul_f32 v18, v29, v18
v_mul_f32 v19, v29, v19
v_cvt_pk_bf16_f32 v28, v26, v26
v_fma_f32 v21, v21, v29, v26
s_nop 0
v_and_b32_dpp v22, v28, v2 quad_perm:[0,0,0,0] row_mask:0xf bank_mask:0xf
v_and_b32_dpp v23, v28, v3 quad_perm:[0,0,0,0] row_mask:0xf bank_mask:0xf
s_waitcnt vmcnt(9)
s_add_u32 s40, %[rot], 16384
s_and_b32 s40, s40, 28672
s_add_u32 s40, s40, 1146880
buffer_load_dwordx4 v[52:55], v0, %[rk], s40 offen nt
v_mfma_f32_4x4x4_16b_bf16 v[12:15], v[22:23], a[64:65], v[12:15] cbsz:2 abid:0
v_mfma_f32_4x4x4_16b_bf16 v[16:19], v[22:23], a[66:67], v[16:19] cbsz:2 abid:0
v_and_b32_dpp v24, v28, v2 quad_perm:[2,2,2,2] row_mask:0xf bank_mask:0xf
v_and_b32_dpp v25, v28, v3 quad_perm:[2,2,2,2] row_mask:0xf bank_mask:0xf
v_mfma_f32_4x4x4_16b_bf16 v[12:15], v[22:23], a[68:69], v[12:15] cbsz:2 abid:2
v_mfma_f32_4x4x4_16b_bf16 v[16:19], v[22:23], a[70:71], v[16:19] cbsz:2 abid:2
s_nop 0
v_mfma_f32_4x4x4_16b_bf16 v[12:15], v[22:23], a[72:73], v[12:15] cbsz:2 abid:1
v_mfma_f32_4x4x4_16b_bf16 v[16:19], v[22:23], a[74:75], v[16:19] cbsz:2 abid:1
s_nop 0
v_mfma_f32_4x4x4_16b_bf16 v[12:15], v[22:23], a[76:77], v[12:15] cbsz:2 abid:3
v_mfma_f32_4x4x4_16b_bf16 v[16:19], v[22:23], a[78:79], v[16:19] cbsz:2 abid:3
s_nop 0
v_mfma_f32_4x4x4_16b_bf16 v[12:15], v[24:25], a[80:81], v[12:15] cbsz:2 abid:0
v_mfma_f32_4x4x4_16b_bf16 v[16:19], v[24:25], a[82:83], v[16:19] cbsz:2 abid:0
v_and_b32_dpp v22, v28, v2 quad_perm:[1,1,1,1] row_mask:0xf bank_mask:0xf
v_and_b32_dpp v23, v28, v3 quad_perm:[1,1,1,1] row_mask:0xf bank_mask:0xf
v_mfma_f32_4x4x4_16b_bf16 v[12:15], v[24:25], a[84:85], v[12:15] cbsz:2 abid:2
v_mfma_f32_4x4x4_16b_bf16 v[16:19], v[24:25], a[86:87], v[16:19] cbsz:2 abid:2
s_waitcnt vmcnt(9)
s_add_u32 s40, %[rot], 20480
s_and_b32 s40, s40, 28672
s_add_u32 s40, s40, 1146880
buffer_load_dwordx4 v[56:59], v0, %[rk], s40 offen nt
v_mfma_f32_4x4x4_16b_bf16 v[12:15], v[24:25], a[88:89], v[12:15] cbsz:2 abid:1
v_mfma_f32_4x4x4_16b_bf16 v[16:19], v[24:25], a[90:91], v[16:19] cbsz:2 abid:1
s_nop 0
v_mfma_f32_4x4x4_16b_bf16 v[12:15], v[24:25], a[92:93], v[12:15] cbsz:2 abid:3
v_mfma_f32_4x4x4_16b_bf16 v[16:19], v[24:25], a[94:95], v[16:19] cbsz:2 abid:3
s_nop 0
v_mfma_f32_4x4x4_16b_bf16 v[12:15], v[22:23], a[96:97], v[12:15] cbsz:2 abid:0
v_mfma_f32_4x4x4_16b_bf16 v[16:19], v[22:23], a[98:99], v[16:19] cbsz:2 abid:0
v_and_b32_dpp v24, v28, v2 quad_perm:[3,3,3,3] row_mask:0xf bank_mask:0xf
v_and_b32_dpp v25, v28, v3 quad_perm:[3,3,3,3] row_mask:0xf bank_mask:0xf
v_mfma_f32_4x4x4_16b_bf16 v[12:15], v[22:23], a[100:101], v[12:15] cbsz:2 abid:2
v_mfma_f32_4x4x4_16b_bf16 v[16:19], v[22:23], a[102:103], v[16:19] cbsz:2 abid:2
s_nop 0
v_mfma_f32_4x4x4_16b_bf16 v[12:15], v[22:23], a[104:105], v[12:15] cbsz:2 abid:1
v_mfma_f32_4x4x4_16b_bf16 v[16:19], v[22:23], a[106:107], v[16:19] cbsz:2 abid:1
s_nop 0
v_mfma_f32_4x4x4_16b_bf16 v[12:15], v[22:23], a[108:109], v[12:15] cbsz:2 abid:3
v_mfma_f32_4x4x4_16b_bf16 v[16:19], v[22:23], a[110:111], v[16:19] cbsz:2 abid:3
s_waitcnt vmcnt(9)
s_add_u32 s40, %[rot], 24576
s_and_b32 s40, s40, 28672
s_add_u32 s40, s40, 1146880
buffer_load_dwordx4 v[60:63], v0, %[rk], s40 offen nt
ds_read_b128 v[212:215], v1 offset:32768
ds_read_b128 v[216:219], v1 offset:33792
v_mfma_f32_4x4x4_16b_bf16 v[12:15], v[24:25], a[112:113], v[12:15] cbsz:2 abid:0
v_mfma_f32_4x4x4_16b_bf16 v[16:19], v[24:25], a[114:115], v[16:19] cbsz:2 abid:0
s_nop 0
v_mfma_f32_4x4x4_16b_bf16 v[12:15], v[24:25], a[116:117], v[12:15] cbsz:2 abid:2
v_mfma_f32_4x4x4_16b_bf16 v[16:19], v[24:25], a[118:119], v[16:19] cbsz:2 abid:2
s_nop 0
v_mfma_f32_4x4x4_16b_bf16 v[12:15], v[24:25], a[120:121], v[12:15] cbsz:2 abid:1
v_mfma_f32_4x4x4_16b_bf16 v[16:19], v[24:25], a[122:123], v[16:19] cbsz:2 abid:1
s_nop 0
v_mfma_f32_4x4x4_16b_bf16 v[12:15], v[24:25], a[124:125], v[12:15] cbsz:2 abid:3
v_mfma_f32_4x4x4_16b_bf16 v[16:19], v[24:25], a[126:127], v[16:19] cbsz:2 abid:3
s_waitcnt lgkmcnt(1)
v_dot2_f32_bf16 v31, v212, v4, 0
v_dot2_f32_bf16 v31, v213, v5, v31
v_dot2_f32_bf16 v31, v214, v6, v31
v_dot2_f32_bf16 v31, v215, v7, v31
s_waitcnt lgkmcnt(0)
v_dot2_f32_bf16 v32, v216, v4, 0
v_dot2_f32_bf16 v32, v217, v5, v32
v_dot2_f32_bf16 v32, v218, v6, v32
v_dot2_f32_bf16 v32, v219, v7, v32
s_waitcnt vmcnt(9)
Lpc20_%=:
s_memrealtime s[56:57]
s_waitcnt lgkmcnt(0)
s_sub_u32 s56, s56, s34
s_cmp_lt_u32 s56, 1267
s_cbranch_scc1 Lpc20_%=
s_add_u32 s40, %[rot], 28672
s_and_b32 s40, s40, 28672
s_add_u32 s40, s40, 1146880
buffer_load_dwordx4 v[64:67], v0, %[rk], s40 offen nt
ds_read_b128 v[212:215], v1 offset:34816
ds_read_b128 v[216:219], v1 offset:35840
ds_read_b128 v[220:223], v1 offset:36864
ds_read_b128 v[224:227], v1 offset:37888
ds_read_b128 v[228:231], v1 offset:38912
s_waitcnt lgkmcnt(4)
v_dot2_f32_bf16 v33, v212, v4, 0
v_dot2_f32_bf16 v33, v213, v5, v33
v_dot2_f32_bf16 v33, v214, v6, v33
v_dot2_f32_bf16 v33, v215, v7, v33
v_add_f32_dpp v31, v31, v31 row_ror:8 row_mask:0xf bank_mask:0x3
s_waitcnt lgkmcnt(3)
v_dot2_f32_bf16 v34, v216, v4, 0
v_dot2_f32_bf16 v34, v217, v5, v34
v_dot2_f32_bf16 v34, v218, v6, v34
v_dot2_f32_bf16 v34, v219, v7, v34
v_add_f32_dpp v31, v32, v32 row_ror:8 row_mask:0xf bank_mask:0xc
s_waitcnt lgkmcnt(2)
v_dot2_f32_bf16 v35, v220, v4, 0
v_dot2_f32_bf16 v35, v221, v5, v35
v_dot2_f32_bf16 v35, v222, v6, v35
v_dot2_f32_bf16 v35, v223, v7, v35
v_add_f32_dpp v33, v33, v33 row_ror:8 row_mask:0xf bank_mask:0x3
s_waitcnt lgkmcnt(1)
v_dot2_f32_bf16 v36, v224, v4, 0
v_dot2_f32_bf16 v36, v225, v5, v36
v_dot2_f32_bf16 v36, v226, v6, v36
v_dot2_f32_bf16 v36, v227, v7, v36
v_add_f32_dpp v33, v34, v34 row_ror:8 row_mask:0xf bank_mask:0xc
v_add_f32_dpp v31, v31, v31 row_shl:4 row_mask:0xf bank_mask:0x5
s_waitcnt lgkmcnt(0)
v_dot2_f32_bf16 v37, v228, v4, 0
v_dot2_f32_bf16 v37, v229, v5, v37
v_dot2_f32_bf16 v37, v230, v6, v37
v_dot2_f32_bf16 v37, v231, v7, v37
v_add_f32_dpp v31, v33, v33 row_shr:4 row_mask:0xf bank_mask:0xa
v_add_f32_dpp v35, v35, v35 row_ror:8 row_mask:0xf bank_mask:0x3
v_dot2_f32_bf16 v32, v168, v4, 0
v_dot2_f32_bf16 v32, v169, v5, v32
v_dot2_f32_bf16 v32, v170, v6, v32
v_dot2_f32_bf16 v32, v171, v7, v32
v_add_f32_dpp v35, v36, v36 row_ror:8 row_mask:0xf bank_mask:0xc
s_waitcnt vmcnt(9)
s_add_u32 s40, %[rot], 0
s_and_b32 s40, s40, 28672
s_add_u32 s40, s40, 1376256
buffer_load_dwordx4 v[68:71], v0, %[rk], s40 offen nt
v_dot2_f32_bf16 v34, v172, v4, 0
v_dot2_f32_bf16 v34, v173, v5, v34
v_dot2_f32_bf16 v34, v174, v6, v34
v_dot2_f32_bf16 v34, v175, v7, v34
v_add_f32_dpp v37, v37, v37 row_ror:8 row_mask:0xf bank_mask:0x3
s_nop 1
v_add_f32_dpp v37, v32, v32 row_ror:8 row_mask:0xf bank_mask:0xc
v_add_f32_dpp v35, v35, v35 row_shl:4 row_mask:0xf bank_mask:0x5
s_nop 1
v_add_f32_dpp v35, v37, v37 row_shr:4 row_mask:0xf bank_mask:0xa
v_cndmask_b32 v30, v35, v31, s[44:45]
v_cndmask_b32 v31, v31, v35, s[44:45]
s_nop 1
v_add_f32_dpp v31, v30, v31 quad_perm:[2,3,0,1] row_mask:0xf bank_mask:0xf
v_add_f32_dpp v34, v34, v34 row_ror:8 row_mask:0xf bank_mask:0x3
s_nop 1
v_add_f32_dpp v34, v34, v34 row_shl:4 row_mask:0xf bank_mask:0x5
s_nop 1
v_add_f32_dpp v34, v34, v34 quad_perm:[2,3,0,1] row_mask:0xf bank_mask:0xf
v_cndmask_b32 v30, v34, v31, s[46:47]
v_cndmask_b32 v31, v31, v34, s[46:47]
s_nop 1
v_add_f32_dpp v26, v30, v31 quad_perm:[1,0,3,2] row_mask:0xf bank_mask:0xf
v_cvt_pk_bf16_f32 v27, v26, v26
v_and_b32 v26, 0xffff0000, v27
)ASM"
R"ASM(v_mul_f32 v26, s55, v26
v_cvt_pk_bf16_f32 v27, v26, v26
v_and_b32 v26, 0xffff0000, v27
s_mov_b32 s48, 0x55575557
s_mov_b32 s49, 0x55575557
v_mov_b32 v27, 0xff800000
v_cndmask_b32 v26, v27, v26, s[48:49]
s_nop 1
v_max_f32_dpp v29, v26, v26 row_ror:8 row_mask:0xf bank_mask:0xf
s_nop 1
v_max_f32_dpp v29, v29, v29 row_ror:4 row_mask:0xf bank_mask:0xf
s_nop 1
v_max_f32_dpp v29, v29, v29 row_ror:2 row_mask:0xf bank_mask:0xf
s_nop 1
v_max_f32_dpp v29, v29, v29 row_ror:1 row_mask:0xf bank_mask:0xf
s_nop 1
v_max_f32_dpp v29, v29, v29 row_bcast:15 row_mask:0xa bank_mask:0xf
s_nop 1
v_max_f32_dpp v29, v29, v29 row_bcast:31 row_mask:0xc bank_mask:0xf
s_nop 0
v_readlane_b32 s50, v29, 63
v_mov_b32 v27, s54
s_nop 0
v_mul_f32 v27, s50, v27
v_max_f32 v30, v27, v20
v_sub_f32 v29, v20, v30
v_mov_b32 v20, v30
v_exp_f32 v29, v29
v_fma_f32 v26, v26, s54, -v20
v_exp_f32 v26, v26
v_mul_f32 v12, v29, v12
v_mul_f32 v13, v29, v13
v_mul_f32 v14, v29, v14
v_mul_f32 v15, v29, v15
v_mul_f32 v16, v29, v16
v_mul_f32 v17, v29, v17
v_mul_f32 v18, v29, v18
v_mul_f32 v19, v29, v19
v_cvt_pk_bf16_f32 v28, v26, v26
v_fma_f32 v21, v21, v29, v26
s_nop 0
v_and_b32_dpp v22, v28, v2 quad_perm:[0,0,0,0] row_mask:0xf bank_mask:0xf
v_and_b32_dpp v23, v28, v3 quad_perm:[0,0,0,0] row_mask:0xf bank_mask:0xf
s_nop 1
v_mfma_f32_4x4x4_16b_bf16 v[12:15], v[22:23], a[128:129], v[12:15] cbsz:2 abid:0
v_mfma_f32_4x4x4_16b_bf16 v[16:19], v[22:23], a[130:131], v[16:19] cbsz:2 abid:0
v_and_b32_dpp v24, v28, v2 quad_perm:[2,2,2,2] row_mask:0xf bank_mask:0xf
v_and_b32_dpp v25, v28, v3 quad_perm:[2,2,2,2] row_mask:0xf bank_mask:0xf
v_mfma_f32_4x4x4_16b_bf16 v[12:15], v[22:23], a[132:133], v[12:15] cbsz:2 abid:2
v_mfma_f32_4x4x4_16b_bf16 v[16:19], v[22:23], a[134:135], v[16:19] cbsz:2 abid:2
s_nop 0
v_mfma_f32_4x4x4_16b_bf16 v[12:15], v[22:23], a[136:137], v[12:15] cbsz:2 abid:1
v_mfma_f32_4x4x4_16b_bf16 v[16:19], v[22:23], a[138:139], v[16:19] cbsz:2 abid:1
s_nop 0
v_mfma_f32_4x4x4_16b_bf16 v[12:15], v[22:23], a[140:141], v[12:15] cbsz:2 abid:3
v_mfma_f32_4x4x4_16b_bf16 v[16:19], v[22:23], a[142:143], v[16:19] cbsz:2 abid:3
s_nop 0
v_mfma_f32_4x4x4_16b_bf16 v[12:15], v[24:25], a[144:145], v[12:15] cbsz:2 abid:0
v_mfma_f32_4x4x4_16b_bf16 v[16:19], v[24:25], a[146:147], v[16:19] cbsz:2 abid:0
v_and_b32_dpp v22, v28, v2 quad_perm:[1,1,1,1] row_mask:0xf bank_mask:0xf
v_and_b32_dpp v23, v28, v3 quad_perm:[1,1,1,1] row_mask:0xf bank_mask:0xf
s_waitcnt vmcnt(9)
s_add_u32 s40, %[rot], 4096
s_and_b32 s40, s40, 28672
s_add_u32 s40, s40, 1376256
buffer_load_dwordx4 v[72:75], v0, %[rk], s40 offen nt
v_mfma_f32_4x4x4_16b_bf16 v[12:15], v[24:25], a[148:149], v[12:15] cbsz:2 abid:2
v_mfma_f32_4x4x4_16b_bf16 v[16:19], v[24:25], a[150:151], v[16:19] cbsz:2 abid:2
s_nop 0
v_mfma_f32_4x4x4_16b_bf16 v[12:15], v[24:25], a[152:153], v[12:15] cbsz:2 abid:1
v_mfma_f32_4x4x4_16b_bf16 v[16:19], v[24:25], a[154:155], v[16:19] cbsz:2 abid:1
s_nop 0
v_mfma_f32_4x4x4_16b_bf16 v[12:15], v[24:25], v[204:205], v[12:15] cbsz:2 abid:3
v_mfma_f32_4x4x4_16b_bf16 v[16:19], v[24:25], v[206:207], v[16:19] cbsz:2 abid:3
s_waitcnt vmcnt(9)
s_add_u32 s40, %[rot], 8192
s_and_b32 s40, s40, 28672
s_add_u32 s40, s40, 1376256
buffer_load_dwordx4 v[76:79], v0, %[rk], s40 offen nt
v_mfma_f32_4x4x4_16b_bf16 v[12:15], v[22:23], v[208:209], v[12:15] cbsz:2 abid:0
v_mfma_f32_4x4x4_16b_bf16 v[16:19], v[22:23], v[210:211], v[16:19] cbsz:2 abid:0
s_waitcnt vmcnt(9)
Lpc21_%=:
s_memrealtime s[56:57]
s_waitcnt lgkmcnt(0)
s_sub_u32 s56, s56, s34
s_cmp_lt_u32 s56, 1324
s_cbranch_scc1 Lpc21_%=
s_add_u32 s40, %[rot], 12288
s_and_b32 s40, s40, 28672
s_add_u32 s40, s40, 1376256
buffer_load_dwordx4 v[80:83], v0, %[rk], s40 offen nt
v_dot2_f32_bf16 v31, v40, v4, 0
v_dot2_f32_bf16 v31, v41, v5, v31
v_dot2_f32_bf16 v31, v42, v6, v31
v_dot2_f32_bf16 v31, v43, v7, v31
s_waitcnt vmcnt(9)
s_add_u32 s40, %[rot], 16384
s_and_b32 s40, s40, 28672
s_add_u32 s40, s40, 1376256
buffer_load_dwordx4 v[84:87], v0, %[rk], s40 offen nt
v_dot2_f32_bf16 v32, v44, v4, 0
v_dot2_f32_bf16 v32, v45, v5, v32
v_dot2_f32_bf16 v32, v46, v6, v32
v_dot2_f32_bf16 v32, v47, v7, v32
s_waitcnt vmcnt(9)
s_add_u32 s40, %[rot], 20480
s_and_b32 s40, s40, 28672
s_add_u32 s40, s40, 1376256
buffer_load_dwordx4 v[88:91], v0, %[rk], s40 offen nt
v_dot2_f32_bf16 v33, v48, v4, 0
v_dot2_f32_bf16 v33, v49, v5, v33
v_dot2_f32_bf16 v33, v50, v6, v33
v_dot2_f32_bf16 v33, v51, v7, v33
v_add_f32_dpp v31, v31, v31 row_ror:8 row_mask:0xf bank_mask:0x3
s_waitcnt vmcnt(9)
s_add_u32 s40, %[rot], 24576
s_and_b32 s40, s40, 28672
s_add_u32 s40, s40, 1376256
buffer_load_dwordx4 v[92:95], v0, %[rk], s40 offen nt
s_mov_b32 s41, 2109440
buffer_load_dwordx4 v[8:11], off, %[rw], s41 sc1
v_dot2_f32_bf16 v34, v52, v4, 0
v_dot2_f32_bf16 v34, v53, v5, v34
v_dot2_f32_bf16 v34, v54, v6, v34
v_dot2_f32_bf16 v34, v55, v7, v34
v_add_f32_dpp v31, v32, v32 row_ror:8 row_mask:0xf bank_mask:0xc
s_waitcnt vmcnt(10)
Lpc22_%=:
s_memrealtime s[56:57]
s_waitcnt lgkmcnt(0)
s_sub_u32 s56, s56, s34
s_cmp_lt_u32 s56, 1382
s_cbranch_scc1 Lpc22_%=
s_add_u32 s40, %[rot], 28672
s_and_b32 s40, s40, 28672
s_add_u32 s40, s40, 1376256
buffer_load_dwordx4 v[96:99], v0, %[rk], s40 offen nt
v_dot2_f32_bf16 v35, v56, v4, 0
v_dot2_f32_bf16 v35, v57, v5, v35
v_dot2_f32_bf16 v35, v58, v6, v35
v_dot2_f32_bf16 v35, v59, v7, v35
v_add_f32_dpp v33, v33, v33 row_ror:8 row_mask:0xf bank_mask:0x3
s_waitcnt vmcnt(10)
s_add_u32 s40, %[rot], 0
s_and_b32 s40, s40, 28672
s_add_u32 s40, s40, 1605632
buffer_load_dwordx4 v[100:103], v0, %[rk], s40 offen nt
v_dot2_f32_bf16 v36, v60, v4, 0
v_dot2_f32_bf16 v36, v61, v5, v36
v_dot2_f32_bf16 v36, v62, v6, v36
v_dot2_f32_bf16 v36, v63, v7, v36
v_add_f32_dpp v33, v34, v34 row_ror:8 row_mask:0xf bank_mask:0xc
v_add_f32_dpp v31, v31, v31 row_shl:4 row_mask:0xf bank_mask:0x5
s_waitcnt vmcnt(10)
s_add_u32 s40, %[rot], 4096
s_and_b32 s40, s40, 28672
s_add_u32 s40, s40, 1146880
buffer_load_dwordx4 a[156:159], v0, %[rv], s40 offen nt
v_dot2_f32_bf16 v37, v64, v4, 0
v_dot2_f32_bf16 v37, v65, v5, v37
v_dot2_f32_bf16 v37, v66, v6, v37
v_dot2_f32_bf16 v37, v67, v7, v37
v_add_f32_dpp v31, v33, v33 row_shr:4 row_mask:0xf bank_mask:0xa
v_add_f32_dpp v35, v35, v35 row_ror:8 row_mask:0xf bank_mask:0x3
s_waitcnt vmcnt(10)
s_add_u32 s40, %[rot], 8192
s_and_b32 s40, s40, 28672
s_add_u32 s40, s40, 1146880
buffer_load_dwordx4 a[160:163], v0, %[rv], s40 offen nt
v_dot2_f32_bf16 v32, v68, v4, 0
v_dot2_f32_bf16 v32, v69, v5, v32
v_dot2_f32_bf16 v32, v70, v6, v32
v_dot2_f32_bf16 v32, v71, v7, v32
v_add_f32_dpp v35, v36, v36 row_ror:8 row_mask:0xf bank_mask:0xc
s_waitcnt vmcnt(10)
Lpc23_%=:
s_memrealtime s[56:57]
s_waitcnt lgkmcnt(0)
s_sub_u32 s56, s56, s34
s_cmp_lt_u32 s56, 1440
s_cbranch_scc1 Lpc23_%=
s_add_u32 s40, %[rot], 12288
s_and_b32 s40, s40, 28672
s_add_u32 s40, s40, 1146880
buffer_load_dwordx4 a[164:167], v0, %[rv], s40 offen nt
v_dot2_f32_bf16 v34, v72, v4, 0
v_dot2_f32_bf16 v34, v73, v5, v34
v_dot2_f32_bf16 v34, v74, v6, v34
v_dot2_f32_bf16 v34, v75, v7, v34
v_add_f32_dpp v37, v37, v37 row_ror:8 row_mask:0xf bank_mask:0x3
s_waitcnt vmcnt(10)
s_add_u32 s40, %[rot], 16384
s_and_b32 s40, s40, 28672
s_add_u32 s40, s40, 1146880
buffer_load_dwordx4 a[168:171], v0, %[rv], s40 offen nt
v_dot2_f32_bf16 v33, v76, v4, 0
v_dot2_f32_bf16 v33, v77, v5, v33
v_dot2_f32_bf16 v33, v78, v6, v33
v_dot2_f32_bf16 v33, v79, v7, v33
v_add_f32_dpp v37, v32, v32 row_ror:8 row_mask:0xf bank_mask:0xc
v_add_f32_dpp v35, v35, v35 row_shl:4 row_mask:0xf bank_mask:0x5
s_waitcnt vmcnt(10)
s_add_u32 s40, %[rot], 20480
s_and_b32 s40, s40, 28672
s_add_u32 s40, s40, 1146880
buffer_load_dwordx4 a[172:175], v0, %[rv], s40 offen nt
v_dot2_f32_bf16 v36, v80, v4, 0
v_dot2_f32_bf16 v36, v81, v5, v36
v_dot2_f32_bf16 v36, v82, v6, v36
v_dot2_f32_bf16 v36, v83, v7, v36
v_add_f32_dpp v35, v37, v37 row_shr:4 row_mask:0xf bank_mask:0xa
v_cndmask_b32 v30, v35, v31, s[44:45]
v_cndmask_b32 v31, v31, v35, s[44:45]
s_waitcnt vmcnt(10)
s_add_u32 s40, %[rot], 24576
s_and_b32 s40, s40, 28672
s_add_u32 s40, s40, 1146880
buffer_load_dwordx4 a[176:179], v0, %[rv], s40 offen nt
v_dot2_f32_bf16 v32, v84, v4, 0
v_dot2_f32_bf16 v32, v85, v5, v32
v_dot2_f32_bf16 v32, v86, v6, v32
v_dot2_f32_bf16 v32, v87, v7, v32
v_add_f32_dpp v31, v30, v31 quad_perm:[2,3,0,1] row_mask:0xf bank_mask:0xf
v_add_f32_dpp v34, v34, v34 row_ror:8 row_mask:0xf bank_mask:0x3
s_waitcnt vmcnt(10)
Lpc24_%=:
s_memrealtime s[56:57]
s_waitcnt lgkmcnt(0)
s_sub_u32 s56, s56, s34
s_cmp_lt_u32 s56, 1487
s_cbranch_scc1 Lpc24_%=
s_add_u32 s40, %[rot], 28672
s_and_b32 s40, s40, 28672
s_add_u32 s40, s40, 1146880
buffer_load_dwordx4 a[180:183], v0, %[rv], s40 offen nt
s_waitcnt vmcnt(9)
v_readfirstlane_b32 s36, v8
v_readfirstlane_b32 s37, v9
v_readfirstlane_b32 s38, v10
v_readfirstlane_b32 s39, v11
s_mov_b32 s42, %[xh]
s_cmp_eq_u64 s[36:37], s[58:59]
s_cbranch_scc1 Lback_p_%=
s_xor_b32 s42, %[xh], 0x2000
s_cmp_eq_u64 s[38:39], s[58:59]
s_cbranch_scc0 Lslow_p_%=
Lback_p_%=:
s_cmp_eq_u64 s[36:37], s[58:59]
s_cselect_b32 s41, s38, s36
s_sub_u32 s41, s41, s34
s_add_i32 s41, s41, 0
s_max_i32 s41, s41, -100
s_min_i32 s41, s41, 60
s_add_u32 s34, s34, s41
s_add_u32 s32, %[po], s42
s_add_u32 s33, %[q2], s42
s_mul_i32 s41, %[w], 262144
s_add_u32 s41, s41, s32
v_mov_b32 v8, -1
v_mov_b32 v9, -1
v_mov_b32 v10, -1
v_mov_b32 v11, -1
v_and_b32 v27, 0x3f0, v0
s_bfm_b64 exec, 33, 0
buffer_store_dwordx4 v[8:11], v27, %[rw], s41 offen sc1
s_mov_b64 exec, -1
v_dot2_f32_bf16 v37, v88, v4, 0
v_dot2_f32_bf16 v37, v89, v5, v37
v_dot2_f32_bf16 v37, v90, v6, v37
v_dot2_f32_bf16 v37, v91, v7, v37
v_add_f32_dpp v34, v33, v33 row_ror:8 row_mask:0xf bank_mask:0xc
v_add_f32_dpp v36, v36, v36 row_ror:8 row_mask:0xf bank_mask:0x3
s_add_u32 s40, %[rot], 0
s_and_b32 s40, s40, 28672
s_add_u32 s40, s40, 1376256
buffer_load_dwordx4 a[184:187], v0, %[rv], s40 offen nt
v_dot2_f32_bf16 v35, v92, v4, 0
v_dot2_f32_bf16 v35, v93, v5, v35
v_dot2_f32_bf16 v35, v94, v6, v35
v_dot2_f32_bf16 v35, v95, v7, v35
v_add_f32_dpp v36, v32, v32 row_ror:8 row_mask:0xf bank_mask:0xc
v_add_f32_dpp v34, v34, v34 row_shl:4 row_mask:0xf bank_mask:0x5
s_waitcnt vmcnt(10)
s_add_u32 s40, %[rot], 4096
s_and_b32 s40, s40, 28672
s_add_u32 s40, s40, 1376256
buffer_load_dwordx4 a[188:191], v0, %[rv], s40 offen nt
v_dot2_f32_bf16 v33, v96, v4, 0
v_dot2_f32_bf16 v33, v97, v5, v33
v_dot2_f32_bf16 v33, v98, v6, v33
v_dot2_f32_bf16 v33, v99, v7, v33
v_add_f32_dpp v34, v36, v36 row_shr:4 row_mask:0xf bank_mask:0xa
v_add_f32_dpp v37, v37, v37 row_ror:8 row_mask:0xf bank_mask:0x3
s_waitcnt vmcnt(10)
s_add_u32 s40, %[rot], 8192
s_and_b32 s40, s40, 28672
s_add_u32 s40, s40, 1376256
buffer_load_dwordx4 a[192:195], v0, %[rv], s40 offen nt
v_dot2_f32_bf16 v32, v100, v4, 0
v_dot2_f32_bf16 v32, v101, v5, v32
v_dot2_f32_bf16 v32, v102, v6, v32
v_dot2_f32_bf16 v32, v103, v7, v32
v_add_f32_dpp v37, v35, v35 row_ror:8 row_mask:0xf bank_mask:0xc
v_add_f32_dpp v33, v33, v33 row_ror:8 row_mask:0xf bank_mask:0x3
s_nop 1
v_add_f32_dpp v33, v32, v32 row_ror:8 row_mask:0xf bank_mask:0xc
v_add_f32_dpp v37, v37, v37 row_shl:4 row_mask:0xf bank_mask:0x5
s_nop 1
v_add_f32_dpp v37, v33, v33 row_shr:4 row_mask:0xf bank_mask:0xa
v_cndmask_b32 v30, v37, v34, s[44:45]
v_cndmask_b32 v34, v34, v37, s[44:45]
s_nop 1
v_add_f32_dpp v34, v30, v34 quad_perm:[2,3,0,1] row_mask:0xf bank_mask:0xf
v_cndmask_b32 v30, v34, v31, s[46:47]
v_cndmask_b32 v31, v31, v34, s[46:47]
s_nop 1
v_add_f32_dpp v26, v30, v31 quad_perm:[1,0,3,2] row_mask:0xf bank_mask:0xf
v_cvt_pk_bf16_f32 v27, v26, v26
v_and_b32 v26, 0xffff0000, v27
v_mul_f32 v26, s55, v26
v_cvt_pk_bf16_f32 v27, v26, v26
v_and_b32 v26, 0xffff0000, v27
s_nop 1
v_max_f32_dpp v29, v26, v26 row_ror:8 row_mask:0xf bank_mask:0xf
s_nop 1
v_max_f32_dpp v29, v29, v29 row_ror:4 row_mask:0xf bank_mask:0xf
s_nop 1
v_max_f32_dpp v29, v29, v29 row_ror:2 row_mask:0xf bank_mask:0xf
s_nop 1
v_max_f32_dpp v29, v29, v29 row_ror:1 row_mask:0xf bank_mask:0xf
s_nop 1
v_max_f32_dpp v29, v29, v29 row_bcast:15 row_mask:0xa bank_mask:0xf
s_nop 1
v_max_f32_dpp v29, v29, v29 row_bcast:31 row_mask:0xc bank_mask:0xf
s_nop 0
v_readlane_b32 s50, v29, 63
v_mov_b32 v27, s54
s_nop 0
v_mul_f32 v27, s50, v27
v_max_f32 v30, v27, v20
v_sub_f32 v29, v20, v30
v_mov_b32 v20, v30
v_exp_f32 v29, v29
v_fma_f32 v26, v26, s54, -v20
v_exp_f32 v26, v26
v_mul_f32 v12, v29, v12
v_mul_f32 v13, v29, v13
v_mul_f32 v14, v29, v14
v_mul_f32 v15, v29, v15
v_mul_f32 v16, v29, v16
v_mul_f32 v17, v29, v17
v_mul_f32 v18, v29, v18
v_mul_f32 v19, v29, v19
v_cvt_pk_bf16_f32 v28, v26, v26
v_fma_f32 v21, v21, v29, v26
s_nop 0
v_and_b32_dpp v22, v28, v2 quad_perm:[0,0,0,0] row_mask:0xf bank_mask:0xf
v_and_b32_dpp v23, v28, v3 quad_perm:[0,0,0,0] row_mask:0xf bank_mask:0xf
s_waitcnt vmcnt(10)
Lpc25_%=:
s_memrealtime s[56:57]
s_waitcnt lgkmcnt(0)
s_sub_u32 s56, s56, s34
s_cmp_lt_u32 s56, 1534
s_cbranch_scc1 Lpc25_%=
s_add_u32 s40, %[rot], 12288
s_and_b32 s40, s40, 28672
s_add_u32 s40, s40, 1376256
buffer_load_dwordx4 a[196:199], v0, %[rv], s40 offen nt
v_mfma_f32_4x4x4_16b_bf16 v[12:15], v[22:23], a[156:157], v[12:15] cbsz:2 abid:0
v_mfma_f32_4x4x4_16b_bf16 v[16:19], v[22:23], a[158:159], v[16:19] cbsz:2 abid:0
v_and_b32_dpp v24, v28, v2 quad_perm:[2,2,2,2] row_mask:0xf bank_mask:0xf
v_and_b32_dpp v25, v28, v3 quad_perm:[2,2,2,2] row_mask:0xf bank_mask:0xf
s_waitcnt vmcnt(10)
s_add_u32 s40, %[rot], 16384
s_and_b32 s40, s40, 28672
s_add_u32 s40, s40, 1376256
buffer_load_dwordx4 a[200:203], v0, %[rv], s40 offen nt
v_mfma_f32_4x4x4_16b_bf16 v[12:15], v[22:23], a[160:161], v[12:15] cbsz:2 abid:2
v_mfma_f32_4x4x4_16b_bf16 v[16:19], v[22:23], a[162:163], v[16:19] cbsz:2 abid:2
s_waitcnt vmcnt(10)
s_add_u32 s40, %[rot], 20480
s_and_b32 s40, s40, 28672
s_add_u32 s40, s40, 1376256
buffer_load_dwordx4 a[204:207], v0, %[rv], s40 offen nt
v_mfma_f32_4x4x4_16b_bf16 v[12:15], v[22:23], a[164:165], v[12:15] cbsz:2 abid:1
v_mfma_f32_4x4x4_16b_bf16 v[16:19], v[22:23], a[166:167], v[16:19] cbsz:2 abid:1
s_waitcnt vmcnt(10)
s_add_u32 s40, %[rot], 24576
s_and_b32 s40, s40, 28672
s_add_u32 s40, s40, 1376256
buffer_load_dwordx4 a[208:211], v0, %[rv], s40 offen nt
v_mfma_f32_4x4x4_16b_bf16 v[12:15], v[22:23], a[168:169], v[12:15] cbsz:2 abid:3
v_mfma_f32_4x4x4_16b_bf16 v[16:19], v[22:23], a[170:171], v[16:19] cbsz:2 abid:3
s_waitcnt vmcnt(10)
Lpc26_%=:
s_memrealtime s[56:57]
s_waitcnt lgkmcnt(0)
s_sub_u32 s56, s56, s34
)ASM"
R"ASM(s_cmp_lt_u32 s56, 1581
s_cbranch_scc1 Lpc26_%=
s_add_u32 s40, %[rot], 28672
s_and_b32 s40, s40, 28672
s_add_u32 s40, s40, 1376256
buffer_load_dwordx4 a[212:215], v0, %[rv], s40 offen nt
v_mfma_f32_4x4x4_16b_bf16 v[12:15], v[24:25], a[172:173], v[12:15] cbsz:2 abid:0
v_mfma_f32_4x4x4_16b_bf16 v[16:19], v[24:25], a[174:175], v[16:19] cbsz:2 abid:0
v_and_b32_dpp v22, v28, v2 quad_perm:[1,1,1,1] row_mask:0xf bank_mask:0xf
v_and_b32_dpp v23, v28, v3 quad_perm:[1,1,1,1] row_mask:0xf bank_mask:0xf
s_waitcnt vmcnt(10)
s_add_u32 s40, %[rot], 0
s_and_b32 s40, s40, 28672
s_add_u32 s40, s40, 1605632
buffer_load_dwordx4 a[216:219], v0, %[rv], s40 offen nt
s_waitcnt vmcnt(9)
v_mov_b32 v8, s58
v_mov_b32 v9, s59
s_mov_b64 exec, 1
buffer_store_dwordx2 v[8:9], off, %[rw], %[fl] sc1
s_mov_b64 exec, -1
v_mfma_f32_4x4x4_16b_bf16 v[12:15], v[24:25], a[176:177], v[12:15] cbsz:2 abid:2
v_mfma_f32_4x4x4_16b_bf16 v[16:19], v[24:25], a[178:179], v[16:19] cbsz:2 abid:2
s_add_u32 s40, %[rot], 4096
s_and_b32 s40, s40, 28672
s_add_u32 s40, s40, 1605632
buffer_load_dwordx4 v[104:107], v0, %[rk], s40 offen nt
v_mfma_f32_4x4x4_16b_bf16 v[12:15], v[24:25], a[180:181], v[12:15] cbsz:2 abid:1
v_mfma_f32_4x4x4_16b_bf16 v[16:19], v[24:25], a[182:183], v[16:19] cbsz:2 abid:1
s_waitcnt vmcnt(10)
s_add_u32 s40, %[rot], 8192
s_and_b32 s40, s40, 28672
s_add_u32 s40, s40, 1605632
buffer_load_dwordx4 v[108:111], v0, %[rk], s40 offen nt
v_mfma_f32_4x4x4_16b_bf16 v[12:15], v[24:25], a[184:185], v[12:15] cbsz:2 abid:3
v_mfma_f32_4x4x4_16b_bf16 v[16:19], v[24:25], a[186:187], v[16:19] cbsz:2 abid:3
s_waitcnt vmcnt(10)
Lpc27_%=:
s_memrealtime s[56:57]
s_waitcnt lgkmcnt(0)
s_sub_u32 s56, s56, s34
s_cmp_lt_u32 s56, 1628
s_cbranch_scc1 Lpc27_%=
s_add_u32 s40, %[rot], 12288
s_and_b32 s40, s40, 28672
s_add_u32 s40, s40, 1605632
buffer_load_dwordx4 v[112:115], v0, %[rk], s40 offen nt
v_mfma_f32_4x4x4_16b_bf16 v[12:15], v[22:23], a[188:189], v[12:15] cbsz:2 abid:0
v_mfma_f32_4x4x4_16b_bf16 v[16:19], v[22:23], a[190:191], v[16:19] cbsz:2 abid:0
v_and_b32_dpp v24, v28, v2 quad_perm:[3,3,3,3] row_mask:0xf bank_mask:0xf
v_and_b32_dpp v25, v28, v3 quad_perm:[3,3,3,3] row_mask:0xf bank_mask:0xf
s_waitcnt vmcnt(10)
s_add_u32 s40, %[rot], 16384
s_and_b32 s40, s40, 28672
s_add_u32 s40, s40, 1605632
buffer_load_dwordx4 v[116:119], v0, %[rk], s40 offen nt
v_mfma_f32_4x4x4_16b_bf16 v[12:15], v[22:23], a[192:193], v[12:15] cbsz:2 abid:2
v_mfma_f32_4x4x4_16b_bf16 v[16:19], v[22:23], a[194:195], v[16:19] cbsz:2 abid:2
s_waitcnt vmcnt(10)
s_add_u32 s40, %[rot], 20480
s_and_b32 s40, s40, 28672
s_add_u32 s40, s40, 1605632
buffer_load_dwordx4 v[120:123], v0, %[rk], s40 offen nt
v_mfma_f32_4x4x4_16b_bf16 v[12:15], v[22:23], a[196:197], v[12:15] cbsz:2 abid:1
v_mfma_f32_4x4x4_16b_bf16 v[16:19], v[22:23], a[198:199], v[16:19] cbsz:2 abid:1
s_waitcnt vmcnt(10)
s_add_u32 s40, %[rot], 24576
s_and_b32 s40, s40, 28672
s_add_u32 s40, s40, 1605632
buffer_load_dwordx4 v[124:127], v0, %[rk], s40 offen nt
v_mfma_f32_4x4x4_16b_bf16 v[12:15], v[22:23], a[200:201], v[12:15] cbsz:2 abid:3
v_mfma_f32_4x4x4_16b_bf16 v[16:19], v[22:23], a[202:203], v[16:19] cbsz:2 abid:3
s_waitcnt vmcnt(10)
Lpc28_%=:
s_memrealtime s[56:57]
s_waitcnt lgkmcnt(0)
s_sub_u32 s56, s56, s34
s_cmp_lt_u32 s56, 1676
s_cbranch_scc1 Lpc28_%=
s_add_u32 s40, %[rot], 28672
s_and_b32 s40, s40, 28672
s_add_u32 s40, s40, 1605632
buffer_load_dwordx4 v[128:131], v0, %[rk], s40 offen nt
v_mfma_f32_4x4x4_16b_bf16 v[12:15], v[24:25], a[204:205], v[12:15] cbsz:2 abid:0
v_mfma_f32_4x4x4_16b_bf16 v[16:19], v[24:25], a[206:207], v[16:19] cbsz:2 abid:0
s_waitcnt vmcnt(10)
s_add_u32 s40, %[rot], 0
s_and_b32 s40, s40, 28672
s_add_u32 s40, s40, 1835008
buffer_load_dwordx4 v[132:135], v0, %[rk], s40 offen nt
v_mfma_f32_4x4x4_16b_bf16 v[12:15], v[24:25], a[208:209], v[12:15] cbsz:2 abid:2
v_mfma_f32_4x4x4_16b_bf16 v[16:19], v[24:25], a[210:211], v[16:19] cbsz:2 abid:2
s_waitcnt vmcnt(10)
s_add_u32 s40, %[rot], 4096
s_and_b32 s40, s40, 28672
s_add_u32 s40, s40, 1835008
buffer_load_dwordx4 v[136:139], v0, %[rk], s40 offen nt
v_mfma_f32_4x4x4_16b_bf16 v[12:15], v[24:25], a[212:213], v[12:15] cbsz:2 abid:1
v_mfma_f32_4x4x4_16b_bf16 v[16:19], v[24:25], a[214:215], v[16:19] cbsz:2 abid:1
s_waitcnt vmcnt(10)
s_add_u32 s40, %[rot], 8192
s_and_b32 s40, s40, 28672
s_add_u32 s40, s40, 1835008
buffer_load_dwordx4 v[140:143], v0, %[rk], s40 offen nt
v_mfma_f32_4x4x4_16b_bf16 v[12:15], v[24:25], a[216:217], v[12:15] cbsz:2 abid:3
v_mfma_f32_4x4x4_16b_bf16 v[16:19], v[24:25], a[218:219], v[16:19] cbsz:2 abid:3
s_waitcnt vmcnt(9)
Lpc29_%=:
s_memrealtime s[56:57]
s_waitcnt lgkmcnt(0)
s_sub_u32 s56, s56, s34
s_cmp_lt_u32 s56, 1723
s_cbranch_scc1 Lpc29_%=
s_add_u32 s40, %[rot], 12288
s_and_b32 s40, s40, 28672
s_add_u32 s40, s40, 1835008
buffer_load_dwordx4 v[144:147], v0, %[rk], s40 offen nt
v_dot2_f32_bf16 v31, v104, v4, 0
v_dot2_f32_bf16 v31, v105, v5, v31
v_dot2_f32_bf16 v31, v106, v6, v31
v_dot2_f32_bf16 v31, v107, v7, v31
s_waitcnt vmcnt(9)
s_add_u32 s40, %[rot], 16384
s_and_b32 s40, s40, 28672
s_add_u32 s40, s40, 1835008
buffer_load_dwordx4 v[148:151], v0, %[rk], s40 offen nt
v_dot2_f32_bf16 v32, v108, v4, 0
v_dot2_f32_bf16 v32, v109, v5, v32
v_dot2_f32_bf16 v32, v110, v6, v32
v_dot2_f32_bf16 v32, v111, v7, v32
s_waitcnt vmcnt(9)
s_add_u32 s40, %[rot], 20480
s_and_b32 s40, s40, 28672
s_add_u32 s40, s40, 1835008
buffer_load_dwordx4 v[152:155], v0, %[rk], s40 offen nt
v_dot2_f32_bf16 v33, v112, v4, 0
v_dot2_f32_bf16 v33, v113, v5, v33
v_dot2_f32_bf16 v33, v114, v6, v33
v_dot2_f32_bf16 v33, v115, v7, v33
v_add_f32_dpp v31, v31, v31 row_ror:8 row_mask:0xf bank_mask:0x3
s_waitcnt vmcnt(9)
s_add_u32 s40, %[rot], 24576
s_and_b32 s40, s40, 28672
s_add_u32 s40, s40, 1835008
buffer_load_dwordx4 v[156:159], v0, %[rk], s40 offen nt
v_dot2_f32_bf16 v34, v116, v4, 0
v_dot2_f32_bf16 v34, v117, v5, v34
v_dot2_f32_bf16 v34, v118, v6, v34
v_dot2_f32_bf16 v34, v119, v7, v34
v_add_f32_dpp v31, v32, v32 row_ror:8 row_mask:0xf bank_mask:0xc
s_waitcnt vmcnt(9)
Lpc30_%=:
s_memrealtime s[56:57]
s_waitcnt lgkmcnt(0)
s_sub_u32 s56, s56, s34
s_cmp_lt_u32 s56, 1770
s_cbranch_scc1 Lpc30_%=
s_add_u32 s40, %[rot], 28672
s_and_b32 s40, s40, 28672
s_add_u32 s40, s40, 1835008
buffer_load_dwordx4 v[160:163], v0, %[rk], s40 offen nt
v_dot2_f32_bf16 v35, v120, v4, 0
v_dot2_f32_bf16 v35, v121, v5, v35
v_dot2_f32_bf16 v35, v122, v6, v35
v_dot2_f32_bf16 v35, v123, v7, v35
v_add_f32_dpp v33, v33, v33 row_ror:8 row_mask:0xf bank_mask:0x3
s_waitcnt vmcnt(9)
s_lshr_b32 s40, %[pf], 12
s_mul_i32 s43, s40, 37
s_lshr_b32 s43, s43, 8
s_mul_i32 s43, s43, 7
s_sub_u32 s40, s40, s43
s_mul_i32 s40, s40, 28672
s_sub_u32 s40, 2064384, s40
buffer_load_dwordx4 v[164:167], v0, %[rk], s40 offen nt
v_dot2_f32_bf16 v36, v124, v4, 0
v_dot2_f32_bf16 v36, v125, v5, v36
v_dot2_f32_bf16 v36, v126, v6, v36
v_dot2_f32_bf16 v36, v127, v7, v36
v_add_f32_dpp v33, v34, v34 row_ror:8 row_mask:0xf bank_mask:0xc
v_add_f32_dpp v31, v31, v31 row_shl:4 row_mask:0xf bank_mask:0x5
s_waitcnt vmcnt(9)
s_add_u32 s40, %[rot], 4096
s_and_b32 s40, s40, 28672
s_add_u32 s40, s40, 1605632
buffer_load_dwordx4 a[220:223], v0, %[rv], s40 offen nt
v_dot2_f32_bf16 v37, v128, v4, 0
v_dot2_f32_bf16 v37, v129, v5, v37
v_dot2_f32_bf16 v37, v130, v6, v37
v_dot2_f32_bf16 v37, v131, v7, v37
v_add_f32_dpp v31, v33, v33 row_shr:4 row_mask:0xf bank_mask:0xa
v_add_f32_dpp v35, v35, v35 row_ror:8 row_mask:0xf bank_mask:0x3
s_waitcnt vmcnt(9)
s_add_u32 s40, %[rot], 8192
s_and_b32 s40, s40, 28672
s_add_u32 s40, s40, 1605632
buffer_load_dwordx4 a[224:227], v0, %[rv], s40 offen nt
v_dot2_f32_bf16 v32, v132, v4, 0
v_dot2_f32_bf16 v32, v133, v5, v32
v_dot2_f32_bf16 v32, v134, v6, v32
v_dot2_f32_bf16 v32, v135, v7, v32
v_add_f32_dpp v35, v36, v36 row_ror:8 row_mask:0xf bank_mask:0xc
s_waitcnt vmcnt(9)
Lpc31_%=:
s_memrealtime s[56:57]
s_waitcnt lgkmcnt(0)
s_sub_u32 s56, s56, s34
s_cmp_lt_u32 s56, 1817
s_cbranch_scc1 Lpc31_%=
s_add_u32 s40, %[rot], 12288
s_and_b32 s40, s40, 28672
s_add_u32 s40, s40, 1605632
buffer_load_dwordx4 a[228:231], v0, %[rv], s40 offen nt
v_dot2_f32_bf16 v34, v136, v4, 0
v_dot2_f32_bf16 v34, v137, v5, v34
v_dot2_f32_bf16 v34, v138, v6, v34
v_dot2_f32_bf16 v34, v139, v7, v34
v_add_f32_dpp v37, v37, v37 row_ror:8 row_mask:0xf bank_mask:0x3
s_waitcnt vmcnt(9)
s_add_u32 s40, %[rot], 16384
s_and_b32 s40, s40, 28672
s_add_u32 s40, s40, 1605632
buffer_load_dwordx4 a[232:235], v0, %[rv], s40 offen nt
v_dot2_f32_bf16 v33, v140, v4, 0
v_dot2_f32_bf16 v33, v141, v5, v33
v_dot2_f32_bf16 v33, v142, v6, v33
v_dot2_f32_bf16 v33, v143, v7, v33
v_add_f32_dpp v37, v32, v32 row_ror:8 row_mask:0xf bank_mask:0xc
v_add_f32_dpp v35, v35, v35 row_shl:4 row_mask:0xf bank_mask:0x5
s_waitcnt vmcnt(9)
s_add_u32 s40, %[rot], 20480
s_and_b32 s40, s40, 28672
s_add_u32 s40, s40, 1605632
buffer_load_dwordx4 a[236:239], v0, %[rv], s40 offen nt
v_dot2_f32_bf16 v36, v144, v4, 0
v_dot2_f32_bf16 v36, v145, v5, v36
v_dot2_f32_bf16 v36, v146, v6, v36
v_dot2_f32_bf16 v36, v147, v7, v36
v_add_f32_dpp v35, v37, v37 row_shr:4 row_mask:0xf bank_mask:0xa
v_cndmask_b32 v30, v35, v31, s[44:45]
v_cndmask_b32 v31, v31, v35, s[44:45]
s_waitcnt vmcnt(9)
s_add_u32 s40, %[rot], 24576
s_and_b32 s40, s40, 28672
s_add_u32 s40, s40, 1605632
buffer_load_dwordx4 a[240:243], v0, %[rv], s40 offen nt
v_dot2_f32_bf16 v32, v148, v4, 0
v_dot2_f32_bf16 v32, v149, v5, v32
v_dot2_f32_bf16 v32, v150, v6, v32
v_dot2_f32_bf16 v32, v151, v7, v32
v_add_f32_dpp v31, v30, v31 quad_perm:[2,3,0,1] row_mask:0xf bank_mask:0xf
v_add_f32_dpp v34, v34, v34 row_ror:8 row_mask:0xf bank_mask:0x3
s_waitcnt vmcnt(9)
Lpc32_%=:
s_memrealtime s[56:57]
s_waitcnt lgkmcnt(0)
s_sub_u32 s56, s56, s34
s_cmp_lt_u32 s56, 1864
s_cbranch_scc1 Lpc32_%=
s_add_u32 s40, %[rot], 28672
s_and_b32 s40, s40, 28672
s_add_u32 s40, s40, 1605632
buffer_load_dwordx4 a[244:247], v0, %[rv], s40 offen nt
v_dot2_f32_bf16 v37, v152, v4, 0
v_dot2_f32_bf16 v37, v153, v5, v37
v_dot2_f32_bf16 v37, v154, v6, v37
v_dot2_f32_bf16 v37, v155, v7, v37
v_add_f32_dpp v34, v33, v33 row_ror:8 row_mask:0xf bank_mask:0xc
v_add_f32_dpp v36, v36, v36 row_ror:8 row_mask:0xf bank_mask:0x3
s_waitcnt vmcnt(9)
s_add_u32 s40, %[rot], 0
s_and_b32 s40, s40, 28672
s_add_u32 s40, s40, 1835008
buffer_load_dwordx4 a[248:251], v0, %[rv], s40 offen nt
v_dot2_f32_bf16 v35, v156, v4, 0
v_dot2_f32_bf16 v35, v157, v5, v35
v_dot2_f32_bf16 v35, v158, v6, v35
v_dot2_f32_bf16 v35, v159, v7, v35
v_add_f32_dpp v36, v32, v32 row_ror:8 row_mask:0xf bank_mask:0xc
v_add_f32_dpp v34, v34, v34 row_shl:4 row_mask:0xf bank_mask:0x5
s_waitcnt vmcnt(9)
s_add_u32 s40, %[rot], 4096
s_and_b32 s40, s40, 28672
s_add_u32 s40, s40, 1835008
buffer_load_dwordx4 a[252:255], v0, %[rv], s40 offen nt
v_dot2_f32_bf16 v33, v160, v4, 0
v_dot2_f32_bf16 v33, v161, v5, v33
v_dot2_f32_bf16 v33, v162, v6, v33
v_dot2_f32_bf16 v33, v163, v7, v33
v_add_f32_dpp v34, v36, v36 row_shr:4 row_mask:0xf bank_mask:0xa
v_add_f32_dpp v37, v37, v37 row_ror:8 row_mask:0xf bank_mask:0x3
s_waitcnt vmcnt(9)
s_add_u32 s40, %[rot], 8192
s_and_b32 s40, s40, 28672
s_add_u32 s40, s40, 1835008
buffer_load_dwordx4 v[176:179], v0, %[rv], s40 offen nt
s_add_u32 s41, %[qo], 256
buffer_load_dwordx4 v[8:11], v39, %[rw], s41 offen sc1
v_dot2_f32_bf16 v32, v164, v4, 0
v_dot2_f32_bf16 v32, v165, v5, v32
v_dot2_f32_bf16 v32, v166, v6, v32
v_dot2_f32_bf16 v32, v167, v7, v32
v_add_f32_dpp v37, v35, v35 row_ror:8 row_mask:0xf bank_mask:0xc
v_add_f32_dpp v33, v33, v33 row_ror:8 row_mask:0xf bank_mask:0x3
s_nop 1
v_add_f32_dpp v33, v32, v32 row_ror:8 row_mask:0xf bank_mask:0xc
v_add_f32_dpp v37, v37, v37 row_shl:4 row_mask:0xf bank_mask:0x5
s_nop 1
v_add_f32_dpp v37, v33, v33 row_shr:4 row_mask:0xf bank_mask:0xa
v_cndmask_b32 v30, v37, v34, s[44:45]
v_cndmask_b32 v34, v34, v37, s[44:45]
s_nop 1
v_add_f32_dpp v34, v30, v34 quad_perm:[2,3,0,1] row_mask:0xf bank_mask:0xf
v_cndmask_b32 v30, v34, v31, s[46:47]
v_cndmask_b32 v31, v31, v34, s[46:47]
s_nop 1
v_add_f32_dpp v26, v30, v31 quad_perm:[1,0,3,2] row_mask:0xf bank_mask:0xf
v_cvt_pk_bf16_f32 v27, v26, v26
v_and_b32 v26, 0xffff0000, v27
v_mul_f32 v26, s55, v26
v_cvt_pk_bf16_f32 v27, v26, v26
v_and_b32 v26, 0xffff0000, v27
s_nop 1
v_max_f32_dpp v29, v26, v26 row_ror:8 row_mask:0xf bank_mask:0xf
s_nop 1
v_max_f32_dpp v29, v29, v29 row_ror:4 row_mask:0xf bank_mask:0xf
s_nop 1
v_max_f32_dpp v29, v29, v29 row_ror:2 row_mask:0xf bank_mask:0xf
s_nop 1
v_max_f32_dpp v29, v29, v29 row_ror:1 row_mask:0xf bank_mask:0xf
s_nop 1
v_max_f32_dpp v29, v29, v29 row_bcast:15 row_mask:0xa bank_mask:0xf
s_nop 1
v_max_f32_dpp v29, v29, v29 row_bcast:31 row_mask:0xc bank_mask:0xf
s_nop 0
v_readlane_b32 s50, v29, 63
v_mov_b32 v27, s54
s_nop 0
v_mul_f32 v27, s50, v27
v_max_f32 v30, v27, v20
v_sub_f32 v29, v20, v30
v_mov_b32 v20, v30
v_exp_f32 v29, v29
v_fma_f32 v26, v26, s54, -v20
v_exp_f32 v26, v26
v_mul_f32 v12, v29, v12
v_mul_f32 v13, v29, v13
v_mul_f32 v14, v29, v14
v_mul_f32 v15, v29, v15
v_mul_f32 v16, v29, v16
v_mul_f32 v17, v29, v17
v_mul_f32 v18, v29, v18
v_mul_f32 v19, v29, v19
v_cvt_pk_bf16_f32 v28, v26, v26
v_fma_f32 v21, v21, v29, v26
s_nop 1
v_add_f32_dpp v21, v21, v21 row_ror:8 row_mask:0xf bank_mask:0xf
s_nop 1
v_add_f32_dpp v21, v21, v21 row_ror:4 row_mask:0xf bank_mask:0xf
s_nop 1
v_add_f32_dpp v21, v21, v21 row_ror:2 row_mask:0xf bank_mask:0xf
s_nop 1
v_add_f32_dpp v21, v21, v21 row_ror:1 row_mask:0xf bank_mask:0xf
s_nop 1
v_add_f32_dpp v21, v21, v21 row_bcast:15 row_mask:0xa bank_mask:0xf
s_nop 1
v_add_f32_dpp v21, v21, v21 row_bcast:31 row_mask:0xc bank_mask:0xf
v_and_b32_dpp v22, v28, v2 quad_perm:[0,0,0,0] row_mask:0xf bank_mask:0xf
v_and_b32_dpp v23, v28, v3 quad_perm:[0,0,0,0] row_mask:0xf bank_mask:0xf
s_waitcnt vmcnt(10)
Lpc33_%=:
s_memrealtime s[56:57]
s_waitcnt lgkmcnt(0)
s_sub_u32 s56, s56, s34
s_cmp_lt_u32 s56, 1912
s_cbranch_scc1 Lpc33_%=
s_add_u32 s40, %[rot], 12288
s_and_b32 s40, s40, 28672
s_add_u32 s40, s40, 1835008
buffer_load_dwordx4 v[180:183], v0, %[rv], s40 offen nt
v_mfma_f32_4x4x4_16b_bf16 v[12:15], v[22:23], a[220:221], v[12:15] cbsz:2 abid:0
v_mfma_f32_4x4x4_16b_bf16 v[16:19], v[22:23], a[222:223], v[16:19] cbsz:2 abid:0
v_and_b32_dpp v24, v28, v2 quad_perm:[2,2,2,2] row_mask:0xf bank_mask:0xf
v_and_b32_dpp v25, v28, v3 quad_perm:[2,2,2,2] row_mask:0xf bank_mask:0xf
s_waitcnt vmcnt(10)
s_add_u32 s40, %[rot], 16384
s_and_b32 s40, s40, 28672
s_add_u32 s40, s40, 1835008
buffer_load_dwordx4 v[184:187], v0, %[rv], s40 offen nt
v_mfma_f32_4x4x4_16b_bf16 v[12:15], v[22:23], a[224:225], v[12:15] cbsz:2 abid:2
v_mfma_f32_4x4x4_16b_bf16 v[16:19], v[22:23], a[226:227], v[16:19] cbsz:2 abid:2
s_waitcnt vmcnt(10)
s_add_u32 s40, %[rot], 20480
s_and_b32 s40, s40, 28672
s_add_u32 s40, s40, 1835008
buffer_load_dwordx4 v[188:191], v0, %[rv], s40 offen nt
v_mfma_f32_4x4x4_16b_bf16 v[12:15], v[22:23], a[228:229], v[12:15] cbsz:2 abid:1
v_mfma_f32_4x4x4_16b_bf16 v[16:19], v[22:23], a[230:231], v[16:19] cbsz:2 abid:1
s_waitcnt vmcnt(10)
s_add_u32 s40, %[rot], 24576
)ASM"
R"ASM(s_and_b32 s40, s40, 28672
s_add_u32 s40, s40, 1835008
buffer_load_dwordx4 v[192:195], v0, %[rv], s40 offen nt
v_mfma_f32_4x4x4_16b_bf16 v[12:15], v[22:23], a[232:233], v[12:15] cbsz:2 abid:3
v_mfma_f32_4x4x4_16b_bf16 v[16:19], v[22:23], a[234:235], v[16:19] cbsz:2 abid:3
s_waitcnt vmcnt(10)
Lpc34_%=:
s_memrealtime s[56:57]
s_waitcnt lgkmcnt(0)
s_sub_u32 s56, s56, s34
s_cmp_lt_u32 s56, 1959
s_cbranch_scc1 Lpc34_%=
s_add_u32 s40, %[rot], 28672
s_and_b32 s40, s40, 28672
s_add_u32 s40, s40, 1835008
buffer_load_dwordx4 v[196:199], v0, %[rv], s40 offen nt
v_mfma_f32_4x4x4_16b_bf16 v[12:15], v[24:25], a[236:237], v[12:15] cbsz:2 abid:0
v_mfma_f32_4x4x4_16b_bf16 v[16:19], v[24:25], a[238:239], v[16:19] cbsz:2 abid:0
v_and_b32_dpp v22, v28, v2 quad_perm:[1,1,1,1] row_mask:0xf bank_mask:0xf
v_and_b32_dpp v23, v28, v3 quad_perm:[1,1,1,1] row_mask:0xf bank_mask:0xf
s_waitcnt vmcnt(10)
s_lshr_b32 s40, %[pf], 12
s_mul_i32 s43, s40, 37
s_lshr_b32 s43, s43, 8
s_mul_i32 s43, s43, 7
s_sub_u32 s40, s40, s43
s_mul_i32 s40, s40, 28672
s_sub_u32 s40, 2064384, s40
buffer_load_dwordx4 v[200:203], v0, %[rv], s40 offen nt
v_mfma_f32_4x4x4_16b_bf16 v[12:15], v[24:25], a[240:241], v[12:15] cbsz:2 abid:2
v_mfma_f32_4x4x4_16b_bf16 v[16:19], v[24:25], a[242:243], v[16:19] cbsz:2 abid:2
s_waitcnt vmcnt(8)
v_add_u32 v0, 67108864, v0
s_add_u32 s40, %[rot], 0
s_and_b32 s40, s40, 28672
s_add_u32 s40, s40, 0
buffer_load_dwordx4 v[212:215], v0, %[rk], s40 offen nt
v_mfma_f32_4x4x4_16b_bf16 v[12:15], v[24:25], a[244:245], v[12:15] cbsz:2 abid:1
v_mfma_f32_4x4x4_16b_bf16 v[16:19], v[24:25], a[246:247], v[16:19] cbsz:2 abid:1
s_nop 0
v_mfma_f32_4x4x4_16b_bf16 v[12:15], v[24:25], a[248:249], v[12:15] cbsz:2 abid:3
v_mfma_f32_4x4x4_16b_bf16 v[16:19], v[24:25], a[250:251], v[16:19] cbsz:2 abid:3
s_nop 0
v_mfma_f32_4x4x4_16b_bf16 v[12:15], v[22:23], a[252:253], v[12:15] cbsz:2 abid:0
v_mfma_f32_4x4x4_16b_bf16 v[16:19], v[22:23], a[254:255], v[16:19] cbsz:2 abid:0
v_and_b32_dpp v24, v28, v2 quad_perm:[3,3,3,3] row_mask:0xf bank_mask:0xf
v_and_b32_dpp v25, v28, v3 quad_perm:[3,3,3,3] row_mask:0xf bank_mask:0xf
s_waitcnt vmcnt(8)
s_add_u32 s40, %[rot], 4096
s_and_b32 s40, s40, 28672
s_add_u32 s40, s40, 0
buffer_load_dwordx4 v[216:219], v0, %[rk], s40 offen nt
v_mfma_f32_4x4x4_16b_bf16 v[12:15], v[22:23], v[176:177], v[12:15] cbsz:2 abid:2
v_mfma_f32_4x4x4_16b_bf16 v[16:19], v[22:23], v[178:179], v[16:19] cbsz:2 abid:2
s_waitcnt vmcnt(7)
Lpc35_%=:
s_memrealtime s[56:57]
s_waitcnt lgkmcnt(0)
s_sub_u32 s56, s56, s34
s_cmp_lt_u32 s56, 2006
s_cbranch_scc1 Lpc35_%=
s_add_u32 s40, %[rot], 8192
s_and_b32 s40, s40, 28672
s_add_u32 s40, s40, 0
buffer_load_dwordx4 v[220:223], v0, %[rk], s40 offen nt
v_mfma_f32_4x4x4_16b_bf16 v[12:15], v[22:23], v[180:181], v[12:15] cbsz:2 abid:1
v_mfma_f32_4x4x4_16b_bf16 v[16:19], v[22:23], v[182:183], v[16:19] cbsz:2 abid:1
s_waitcnt vmcnt(7)
s_add_u32 s40, %[rot], 12288
s_and_b32 s40, s40, 28672
s_add_u32 s40, s40, 0
buffer_load_dwordx4 v[224:227], v0, %[rk], s40 offen nt
v_mfma_f32_4x4x4_16b_bf16 v[12:15], v[22:23], v[184:185], v[12:15] cbsz:2 abid:3
v_mfma_f32_4x4x4_16b_bf16 v[16:19], v[22:23], v[186:187], v[16:19] cbsz:2 abid:3
s_waitcnt vmcnt(7)
s_add_u32 s40, %[rot], 16384
s_and_b32 s40, s40, 28672
s_add_u32 s40, s40, 0
buffer_load_dwordx4 v[228:231], v0, %[rk], s40 offen nt
v_mfma_f32_4x4x4_16b_bf16 v[12:15], v[24:25], v[188:189], v[12:15] cbsz:2 abid:0
v_mfma_f32_4x4x4_16b_bf16 v[16:19], v[24:25], v[190:191], v[16:19] cbsz:2 abid:0
s_waitcnt vmcnt(7)
s_add_u32 s40, %[rot], 20480
s_and_b32 s40, s40, 28672
s_add_u32 s40, s40, 0
buffer_load_dwordx4 v[232:235], v0, %[rk], s40 offen nt
v_mfma_f32_4x4x4_16b_bf16 v[12:15], v[24:25], v[192:193], v[12:15] cbsz:2 abid:2
v_mfma_f32_4x4x4_16b_bf16 v[16:19], v[24:25], v[194:195], v[16:19] cbsz:2 abid:2
s_waitcnt vmcnt(7)
Lpc36_%=:
s_memrealtime s[56:57]
s_waitcnt lgkmcnt(0)
s_sub_u32 s56, s56, s34
s_cmp_lt_u32 s56, 2053
s_cbranch_scc1 Lpc36_%=
s_add_u32 s40, %[rot], 24576
s_and_b32 s40, s40, 28672
s_add_u32 s40, s40, 0
buffer_load_dwordx4 v[236:239], v0, %[rk], s40 offen nt
v_mfma_f32_4x4x4_16b_bf16 v[12:15], v[24:25], v[196:197], v[12:15] cbsz:2 abid:1
v_mfma_f32_4x4x4_16b_bf16 v[16:19], v[24:25], v[198:199], v[16:19] cbsz:2 abid:1
s_waitcnt vmcnt(7)
s_add_u32 s40, %[rot], 28672
s_and_b32 s40, s40, 28672
s_add_u32 s40, s40, 0
buffer_load_dwordx4 v[240:243], v0, %[rk], s40 offen nt
v_mfma_f32_4x4x4_16b_bf16 v[12:15], v[24:25], v[200:201], v[12:15] cbsz:2 abid:3
v_mfma_f32_4x4x4_16b_bf16 v[16:19], v[24:25], v[202:203], v[16:19] cbsz:2 abid:3
s_mov_b32 s42, 0
s_branch Le1_%=
Le1ret0_%=:
s_waitcnt vmcnt(7)
s_add_u32 s40, %[rot], 0
s_and_b32 s40, s40, 28672
s_add_u32 s40, s40, 229376
buffer_load_dwordx4 v[244:247], v0, %[rk], s40 offen nt
v_max3_u32 v27, v8, v9, v10
v_max_u32 v27, v27, v11
v_cmp_eq_u32 vcc, -1, v27
s_cbranch_vccnz Lslow_q1b1_%=
Lback_q1b1_%=:
v_dot2_f32_bf16 v31, v212, v8, 0
v_dot2_f32_bf16 v31, v213, v9, v31
v_dot2_f32_bf16 v31, v214, v10, v31
v_dot2_f32_bf16 v31, v215, v11, v31
s_waitcnt vmcnt(7)
s_add_u32 s40, %[rot], 4096
s_and_b32 s40, s40, 28672
s_add_u32 s40, s40, 229376
buffer_load_dwordx4 v[248:251], v0, %[rk], s40 offen nt
v_dot2_f32_bf16 v32, v216, v8, 0
v_dot2_f32_bf16 v32, v217, v9, v32
v_dot2_f32_bf16 v32, v218, v10, v32
v_dot2_f32_bf16 v32, v219, v11, v32
s_waitcnt vmcnt(7)
Lpc37_%=:
s_memrealtime s[56:57]
s_waitcnt lgkmcnt(0)
s_sub_u32 s56, s56, s34
s_cmp_lt_u32 s56, 2100
s_cbranch_scc1 Lpc37_%=
s_add_u32 s40, %[rot], 8192
s_and_b32 s40, s40, 28672
s_add_u32 s40, s40, 229376
buffer_load_dwordx4 v[252:255], v0, %[rk], s40 offen nt
v_dot2_f32_bf16 v33, v220, v8, 0
v_dot2_f32_bf16 v33, v221, v9, v33
v_dot2_f32_bf16 v33, v222, v10, v33
v_dot2_f32_bf16 v33, v223, v11, v33
v_add_f32_dpp v31, v31, v31 row_ror:8 row_mask:0xf bank_mask:0x3
s_waitcnt vmcnt(7)
s_add_u32 s40, %[rot], 12288
s_and_b32 s40, s40, 28672
s_add_u32 s40, s40, 229376
buffer_load_dwordx4 v[212:215], v0, %[rk], s40 offen nt
v_dot2_f32_bf16 v34, v224, v8, 0
v_dot2_f32_bf16 v34, v225, v9, v34
v_dot2_f32_bf16 v34, v226, v10, v34
v_dot2_f32_bf16 v34, v227, v11, v34
v_add_f32_dpp v31, v32, v32 row_ror:8 row_mask:0xf bank_mask:0xc
s_waitcnt vmcnt(7)
s_add_u32 s40, %[rot], 16384
s_and_b32 s40, s40, 28672
s_add_u32 s40, s40, 229376
buffer_load_dwordx4 v[216:219], v0, %[rk], s40 offen nt
v_dot2_f32_bf16 v35, v228, v8, 0
v_dot2_f32_bf16 v35, v229, v9, v35
v_dot2_f32_bf16 v35, v230, v10, v35
v_dot2_f32_bf16 v35, v231, v11, v35
v_add_f32_dpp v33, v33, v33 row_ror:8 row_mask:0xf bank_mask:0x3
s_waitcnt vmcnt(7)
s_add_u32 s40, %[rot], 0
s_and_b32 s40, s40, 28672
s_add_u32 s40, s40, 0
buffer_load_dwordx4 v[220:223], v0, %[rv], s40 offen nt
v_dot2_f32_bf16 v36, v232, v8, 0
v_dot2_f32_bf16 v36, v233, v9, v36
v_dot2_f32_bf16 v36, v234, v10, v36
v_dot2_f32_bf16 v36, v235, v11, v36
v_add_f32_dpp v33, v34, v34 row_ror:8 row_mask:0xf bank_mask:0xc
v_add_f32_dpp v31, v31, v31 row_shl:4 row_mask:0xf bank_mask:0x5
s_waitcnt vmcnt(7)
Lpc38_%=:
s_memrealtime s[56:57]
s_waitcnt lgkmcnt(0)
s_sub_u32 s56, s56, s34
s_cmp_lt_u32 s56, 2148
s_cbranch_scc1 Lpc38_%=
s_add_u32 s40, %[rot], 4096
s_and_b32 s40, s40, 28672
s_add_u32 s40, s40, 0
buffer_load_dwordx4 v[224:227], v0, %[rv], s40 offen
v_dot2_f32_bf16 v37, v236, v8, 0
v_dot2_f32_bf16 v37, v237, v9, v37
v_dot2_f32_bf16 v37, v238, v10, v37
v_dot2_f32_bf16 v37, v239, v11, v37
v_add_f32_dpp v31, v33, v33 row_shr:4 row_mask:0xf bank_mask:0xa
v_add_f32_dpp v35, v35, v35 row_ror:8 row_mask:0xf bank_mask:0x3
s_waitcnt vmcnt(7)
s_add_u32 s40, %[rot], 8192
s_and_b32 s40, s40, 28672
s_add_u32 s40, s40, 0
buffer_load_dwordx4 v[228:231], v0, %[rv], s40 offen nt
buffer_load_dword v228, v0, %[rv], s40 offen sc1
v_dot2_f32_bf16 v32, v240, v8, 0
v_dot2_f32_bf16 v32, v241, v9, v32
v_dot2_f32_bf16 v32, v242, v10, v32
v_dot2_f32_bf16 v32, v243, v11, v32
v_add_f32_dpp v35, v36, v36 row_ror:8 row_mask:0xf bank_mask:0xc
s_waitcnt vmcnt(8)
s_add_u32 s40, %[rot], 12288
s_and_b32 s40, s40, 28672
s_add_u32 s40, s40, 0
buffer_load_dwordx4 v[232:235], v0, %[rv], s40 offen
v_dot2_f32_bf16 v34, v244, v8, 0
v_dot2_f32_bf16 v34, v245, v9, v34
v_dot2_f32_bf16 v34, v246, v10, v34
v_dot2_f32_bf16 v34, v247, v11, v34
v_add_f32_dpp v37, v37, v37 row_ror:8 row_mask:0xf bank_mask:0x3
s_waitcnt vmcnt(8)
s_add_u32 s40, %[rot], 16384
s_and_b32 s40, s40, 28672
s_add_u32 s40, s40, 0
buffer_load_dwordx4 v[236:239], v0, %[rv], s40 offen nt
buffer_load_dword v236, v0, %[rv], s40 offen sc1
v_dot2_f32_bf16 v33, v248, v8, 0
v_dot2_f32_bf16 v33, v249, v9, v33
v_dot2_f32_bf16 v33, v250, v10, v33
v_dot2_f32_bf16 v33, v251, v11, v33
v_add_f32_dpp v37, v32, v32 row_ror:8 row_mask:0xf bank_mask:0xc
v_add_f32_dpp v35, v35, v35 row_shl:4 row_mask:0xf bank_mask:0x5
s_waitcnt vmcnt(9)
Lpc39_%=:
s_memrealtime s[56:57]
s_waitcnt lgkmcnt(0)
s_sub_u32 s56, s56, s34
s_cmp_lt_u32 s56, 2195
s_cbranch_scc1 Lpc39_%=
s_add_u32 s40, %[rot], 20480
s_and_b32 s40, s40, 28672
s_add_u32 s40, s40, 0
buffer_load_dwordx4 v[240:243], v0, %[rv], s40 offen
v_dot2_f32_bf16 v36, v252, v8, 0
v_dot2_f32_bf16 v36, v253, v9, v36
v_dot2_f32_bf16 v36, v254, v10, v36
v_dot2_f32_bf16 v36, v255, v11, v36
v_add_f32_dpp v35, v37, v37 row_shr:4 row_mask:0xf bank_mask:0xa
v_cndmask_b32 v30, v35, v31, s[44:45]
v_cndmask_b32 v31, v31, v35, s[44:45]
s_waitcnt vmcnt(9)
s_add_u32 s40, %[rot], 24576
s_and_b32 s40, s40, 28672
s_add_u32 s40, s40, 0
buffer_load_dwordx4 v[244:247], v0, %[rv], s40 offen nt
buffer_load_dword v244, v0, %[rv], s40 offen sc1
v_dot2_f32_bf16 v32, v212, v8, 0
v_dot2_f32_bf16 v32, v213, v9, v32
v_dot2_f32_bf16 v32, v214, v10, v32
v_dot2_f32_bf16 v32, v215, v11, v32
v_add_f32_dpp v31, v30, v31 quad_perm:[2,3,0,1] row_mask:0xf bank_mask:0xf
v_add_f32_dpp v34, v34, v34 row_ror:8 row_mask:0xf bank_mask:0x3
s_waitcnt vmcnt(10)
s_add_u32 s40, %[rot], 28672
s_and_b32 s40, s40, 28672
s_add_u32 s40, s40, 0
buffer_load_dwordx4 v[248:251], v0, %[rv], s40 offen
v_dot2_f32_bf16 v37, v216, v8, 0
v_dot2_f32_bf16 v37, v217, v9, v37
v_dot2_f32_bf16 v37, v218, v10, v37
v_dot2_f32_bf16 v37, v219, v11, v37
v_add_f32_dpp v34, v33, v33 row_ror:8 row_mask:0xf bank_mask:0xc
v_add_f32_dpp v36, v36, v36 row_ror:8 row_mask:0xf bank_mask:0x3
s_nop 1
v_add_f32_dpp v36, v32, v32 row_ror:8 row_mask:0xf bank_mask:0xc
v_add_f32_dpp v34, v34, v34 row_shl:4 row_mask:0xf bank_mask:0x5
s_nop 1
v_add_f32_dpp v34, v36, v36 row_shr:4 row_mask:0xf bank_mask:0xa
v_add_f32_dpp v37, v37, v37 row_ror:8 row_mask:0xf bank_mask:0x3
s_nop 1
v_add_f32_dpp v37, v37, v37 row_shl:4 row_mask:0xf bank_mask:0x5
v_cndmask_b32 v30, v37, v34, s[44:45]
v_cndmask_b32 v34, v34, v37, s[44:45]
s_nop 1
v_add_f32_dpp v34, v30, v34 quad_perm:[2,3,0,1] row_mask:0xf bank_mask:0xf
v_cndmask_b32 v30, v34, v31, s[46:47]
v_cndmask_b32 v31, v31, v34, s[46:47]
s_nop 1
v_add_f32_dpp v26, v30, v31 quad_perm:[1,0,3,2] row_mask:0xf bank_mask:0xf
v_cvt_pk_bf16_f32 v27, v26, v26
v_and_b32 v26, 0xffff0000, v27
v_mul_f32 v26, s55, v26
v_cvt_pk_bf16_f32 v27, v26, v26
v_and_b32 v26, 0xffff0000, v27
s_mov_b32 s48, 0x777f777f
s_mov_b32 s49, 0x777f777f
v_mov_b32 v27, 0xff800000
v_cndmask_b32 v26, v27, v26, s[48:49]
s_nop 1
v_max_f32_dpp v29, v26, v26 row_ror:8 row_mask:0xf bank_mask:0xf
s_nop 1
v_max_f32_dpp v29, v29, v29 row_ror:4 row_mask:0xf bank_mask:0xf
s_nop 1
v_max_f32_dpp v29, v29, v29 row_ror:2 row_mask:0xf bank_mask:0xf
s_nop 1
v_max_f32_dpp v29, v29, v29 row_ror:1 row_mask:0xf bank_mask:0xf
s_nop 1
v_max_f32_dpp v29, v29, v29 row_bcast:15 row_mask:0xa bank_mask:0xf
s_nop 1
v_max_f32_dpp v29, v29, v29 row_bcast:31 row_mask:0xc bank_mask:0xf
s_nop 0
v_readlane_b32 s50, v29, 63
v_mov_b32 v27, s54
s_nop 0
v_mul_f32 v20, s50, v27
v_fma_f32 v26, v26, s54, -v20
v_exp_f32 v26, v26
s_nop 0
v_cvt_pk_bf16_f32 v28, v26, v26
v_mov_b32 v21, v26
s_nop 0
v_and_b32_dpp v22, v28, v2 quad_perm:[0,0,0,0] row_mask:0xf bank_mask:0xf
v_and_b32_dpp v23, v28, v3 quad_perm:[0,0,0,0] row_mask:0xf bank_mask:0xf
s_waitcnt vmcnt(10)
s_add_u32 s40, %[rot], 0
s_and_b32 s40, s40, 28672
s_add_u32 s40, s40, 229376
buffer_load_dwordx4 v[252:255], v0, %[rv], s40 offen nt
buffer_load_dword v252, v0, %[rv], s40 offen sc1
v_mfma_f32_4x4x4_16b_bf16 v[12:15], v[22:23], v[220:221], 0 cbsz:2 abid:0
v_mfma_f32_4x4x4_16b_bf16 v[16:19], v[22:23], v[222:223], 0 cbsz:2 abid:0
v_and_b32_dpp v24, v28, v2 quad_perm:[2,2,2,2] row_mask:0xf bank_mask:0xf
v_and_b32_dpp v25, v28, v3 quad_perm:[2,2,2,2] row_mask:0xf bank_mask:0xf
s_waitcnt vmcnt(11)
Lpc40_%=:
s_memrealtime s[56:57]
s_waitcnt lgkmcnt(0)
s_sub_u32 s56, s56, s34
s_cmp_lt_u32 s56, 2242
s_cbranch_scc1 Lpc40_%=
s_add_u32 s40, %[rot], 4096
s_and_b32 s40, s40, 28672
s_add_u32 s40, s40, 229376
buffer_load_dwordx4 v[212:215], v0, %[rv], s40 offen
v_mfma_f32_4x4x4_16b_bf16 v[12:15], v[22:23], v[224:225], v[12:15] cbsz:2 abid:2
v_mfma_f32_4x4x4_16b_bf16 v[16:19], v[22:23], v[226:227], v[16:19] cbsz:2 abid:2
s_waitcnt vmcnt(10)
s_add_u32 s40, %[rot], 8192
s_and_b32 s40, s40, 28672
s_add_u32 s40, s40, 229376
buffer_load_dwordx4 v[216:219], v0, %[rv], s40 offen nt
buffer_load_dword v216, v0, %[rv], s40 offen sc1
v_mfma_f32_4x4x4_16b_bf16 v[12:15], v[22:23], v[228:229], v[12:15] cbsz:2 abid:1
v_mfma_f32_4x4x4_16b_bf16 v[16:19], v[22:23], v[230:231], v[16:19] cbsz:2 abid:1
s_waitcnt vmcnt(11)
s_add_u32 s40, %[rot], 12288
s_and_b32 s40, s40, 28672
s_add_u32 s40, s40, 229376
buffer_load_dwordx4 v[220:223], v0, %[rv], s40 offen
v_mfma_f32_4x4x4_16b_bf16 v[12:15], v[22:23], v[232:233], v[12:15] cbsz:2 abid:3
v_mfma_f32_4x4x4_16b_bf16 v[16:19], v[22:23], v[234:235], v[16:19] cbsz:2 abid:3
s_waitcnt vmcnt(10)
s_add_u32 s40, %[rot], 16384
s_and_b32 s40, s40, 28672
s_add_u32 s40, s40, 229376
buffer_load_dwordx4 v[224:227], v0, %[rv], s40 offen nt
buffer_load_dword v224, v0, %[rv], s40 offen sc1
v_mfma_f32_4x4x4_16b_bf16 v[12:15], v[24:25], v[236:237], v[12:15] cbsz:2 abid:0
v_mfma_f32_4x4x4_16b_bf16 v[16:19], v[24:25], v[238:239], v[16:19] cbsz:2 abid:0
v_and_b32_dpp v22, v28, v2 quad_perm:[1,1,1,1] row_mask:0xf bank_mask:0xf
v_and_b32_dpp v23, v28, v3 quad_perm:[1,1,1,1] row_mask:0xf bank_mask:0xf
s_waitcnt vmcnt(11)
Lpc41_%=:
s_memrealtime s[56:57]
s_waitcnt lgkmcnt(0)
s_sub_u32 s56, s56, s34
s_cmp_lt_u32 s56, 2289
s_cbranch_scc1 Lpc41_%=
s_add_u32 s40, %[rot], 20480
s_and_b32 s40, s40, 28672
s_add_u32 s40, s40, 229376
buffer_load_dwordx4 v[228:231], v0, %[rk], s40 offen
v_mfma_f32_4x4x4_16b_bf16 v[12:15], v[24:25], v[240:241], v[12:15] cbsz:2 abid:2
v_mfma_f32_4x4x4_16b_bf16 v[16:19], v[24:25], v[242:243], v[16:19] cbsz:2 abid:2
s_waitcnt vmcnt(10)
s_add_u32 s40, %[rot], 24576
s_and_b32 s40, s40, 28672
s_add_u32 s40, s40, 229376
buffer_load_dwordx4 v[232:235], v0, %[rk], s40 offen nt
buffer_load_dword v232, v0, %[rk], s40 offen sc1
v_mfma_f32_4x4x4_16b_bf16 v[12:15], v[24:25], v[244:245], v[12:15] cbsz:2 abid:1
v_mfma_f32_4x4x4_16b_bf16 v[16:19], v[24:25], v[246:247], v[16:19] cbsz:2 abid:1
s_waitcnt vmcnt(11)
s_add_u32 s40, %[rot], 28672
s_and_b32 s40, s40, 28672
s_add_u32 s40, s40, 229376
buffer_load_dwordx4 v[236:239], v0, %[rk], s40 offen
v_mfma_f32_4x4x4_16b_bf16 v[12:15], v[24:25], v[248:249], v[12:15] cbsz:2 abid:3
v_mfma_f32_4x4x4_16b_bf16 v[16:19], v[24:25], v[250:251], v[16:19] cbsz:2 abid:3
s_waitcnt vmcnt(10)
s_add_u32 s40, %[rot], 0
)ASM"
R"ASM(s_and_b32 s40, s40, 28672
s_add_u32 s40, s40, 458752
buffer_load_dwordx4 v[240:243], v0, %[rk], s40 offen nt
buffer_load_dword v240, v0, %[rk], s40 offen sc1
v_mfma_f32_4x4x4_16b_bf16 v[12:15], v[22:23], v[252:253], v[12:15] cbsz:2 abid:0
v_mfma_f32_4x4x4_16b_bf16 v[16:19], v[22:23], v[254:255], v[16:19] cbsz:2 abid:0
v_and_b32_dpp v24, v28, v2 quad_perm:[3,3,3,3] row_mask:0xf bank_mask:0xf
v_and_b32_dpp v25, v28, v3 quad_perm:[3,3,3,3] row_mask:0xf bank_mask:0xf
s_waitcnt vmcnt(11)
Lpc42_%=:
s_memrealtime s[56:57]
s_waitcnt lgkmcnt(0)
s_sub_u32 s56, s56, s34
s_cmp_lt_u32 s56, 2336
s_cbranch_scc1 Lpc42_%=
s_add_u32 s40, %[rot], 4096
s_and_b32 s40, s40, 28672
s_add_u32 s40, s40, 458752
buffer_load_dwordx4 v[244:247], v0, %[rk], s40 offen
v_mfma_f32_4x4x4_16b_bf16 v[12:15], v[22:23], v[212:213], v[12:15] cbsz:2 abid:2
v_mfma_f32_4x4x4_16b_bf16 v[16:19], v[22:23], v[214:215], v[16:19] cbsz:2 abid:2
s_waitcnt vmcnt(10)
s_add_u32 s40, %[rot], 8192
s_and_b32 s40, s40, 28672
s_add_u32 s40, s40, 458752
buffer_load_dwordx4 v[248:251], v0, %[rk], s40 offen nt
buffer_load_dword v248, v0, %[rk], s40 offen sc1
v_mfma_f32_4x4x4_16b_bf16 v[12:15], v[22:23], v[216:217], v[12:15] cbsz:2 abid:1
v_mfma_f32_4x4x4_16b_bf16 v[16:19], v[22:23], v[218:219], v[16:19] cbsz:2 abid:1
s_waitcnt vmcnt(11)
s_add_u32 s40, %[rot], 12288
s_and_b32 s40, s40, 28672
s_add_u32 s40, s40, 458752
buffer_load_dwordx4 v[252:255], v0, %[rk], s40 offen
v_mfma_f32_4x4x4_16b_bf16 v[12:15], v[22:23], v[220:221], v[12:15] cbsz:2 abid:3
v_mfma_f32_4x4x4_16b_bf16 v[16:19], v[22:23], v[222:223], v[16:19] cbsz:2 abid:3
s_waitcnt vmcnt(10)
s_add_u32 s40, %[rot], 16384
s_and_b32 s40, s40, 28672
s_add_u32 s40, s40, 458752
buffer_load_dwordx4 v[212:215], v0, %[rk], s40 offen nt
buffer_load_dword v212, v0, %[rk], s40 offen sc1
v_mfma_f32_4x4x4_16b_bf16 v[12:15], v[24:25], v[224:225], v[12:15] cbsz:2 abid:0
v_mfma_f32_4x4x4_16b_bf16 v[16:19], v[24:25], v[226:227], v[16:19] cbsz:2 abid:0
s_waitcnt vmcnt(11)
Lpc43_%=:
s_memrealtime s[56:57]
s_waitcnt lgkmcnt(0)
s_sub_u32 s56, s56, s34
s_cmp_lt_u32 s56, 2384
s_cbranch_scc1 Lpc43_%=
s_add_u32 s40, %[rot], 20480
s_and_b32 s40, s40, 28672
s_add_u32 s40, s40, 458752
buffer_load_dwordx4 v[216:219], v0, %[rk], s40 offen
v_dot2_f32_bf16 v31, v228, v8, 0
v_dot2_f32_bf16 v31, v229, v9, v31
v_dot2_f32_bf16 v31, v230, v10, v31
v_dot2_f32_bf16 v31, v231, v11, v31
s_waitcnt vmcnt(10)
s_add_u32 s40, %[rot], 24576
s_and_b32 s40, s40, 28672
s_add_u32 s40, s40, 458752
buffer_load_dwordx4 v[220:223], v0, %[rk], s40 offen nt
buffer_load_dword v220, v0, %[rk], s40 offen sc1
v_dot2_f32_bf16 v32, v232, v8, 0
v_dot2_f32_bf16 v32, v233, v9, v32
v_dot2_f32_bf16 v32, v234, v10, v32
v_dot2_f32_bf16 v32, v235, v11, v32
s_waitcnt vmcnt(11)
s_add_u32 s40, %[rot], 28672
s_and_b32 s40, s40, 28672
s_add_u32 s40, s40, 458752
buffer_load_dwordx4 v[224:227], v0, %[rk], s40 offen
v_dot2_f32_bf16 v33, v236, v8, 0
v_dot2_f32_bf16 v33, v237, v9, v33
v_dot2_f32_bf16 v33, v238, v10, v33
v_dot2_f32_bf16 v33, v239, v11, v33
v_add_f32_dpp v31, v31, v31 row_ror:8 row_mask:0xf bank_mask:0x3
s_waitcnt vmcnt(10)
s_add_u32 s40, %[rot], 0
s_and_b32 s40, s40, 28672
s_add_u32 s40, s40, 688128
buffer_load_dwordx4 v[228:231], v0, %[rk], s40 offen nt
buffer_load_dword v228, v0, %[rk], s40 offen sc1
v_dot2_f32_bf16 v34, v240, v8, 0
v_dot2_f32_bf16 v34, v241, v9, v34
v_dot2_f32_bf16 v34, v242, v10, v34
v_dot2_f32_bf16 v34, v243, v11, v34
v_add_f32_dpp v31, v32, v32 row_ror:8 row_mask:0xf bank_mask:0xc
s_waitcnt vmcnt(11)
Lpc44_%=:
s_memrealtime s[56:57]
s_waitcnt lgkmcnt(0)
s_sub_u32 s56, s56, s34
s_cmp_lt_u32 s56, 2431
s_cbranch_scc1 Lpc44_%=
s_add_u32 s40, %[rot], 4096
s_and_b32 s40, s40, 28672
s_add_u32 s40, s40, 688128
buffer_load_dwordx4 v[232:235], v0, %[rk], s40 offen
v_dot2_f32_bf16 v35, v244, v8, 0
v_dot2_f32_bf16 v35, v245, v9, v35
v_dot2_f32_bf16 v35, v246, v10, v35
v_dot2_f32_bf16 v35, v247, v11, v35
v_add_f32_dpp v33, v33, v33 row_ror:8 row_mask:0xf bank_mask:0x3
s_waitcnt vmcnt(10)
s_add_u32 s40, %[rot], 20480
s_and_b32 s40, s40, 28672
s_add_u32 s40, s40, 229376
buffer_load_dwordx4 v[236:239], v0, %[rv], s40 offen nt
buffer_load_dword v236, v0, %[rv], s40 offen sc1
v_dot2_f32_bf16 v36, v248, v8, 0
v_dot2_f32_bf16 v36, v249, v9, v36
v_dot2_f32_bf16 v36, v250, v10, v36
v_dot2_f32_bf16 v36, v251, v11, v36
v_add_f32_dpp v33, v34, v34 row_ror:8 row_mask:0xf bank_mask:0xc
v_add_f32_dpp v31, v31, v31 row_shl:4 row_mask:0xf bank_mask:0x5
s_waitcnt vmcnt(11)
s_add_u32 s40, %[rot], 24576
s_and_b32 s40, s40, 28672
s_add_u32 s40, s40, 229376
buffer_load_dwordx4 v[240:243], v0, %[rv], s40 offen
v_dot2_f32_bf16 v37, v252, v8, 0
v_dot2_f32_bf16 v37, v253, v9, v37
v_dot2_f32_bf16 v37, v254, v10, v37
v_dot2_f32_bf16 v37, v255, v11, v37
v_add_f32_dpp v31, v33, v33 row_shr:4 row_mask:0xf bank_mask:0xa
v_add_f32_dpp v35, v35, v35 row_ror:8 row_mask:0xf bank_mask:0x3
s_waitcnt vmcnt(10)
s_add_u32 s40, %[rot], 28672
s_and_b32 s40, s40, 28672
s_add_u32 s40, s40, 229376
buffer_load_dwordx4 v[244:247], v0, %[rv], s40 offen nt
buffer_load_dword v244, v0, %[rv], s40 offen sc1
buffer_load_dwordx2 v[4:5], off, %[rw], %[cf2] sc1
v_dot2_f32_bf16 v32, v212, v8, 0
v_dot2_f32_bf16 v32, v213, v9, v32
v_dot2_f32_bf16 v32, v214, v10, v32
v_dot2_f32_bf16 v32, v215, v11, v32
v_add_f32_dpp v35, v36, v36 row_ror:8 row_mask:0xf bank_mask:0xc
s_waitcnt vmcnt(12)
Lpc45_%=:
s_memrealtime s[56:57]
s_waitcnt lgkmcnt(0)
s_sub_u32 s56, s56, s34
s_cmp_lt_u32 s56, 2478
s_cbranch_scc1 Lpc45_%=
s_add_u32 s40, %[rot], 0
s_and_b32 s40, s40, 28672
s_add_u32 s40, s40, 458752
buffer_load_dwordx4 v[248:251], v0, %[rv], s40 offen
v_dot2_f32_bf16 v34, v216, v8, 0
v_dot2_f32_bf16 v34, v217, v9, v34
v_dot2_f32_bf16 v34, v218, v10, v34
v_dot2_f32_bf16 v34, v219, v11, v34
v_add_f32_dpp v37, v37, v37 row_ror:8 row_mask:0xf bank_mask:0x3
s_waitcnt vmcnt(11)
s_add_u32 s40, %[rot], 4096
s_and_b32 s40, s40, 28672
s_add_u32 s40, s40, 458752
buffer_load_dwordx4 v[252:255], v0, %[rv], s40 offen nt
buffer_load_dword v252, v0, %[rv], s40 offen sc1
v_dot2_f32_bf16 v33, v220, v8, 0
v_dot2_f32_bf16 v33, v221, v9, v33
v_dot2_f32_bf16 v33, v222, v10, v33
v_dot2_f32_bf16 v33, v223, v11, v33
v_add_f32_dpp v37, v32, v32 row_ror:8 row_mask:0xf bank_mask:0xc
v_add_f32_dpp v35, v35, v35 row_shl:4 row_mask:0xf bank_mask:0x5
s_waitcnt vmcnt(12)
s_add_u32 s40, %[rot], 8192
s_and_b32 s40, s40, 28672
s_add_u32 s40, s40, 458752
buffer_load_dwordx4 v[212:215], v0, %[rv], s40 offen
v_dot2_f32_bf16 v36, v224, v8, 0
v_dot2_f32_bf16 v36, v225, v9, v36
v_dot2_f32_bf16 v36, v226, v10, v36
v_dot2_f32_bf16 v36, v227, v11, v36
v_add_f32_dpp v35, v37, v37 row_shr:4 row_mask:0xf bank_mask:0xa
v_cndmask_b32 v30, v35, v31, s[44:45]
v_cndmask_b32 v31, v31, v35, s[44:45]
s_waitcnt vmcnt(11)
s_add_u32 s40, %[rot], 12288
s_and_b32 s40, s40, 28672
s_add_u32 s40, s40, 458752
buffer_load_dwordx4 v[216:219], v0, %[rv], s40 offen nt
buffer_load_dword v216, v0, %[rv], s40 offen sc1
v_dot2_f32_bf16 v32, v228, v8, 0
v_dot2_f32_bf16 v32, v229, v9, v32
v_dot2_f32_bf16 v32, v230, v10, v32
v_dot2_f32_bf16 v32, v231, v11, v32
v_add_f32_dpp v31, v30, v31 quad_perm:[2,3,0,1] row_mask:0xf bank_mask:0xf
v_add_f32_dpp v34, v34, v34 row_ror:8 row_mask:0xf bank_mask:0x3
s_waitcnt vmcnt(12)
Lpc46_%=:
s_memrealtime s[56:57]
s_waitcnt lgkmcnt(0)
s_sub_u32 s56, s56, s34
s_cmp_lt_u32 s56, 2525
s_cbranch_scc1 Lpc46_%=
s_add_u32 s40, %[rot], 16384
s_and_b32 s40, s40, 28672
s_add_u32 s40, s40, 458752
buffer_load_dwordx4 v[220:223], v0, %[rv], s40 offen
v_dot2_f32_bf16 v37, v232, v8, 0
v_dot2_f32_bf16 v37, v233, v9, v37
v_dot2_f32_bf16 v37, v234, v10, v37
v_dot2_f32_bf16 v37, v235, v11, v37
v_add_f32_dpp v34, v33, v33 row_ror:8 row_mask:0xf bank_mask:0xc
v_add_f32_dpp v36, v36, v36 row_ror:8 row_mask:0xf bank_mask:0x3
s_nop 1
v_add_f32_dpp v36, v32, v32 row_ror:8 row_mask:0xf bank_mask:0xc
v_add_f32_dpp v34, v34, v34 row_shl:4 row_mask:0xf bank_mask:0x5
s_nop 1
v_add_f32_dpp v34, v36, v36 row_shr:4 row_mask:0xf bank_mask:0xa
v_add_f32_dpp v37, v37, v37 row_ror:8 row_mask:0xf bank_mask:0x3
s_nop 1
v_add_f32_dpp v37, v37, v37 row_shl:4 row_mask:0xf bank_mask:0x5
v_cndmask_b32 v30, v37, v34, s[44:45]
v_cndmask_b32 v34, v34, v37, s[44:45]
s_nop 1
v_add_f32_dpp v34, v30, v34 quad_perm:[2,3,0,1] row_mask:0xf bank_mask:0xf
v_cndmask_b32 v30, v34, v31, s[46:47]
v_cndmask_b32 v31, v31, v34, s[46:47]
s_nop 1
v_add_f32_dpp v26, v30, v31 quad_perm:[1,0,3,2] row_mask:0xf bank_mask:0xf
v_cvt_pk_bf16_f32 v27, v26, v26
v_and_b32 v26, 0xffff0000, v27
v_mul_f32 v26, s55, v26
v_cvt_pk_bf16_f32 v27, v26, v26
v_and_b32 v26, 0xffff0000, v27
s_mov_b32 s48, 0x777f777f
s_mov_b32 s49, 0x777f777f
v_mov_b32 v27, 0xff800000
v_cndmask_b32 v26, v27, v26, s[48:49]
s_nop 1
v_max_f32_dpp v29, v26, v26 row_ror:8 row_mask:0xf bank_mask:0xf
s_nop 1
v_max_f32_dpp v29, v29, v29 row_ror:4 row_mask:0xf bank_mask:0xf
s_nop 1
v_max_f32_dpp v29, v29, v29 row_ror:2 row_mask:0xf bank_mask:0xf
s_nop 1
v_max_f32_dpp v29, v29, v29 row_ror:1 row_mask:0xf bank_mask:0xf
s_nop 1
v_max_f32_dpp v29, v29, v29 row_bcast:15 row_mask:0xa bank_mask:0xf
s_nop 1
v_max_f32_dpp v29, v29, v29 row_bcast:31 row_mask:0xc bank_mask:0xf
s_nop 0
v_readlane_b32 s50, v29, 63
v_mov_b32 v27, s54
s_nop 0
v_mul_f32 v27, s50, v27
v_max_f32 v30, v27, v20
v_sub_f32 v29, v20, v30
v_mov_b32 v20, v30
v_exp_f32 v29, v29
v_fma_f32 v26, v26, s54, -v20
v_exp_f32 v26, v26
v_mul_f32 v12, v29, v12
v_mul_f32 v13, v29, v13
v_mul_f32 v14, v29, v14
v_mul_f32 v15, v29, v15
v_mul_f32 v16, v29, v16
v_mul_f32 v17, v29, v17
v_mul_f32 v18, v29, v18
v_mul_f32 v19, v29, v19
v_cvt_pk_bf16_f32 v28, v26, v26
v_fma_f32 v21, v21, v29, v26
s_nop 0
v_and_b32_dpp v22, v28, v2 quad_perm:[0,0,0,0] row_mask:0xf bank_mask:0xf
v_and_b32_dpp v23, v28, v3 quad_perm:[0,0,0,0] row_mask:0xf bank_mask:0xf
s_waitcnt vmcnt(11)
s_add_u32 s40, %[rot], 20480
s_and_b32 s40, s40, 28672
s_add_u32 s40, s40, 458752
buffer_load_dwordx4 v[224:227], v0, %[rv], s40 offen nt
buffer_load_dword v224, v0, %[rv], s40 offen sc1
v_mfma_f32_4x4x4_16b_bf16 v[12:15], v[22:23], v[236:237], v[12:15] cbsz:2 abid:0
v_mfma_f32_4x4x4_16b_bf16 v[16:19], v[22:23], v[238:239], v[16:19] cbsz:2 abid:0
v_and_b32_dpp v24, v28, v2 quad_perm:[2,2,2,2] row_mask:0xf bank_mask:0xf
v_and_b32_dpp v25, v28, v3 quad_perm:[2,2,2,2] row_mask:0xf bank_mask:0xf
s_waitcnt vmcnt(12)
s_add_u32 s40, %[rot], 24576
s_and_b32 s40, s40, 28672
s_add_u32 s40, s40, 458752
buffer_load_dwordx4 v[228:231], v0, %[rv], s40 offen
v_mfma_f32_4x4x4_16b_bf16 v[12:15], v[22:23], v[240:241], v[12:15] cbsz:2 abid:2
v_mfma_f32_4x4x4_16b_bf16 v[16:19], v[22:23], v[242:243], v[16:19] cbsz:2 abid:2
s_waitcnt vmcnt(11)
s_add_u32 s40, %[rot], 28672
s_and_b32 s40, s40, 28672
s_add_u32 s40, s40, 458752
buffer_load_dwordx4 v[232:235], v0, %[rv], s40 offen nt
buffer_load_dword v232, v0, %[rv], s40 offen sc1
v_mfma_f32_4x4x4_16b_bf16 v[12:15], v[22:23], v[244:245], v[12:15] cbsz:2 abid:1
v_mfma_f32_4x4x4_16b_bf16 v[16:19], v[22:23], v[246:247], v[16:19] cbsz:2 abid:1
s_waitcnt vmcnt(11)
Lpc47_%=:
s_memrealtime s[56:57]
s_waitcnt lgkmcnt(0)
s_sub_u32 s56, s56, s34
s_cmp_lt_u32 s56, 2572
s_cbranch_scc1 Lpc47_%=
s_add_u32 s40, %[rot], 0
s_and_b32 s40, s40, 28672
s_add_u32 s40, s40, 688128
buffer_load_dwordx4 v[236:239], v0, %[rv], s40 offen
v_xor_b32 v4, s58, v4
v_xor_b32 v5, s59, v5
v_or_b32 v4, v4, v5
v_cmp_ne_u32 vcc, 0, v4
s_cbranch_vccnz Lslow_cf2_%=
Lback_cf2_%=:
v_mfma_f32_4x4x4_16b_bf16 v[12:15], v[22:23], v[248:249], v[12:15] cbsz:2 abid:3
v_mfma_f32_4x4x4_16b_bf16 v[16:19], v[22:23], v[250:251], v[16:19] cbsz:2 abid:3
s_waitcnt vmcnt(10)
s_add_u32 s40, %[rot], 4096
s_and_b32 s40, s40, 28672
s_add_u32 s40, s40, 688128
buffer_load_dwordx4 v[240:243], v0, %[rv], s40 offen nt
buffer_load_dword v240, v0, %[rv], s40 offen sc1
s_add_u32 s41, s33, 262144
buffer_load_dwordx4 v[4:7], v39, %[rw], s41 offen sc1
v_mfma_f32_4x4x4_16b_bf16 v[12:15], v[24:25], v[252:253], v[12:15] cbsz:2 abid:0
v_mfma_f32_4x4x4_16b_bf16 v[16:19], v[24:25], v[254:255], v[16:19] cbsz:2 abid:0
v_and_b32_dpp v22, v28, v2 quad_perm:[1,1,1,1] row_mask:0xf bank_mask:0xf
v_and_b32_dpp v23, v28, v3 quad_perm:[1,1,1,1] row_mask:0xf bank_mask:0xf
s_waitcnt vmcnt(12)
s_add_u32 s40, %[rot], 8192
s_and_b32 s40, s40, 28672
s_add_u32 s40, s40, 688128
buffer_load_dwordx4 v[244:247], v0, %[rk], s40 offen nt
v_mfma_f32_4x4x4_16b_bf16 v[12:15], v[24:25], v[212:213], v[12:15] cbsz:2 abid:2
v_mfma_f32_4x4x4_16b_bf16 v[16:19], v[24:25], v[214:215], v[16:19] cbsz:2 abid:2
s_waitcnt vmcnt(11)
s_add_u32 s40, %[rot], 12288
s_and_b32 s40, s40, 28672
s_add_u32 s40, s40, 688128
buffer_load_dwordx4 v[248:251], v0, %[rk], s40 offen nt
v_mfma_f32_4x4x4_16b_bf16 v[12:15], v[24:25], v[216:217], v[12:15] cbsz:2 abid:1
v_mfma_f32_4x4x4_16b_bf16 v[16:19], v[24:25], v[218:219], v[16:19] cbsz:2 abid:1
s_waitcnt vmcnt(11)
Lpc48_%=:
s_memrealtime s[56:57]
s_waitcnt lgkmcnt(0)
s_sub_u32 s56, s56, s34
s_cmp_lt_u32 s56, 2620
s_cbranch_scc1 Lpc48_%=
s_add_u32 s40, %[rot], 16384
s_and_b32 s40, s40, 28672
s_add_u32 s40, s40, 688128
buffer_load_dwordx4 v[252:255], v0, %[rk], s40 offen nt
v_mfma_f32_4x4x4_16b_bf16 v[12:15], v[24:25], v[220:221], v[12:15] cbsz:2 abid:3
v_mfma_f32_4x4x4_16b_bf16 v[16:19], v[24:25], v[222:223], v[16:19] cbsz:2 abid:3
s_waitcnt vmcnt(10)
s_add_u32 s40, %[rot], 20480
s_and_b32 s40, s40, 28672
s_add_u32 s40, s40, 688128
buffer_load_dwordx4 v[212:215], v0, %[rk], s40 offen nt
v_mfma_f32_4x4x4_16b_bf16 v[12:15], v[22:23], v[224:225], v[12:15] cbsz:2 abid:0
v_mfma_f32_4x4x4_16b_bf16 v[16:19], v[22:23], v[226:227], v[16:19] cbsz:2 abid:0
v_and_b32_dpp v24, v28, v2 quad_perm:[3,3,3,3] row_mask:0xf bank_mask:0xf
v_and_b32_dpp v25, v28, v3 quad_perm:[3,3,3,3] row_mask:0xf bank_mask:0xf
s_waitcnt vmcnt(10)
s_add_u32 s40, %[rot], 24576
s_and_b32 s40, s40, 28672
s_add_u32 s40, s40, 688128
buffer_load_dwordx4 v[216:219], v0, %[rk], s40 offen nt
v_mfma_f32_4x4x4_16b_bf16 v[12:15], v[22:23], v[228:229], v[12:15] cbsz:2 abid:2
v_mfma_f32_4x4x4_16b_bf16 v[16:19], v[22:23], v[230:231], v[16:19] cbsz:2 abid:2
s_waitcnt vmcnt(9)
s_add_u32 s40, %[rot], 28672
s_and_b32 s40, s40, 28672
s_add_u32 s40, s40, 688128
buffer_load_dwordx4 v[220:223], v0, %[rk], s40 offen nt
v_mfma_f32_4x4x4_16b_bf16 v[12:15], v[22:23], v[232:233], v[12:15] cbsz:2 abid:1
v_mfma_f32_4x4x4_16b_bf16 v[16:19], v[22:23], v[234:235], v[16:19] cbsz:2 abid:1
s_waitcnt vmcnt(9)
Lpc49_%=:
s_memrealtime s[56:57]
s_waitcnt lgkmcnt(0)
s_sub_u32 s56, s56, s34
s_cmp_lt_u32 s56, 2667
s_cbranch_scc1 Lpc49_%=
s_add_u32 s40, %[rot], 0
s_and_b32 s40, s40, 28672
s_add_u32 s40, s40, 917504
buffer_load_dwordx4 v[224:227], v0, %[rk], s40 offen nt
v_mfma_f32_4x4x4_16b_bf16 v[12:15], v[22:23], v[236:237], v[12:15] cbsz:2 abid:3
v_mfma_f32_4x4x4_16b_bf16 v[16:19], v[22:23], v[238:239], v[16:19] cbsz:2 abid:3
s_waitcnt vmcnt(8)
s_add_u32 s40, %[rot], 4096
s_and_b32 s40, s40, 28672
s_add_u32 s40, s40, 917504
buffer_load_dwordx4 v[228:231], v0, %[rk], s40 offen nt
v_mfma_f32_4x4x4_16b_bf16 v[12:15], v[24:25], v[240:241], v[12:15] cbsz:2 abid:0
v_mfma_f32_4x4x4_16b_bf16 v[16:19], v[24:25], v[242:243], v[16:19] cbsz:2 abid:0
s_waitcnt vmcnt(7)
s_add_u32 s40, %[rot], 8192
s_and_b32 s40, s40, 28672
)ASM"
R"ASM(s_add_u32 s40, s40, 917504
buffer_load_dwordx4 v[232:235], v0, %[rk], s40 offen nt
s_waitcnt vmcnt(7)
s_add_u32 s40, %[rot], 12288
s_and_b32 s40, s40, 28672
s_add_u32 s40, s40, 917504
buffer_load_dwordx4 v[236:239], v0, %[rk], s40 offen nt
s_waitcnt vmcnt(7)
Lpc50_%=:
s_memrealtime s[56:57]
s_waitcnt lgkmcnt(0)
s_sub_u32 s56, s56, s34
s_cmp_lt_u32 s56, 2714
s_cbranch_scc1 Lpc50_%=
s_add_u32 s40, %[rot], 16384
s_and_b32 s40, s40, 28672
s_add_u32 s40, s40, 917504
buffer_load_dwordx4 v[240:243], v0, %[rk], s40 offen nt
v_max3_u32 v27, v4, v5, v6
v_max_u32 v27, v27, v7
v_cmp_eq_u32 vcc, -1, v27
s_cbranch_vccnz Lslow_q2b0_%=
Lback_q2b0_%=:
s_branch Lp2_%=
Lp2ret0_%=:
v_dot2_f32_bf16 v31, v244, v8, 0
v_dot2_f32_bf16 v31, v245, v9, v31
v_dot2_f32_bf16 v31, v246, v10, v31
v_dot2_f32_bf16 v31, v247, v11, v31
v_mov_b32 v40, v244
v_mov_b32 v41, v245
v_mov_b32 v42, v246
v_mov_b32 v43, v247
s_waitcnt vmcnt(15)
s_memrealtime s[56:57]
s_waitcnt lgkmcnt(0)
s_sub_u32 s56, s56, s34
s_cmp_lt_u32 s56, 3253
s_cbranch_scc0 Lfa234_%=
s_waitcnt vmcnt(4)
Lfa234_%=:
s_add_u32 s40, %[rot], 24576
s_and_b32 s40, s40, 28672
s_add_u32 s40, s40, 917504
buffer_load_dwordx4 a[204:207], v0, %[rv], s40 offen nt
v_dot2_f32_bf16 v32, v248, v8, 0
v_dot2_f32_bf16 v32, v249, v9, v32
v_dot2_f32_bf16 v32, v250, v10, v32
v_dot2_f32_bf16 v32, v251, v11, v32
v_mov_b32 v44, v248
v_mov_b32 v45, v249
v_mov_b32 v46, v250
v_mov_b32 v47, v251
v_dot2_f32_bf16 v33, v252, v8, 0
v_dot2_f32_bf16 v33, v253, v9, v33
v_dot2_f32_bf16 v33, v254, v10, v33
v_dot2_f32_bf16 v33, v255, v11, v33
v_add_f32_dpp v31, v31, v31 row_ror:8 row_mask:0xf bank_mask:0x3
v_mov_b32 v48, v252
v_mov_b32 v49, v253
v_mov_b32 v50, v254
v_mov_b32 v51, v255
v_dot2_f32_bf16 v34, v212, v8, 0
v_dot2_f32_bf16 v34, v213, v9, v34
v_dot2_f32_bf16 v34, v214, v10, v34
v_dot2_f32_bf16 v34, v215, v11, v34
v_add_f32_dpp v31, v32, v32 row_ror:8 row_mask:0xf bank_mask:0xc
v_mov_b32 v52, v212
v_mov_b32 v53, v213
v_mov_b32 v54, v214
v_mov_b32 v55, v215
v_dot2_f32_bf16 v35, v216, v8, 0
v_dot2_f32_bf16 v35, v217, v9, v35
v_dot2_f32_bf16 v35, v218, v10, v35
v_dot2_f32_bf16 v35, v219, v11, v35
v_add_f32_dpp v33, v33, v33 row_ror:8 row_mask:0xf bank_mask:0x3
v_mov_b32 v56, v216
v_mov_b32 v57, v217
v_mov_b32 v58, v218
v_mov_b32 v59, v219
v_dot2_f32_bf16 v36, v220, v8, 0
v_dot2_f32_bf16 v36, v221, v9, v36
v_dot2_f32_bf16 v36, v222, v10, v36
v_dot2_f32_bf16 v36, v223, v11, v36
v_add_f32_dpp v33, v34, v34 row_ror:8 row_mask:0xf bank_mask:0xc
v_add_f32_dpp v31, v31, v31 row_shl:4 row_mask:0xf bank_mask:0x5
v_mov_b32 v60, v220
v_mov_b32 v61, v221
v_mov_b32 v62, v222
v_mov_b32 v63, v223
v_dot2_f32_bf16 v37, v224, v8, 0
v_dot2_f32_bf16 v37, v225, v9, v37
v_dot2_f32_bf16 v37, v226, v10, v37
v_dot2_f32_bf16 v37, v227, v11, v37
v_add_f32_dpp v31, v33, v33 row_shr:4 row_mask:0xf bank_mask:0xa
v_add_f32_dpp v35, v35, v35 row_ror:8 row_mask:0xf bank_mask:0x3
v_mov_b32 v64, v224
v_mov_b32 v65, v225
v_mov_b32 v66, v226
v_mov_b32 v67, v227
s_waitcnt vmcnt(15)
s_add_u32 s40, %[rot], 28672
s_and_b32 s40, s40, 28672
s_add_u32 s40, s40, 917504
buffer_load_dwordx4 a[208:211], v0, %[rv], s40 offen nt
v_dot2_f32_bf16 v32, v228, v8, 0
v_dot2_f32_bf16 v32, v229, v9, v32
v_dot2_f32_bf16 v32, v230, v10, v32
v_dot2_f32_bf16 v32, v231, v11, v32
v_add_f32_dpp v35, v36, v36 row_ror:8 row_mask:0xf bank_mask:0xc
v_mov_b32 v68, v228
v_mov_b32 v69, v229
v_mov_b32 v70, v230
v_mov_b32 v71, v231
v_dot2_f32_bf16 v34, v232, v8, 0
v_dot2_f32_bf16 v34, v233, v9, v34
v_dot2_f32_bf16 v34, v234, v10, v34
v_dot2_f32_bf16 v34, v235, v11, v34
v_add_f32_dpp v37, v37, v37 row_ror:8 row_mask:0xf bank_mask:0x3
v_mov_b32 v72, v232
v_mov_b32 v73, v233
v_mov_b32 v74, v234
v_mov_b32 v75, v235
v_dot2_f32_bf16 v33, v236, v8, 0
v_dot2_f32_bf16 v33, v237, v9, v33
v_dot2_f32_bf16 v33, v238, v10, v33
v_dot2_f32_bf16 v33, v239, v11, v33
v_add_f32_dpp v37, v32, v32 row_ror:8 row_mask:0xf bank_mask:0xc
v_add_f32_dpp v35, v35, v35 row_shl:4 row_mask:0xf bank_mask:0x5
v_mov_b32 v76, v236
v_mov_b32 v77, v237
v_mov_b32 v78, v238
v_mov_b32 v79, v239
v_dot2_f32_bf16 v36, v240, v8, 0
v_dot2_f32_bf16 v36, v241, v9, v36
v_dot2_f32_bf16 v36, v242, v10, v36
v_dot2_f32_bf16 v36, v243, v11, v36
v_add_f32_dpp v35, v37, v37 row_shr:4 row_mask:0xf bank_mask:0xa
v_cndmask_b32 v30, v35, v31, s[44:45]
v_cndmask_b32 v31, v31, v35, s[44:45]
v_mov_b32 v80, v240
v_mov_b32 v81, v241
v_mov_b32 v82, v242
v_mov_b32 v83, v243
v_dot2_f32_bf16 v32, v84, v8, 0
v_dot2_f32_bf16 v32, v85, v9, v32
v_dot2_f32_bf16 v32, v86, v10, v32
v_dot2_f32_bf16 v32, v87, v11, v32
v_add_f32_dpp v31, v30, v31 quad_perm:[2,3,0,1] row_mask:0xf bank_mask:0xf
v_add_f32_dpp v34, v34, v34 row_ror:8 row_mask:0xf bank_mask:0x3
v_dot2_f32_bf16 v37, v88, v8, 0
v_dot2_f32_bf16 v37, v89, v9, v37
v_dot2_f32_bf16 v37, v90, v10, v37
v_dot2_f32_bf16 v37, v91, v11, v37
v_add_f32_dpp v34, v33, v33 row_ror:8 row_mask:0xf bank_mask:0xc
v_add_f32_dpp v36, v36, v36 row_ror:8 row_mask:0xf bank_mask:0x3
s_waitcnt vmcnt(15)
s_memrealtime s[56:57]
s_waitcnt lgkmcnt(0)
s_sub_u32 s56, s56, s34
s_cmp_lt_u32 s56, 3278
s_cbranch_scc0 Lfa236_%=
s_waitcnt vmcnt(4)
Lfa236_%=:
s_add_u32 s40, %[rot], 0
s_and_b32 s40, s40, 28672
s_add_u32 s40, s40, 1146880
buffer_load_dwordx4 a[212:215], v0, %[rv], s40 offen nt
v_dot2_f32_bf16 v35, v92, v8, 0
v_dot2_f32_bf16 v35, v93, v9, v35
v_dot2_f32_bf16 v35, v94, v10, v35
v_dot2_f32_bf16 v35, v95, v11, v35
v_add_f32_dpp v36, v32, v32 row_ror:8 row_mask:0xf bank_mask:0xc
v_add_f32_dpp v34, v34, v34 row_shl:4 row_mask:0xf bank_mask:0x5
v_dot2_f32_bf16 v33, v96, v8, 0
v_dot2_f32_bf16 v33, v97, v9, v33
v_dot2_f32_bf16 v33, v98, v10, v33
v_dot2_f32_bf16 v33, v99, v11, v33
v_add_f32_dpp v34, v36, v36 row_shr:4 row_mask:0xf bank_mask:0xa
v_add_f32_dpp v37, v37, v37 row_ror:8 row_mask:0xf bank_mask:0x3
v_dot2_f32_bf16 v32, v100, v8, 0
v_dot2_f32_bf16 v32, v101, v9, v32
v_dot2_f32_bf16 v32, v102, v10, v32
v_dot2_f32_bf16 v32, v103, v11, v32
v_add_f32_dpp v37, v35, v35 row_ror:8 row_mask:0xf bank_mask:0xc
v_add_f32_dpp v33, v33, v33 row_ror:8 row_mask:0xf bank_mask:0x3
s_nop 1
v_add_f32_dpp v33, v32, v32 row_ror:8 row_mask:0xf bank_mask:0xc
v_add_f32_dpp v37, v37, v37 row_shl:4 row_mask:0xf bank_mask:0x5
s_nop 1
v_add_f32_dpp v37, v33, v33 row_shr:4 row_mask:0xf bank_mask:0xa
v_cndmask_b32 v30, v37, v34, s[44:45]
v_cndmask_b32 v34, v34, v37, s[44:45]
s_nop 1
v_add_f32_dpp v34, v30, v34 quad_perm:[2,3,0,1] row_mask:0xf bank_mask:0xf
v_cndmask_b32 v30, v34, v31, s[46:47]
v_cndmask_b32 v31, v31, v34, s[46:47]
s_nop 1
v_add_f32_dpp v26, v30, v31 quad_perm:[1,0,3,2] row_mask:0xf bank_mask:0xf
v_cvt_pk_bf16_f32 v27, v26, v26
v_and_b32 v26, 0xffff0000, v27
v_mul_f32 v26, s55, v26
v_cvt_pk_bf16_f32 v27, v26, v26
v_and_b32 v26, 0xffff0000, v27
s_nop 1
v_max_f32_dpp v29, v26, v26 row_ror:8 row_mask:0xf bank_mask:0xf
s_nop 1
v_max_f32_dpp v29, v29, v29 row_ror:4 row_mask:0xf bank_mask:0xf
s_nop 1
v_max_f32_dpp v29, v29, v29 row_ror:2 row_mask:0xf bank_mask:0xf
s_nop 1
v_max_f32_dpp v29, v29, v29 row_ror:1 row_mask:0xf bank_mask:0xf
s_nop 1
v_max_f32_dpp v29, v29, v29 row_bcast:15 row_mask:0xa bank_mask:0xf
s_nop 1
v_max_f32_dpp v29, v29, v29 row_bcast:31 row_mask:0xc bank_mask:0xf
s_nop 0
v_readlane_b32 s50, v29, 63
v_mov_b32 v27, s54
s_nop 0
v_mul_f32 v27, s50, v27
v_max_f32 v30, v27, v20
v_sub_f32 v29, v20, v30
v_mov_b32 v20, v30
v_exp_f32 v29, v29
v_fma_f32 v26, v26, s54, -v20
v_exp_f32 v26, v26
v_mul_f32 v12, v29, v12
v_mul_f32 v13, v29, v13
v_mul_f32 v14, v29, v14
v_mul_f32 v15, v29, v15
v_mul_f32 v16, v29, v16
v_mul_f32 v17, v29, v17
v_mul_f32 v18, v29, v18
v_mul_f32 v19, v29, v19
v_cvt_pk_bf16_f32 v28, v26, v26
v_fma_f32 v21, v21, v29, v26
s_nop 0
v_and_b32_dpp v22, v28, v2 quad_perm:[0,0,0,0] row_mask:0xf bank_mask:0xf
v_and_b32_dpp v23, v28, v3 quad_perm:[0,0,0,0] row_mask:0xf bank_mask:0xf
s_waitcnt vmcnt(15)
s_add_u32 s40, %[rot], 4096
s_and_b32 s40, s40, 28672
s_add_u32 s40, s40, 1146880
buffer_load_dwordx4 a[216:219], v0, %[rv], s40 offen nt
s_waitcnt vmcnt(15)
s_memrealtime s[56:57]
s_waitcnt lgkmcnt(0)
s_sub_u32 s56, s56, s34
s_cmp_lt_u32 s56, 3304
s_cbranch_scc0 Lfa238_%=
s_waitcnt vmcnt(4)
Lfa238_%=:
s_add_u32 s40, %[rot], 8192
s_and_b32 s40, s40, 28672
s_add_u32 s40, s40, 1376256
buffer_load_dwordx4 v[136:139], v0, %[rk], s40 offen nt
v_mfma_f32_4x4x4_16b_bf16 v[12:15], v[22:23], a[156:157], v[12:15] cbsz:2 abid:0
v_mfma_f32_4x4x4_16b_bf16 v[16:19], v[22:23], a[158:159], v[16:19] cbsz:2 abid:0
v_and_b32_dpp v24, v28, v2 quad_perm:[2,2,2,2] row_mask:0xf bank_mask:0xf
v_and_b32_dpp v25, v28, v3 quad_perm:[2,2,2,2] row_mask:0xf bank_mask:0xf
s_waitcnt vmcnt(15)
s_add_u32 s40, %[rot], 12288
s_and_b32 s40, s40, 28672
s_add_u32 s40, s40, 1376256
buffer_load_dwordx4 v[140:143], v0, %[rk], s40 offen nt
v_mfma_f32_4x4x4_16b_bf16 v[12:15], v[22:23], a[160:161], v[12:15] cbsz:2 abid:2
v_mfma_f32_4x4x4_16b_bf16 v[16:19], v[22:23], a[162:163], v[16:19] cbsz:2 abid:2
s_waitcnt vmcnt(15)
s_memrealtime s[56:57]
s_waitcnt lgkmcnt(0)
s_sub_u32 s56, s56, s34
s_cmp_lt_u32 s56, 3329
s_cbranch_scc0 Lfa240_%=
s_waitcnt vmcnt(4)
Lfa240_%=:
s_add_u32 s40, %[rot], 16384
s_and_b32 s40, s40, 28672
s_add_u32 s40, s40, 1376256
buffer_load_dwordx4 v[144:147], v0, %[rk], s40 offen nt
v_mfma_f32_4x4x4_16b_bf16 v[12:15], v[22:23], a[164:165], v[12:15] cbsz:2 abid:1
v_mfma_f32_4x4x4_16b_bf16 v[16:19], v[22:23], a[166:167], v[16:19] cbsz:2 abid:1
s_waitcnt vmcnt(15)
s_add_u32 s40, %[rot], 20480
s_and_b32 s40, s40, 28672
s_add_u32 s40, s40, 1376256
buffer_load_dwordx4 v[148:151], v0, %[rk], s40 offen nt
v_mfma_f32_4x4x4_16b_bf16 v[12:15], v[22:23], a[168:169], v[12:15] cbsz:2 abid:3
v_mfma_f32_4x4x4_16b_bf16 v[16:19], v[22:23], a[170:171], v[16:19] cbsz:2 abid:3
s_waitcnt vmcnt(15)
s_memrealtime s[56:57]
s_waitcnt lgkmcnt(0)
s_sub_u32 s56, s56, s34
s_cmp_lt_u32 s56, 3355
s_cbranch_scc0 Lfa242_%=
s_waitcnt vmcnt(4)
Lfa242_%=:
s_add_u32 s40, %[rot], 24576
s_and_b32 s40, s40, 28672
s_add_u32 s40, s40, 1376256
buffer_load_dwordx4 v[152:155], v0, %[rk], s40 offen nt
v_mfma_f32_4x4x4_16b_bf16 v[12:15], v[24:25], a[172:173], v[12:15] cbsz:2 abid:0
v_mfma_f32_4x4x4_16b_bf16 v[16:19], v[24:25], a[174:175], v[16:19] cbsz:2 abid:0
v_and_b32_dpp v22, v28, v2 quad_perm:[1,1,1,1] row_mask:0xf bank_mask:0xf
v_and_b32_dpp v23, v28, v3 quad_perm:[1,1,1,1] row_mask:0xf bank_mask:0xf
s_waitcnt vmcnt(15)
s_add_u32 s40, %[rot], 28672
s_and_b32 s40, s40, 28672
s_add_u32 s40, s40, 1376256
buffer_load_dwordx4 v[156:159], v0, %[rk], s40 offen nt
v_mfma_f32_4x4x4_16b_bf16 v[12:15], v[24:25], a[176:177], v[12:15] cbsz:2 abid:2
v_mfma_f32_4x4x4_16b_bf16 v[16:19], v[24:25], a[178:179], v[16:19] cbsz:2 abid:2
s_waitcnt vmcnt(15)
s_memrealtime s[56:57]
s_waitcnt lgkmcnt(0)
s_sub_u32 s56, s56, s34
s_cmp_lt_u32 s56, 3380
s_cbranch_scc0 Lfa244_%=
s_waitcnt vmcnt(4)
Lfa244_%=:
s_add_u32 s40, %[rot], 0
s_and_b32 s40, s40, 28672
s_add_u32 s40, s40, 1605632
buffer_load_dwordx4 v[160:163], v0, %[rk], s40 offen nt
v_mfma_f32_4x4x4_16b_bf16 v[12:15], v[24:25], a[180:181], v[12:15] cbsz:2 abid:1
v_mfma_f32_4x4x4_16b_bf16 v[16:19], v[24:25], a[182:183], v[16:19] cbsz:2 abid:1
s_waitcnt vmcnt(15)
s_add_u32 s40, %[rot], 4096
s_and_b32 s40, s40, 28672
s_add_u32 s40, s40, 1605632
buffer_load_dwordx4 v[164:167], v0, %[rk], s40 offen nt
v_mfma_f32_4x4x4_16b_bf16 v[12:15], v[24:25], a[184:185], v[12:15] cbsz:2 abid:3
v_mfma_f32_4x4x4_16b_bf16 v[16:19], v[24:25], a[186:187], v[16:19] cbsz:2 abid:3
s_waitcnt vmcnt(15)
s_memrealtime s[56:57]
s_waitcnt lgkmcnt(0)
s_sub_u32 s56, s56, s34
s_cmp_lt_u32 s56, 3405
s_cbranch_scc0 Lfa246_%=
s_waitcnt vmcnt(4)
Lfa246_%=:
s_add_u32 s40, %[rot], 8192
s_and_b32 s40, s40, 28672
s_add_u32 s40, s40, 1146880
buffer_load_dwordx4 a[220:223], v0, %[rv], s40 offen nt
v_mfma_f32_4x4x4_16b_bf16 v[12:15], v[22:23], a[188:189], v[12:15] cbsz:2 abid:0
v_mfma_f32_4x4x4_16b_bf16 v[16:19], v[22:23], a[190:191], v[16:19] cbsz:2 abid:0
v_and_b32_dpp v24, v28, v2 quad_perm:[3,3,3,3] row_mask:0xf bank_mask:0xf
v_and_b32_dpp v25, v28, v3 quad_perm:[3,3,3,3] row_mask:0xf bank_mask:0xf
s_waitcnt vmcnt(15)
s_add_u32 s40, %[rot], 12288
s_and_b32 s40, s40, 28672
s_add_u32 s40, s40, 1146880
buffer_load_dwordx4 a[224:227], v0, %[rv], s40 offen nt
v_mfma_f32_4x4x4_16b_bf16 v[12:15], v[22:23], a[192:193], v[12:15] cbsz:2 abid:2
v_mfma_f32_4x4x4_16b_bf16 v[16:19], v[22:23], a[194:195], v[16:19] cbsz:2 abid:2
s_waitcnt vmcnt(15)
s_memrealtime s[56:57]
s_waitcnt lgkmcnt(0)
s_sub_u32 s56, s56, s34
s_cmp_lt_u32 s56, 3431
s_cbranch_scc0 Lfa248_%=
s_waitcnt vmcnt(4)
Lfa248_%=:
s_add_u32 s40, %[rot], 16384
s_and_b32 s40, s40, 28672
s_add_u32 s40, s40, 1146880
buffer_load_dwordx4 a[228:231], v0, %[rv], s40 offen nt
v_mfma_f32_4x4x4_16b_bf16 v[12:15], v[22:23], a[196:197], v[12:15] cbsz:2 abid:1
v_mfma_f32_4x4x4_16b_bf16 v[16:19], v[22:23], a[198:199], v[16:19] cbsz:2 abid:1
s_waitcnt vmcnt(15)
s_add_u32 s40, %[rot], 20480
s_and_b32 s40, s40, 28672
s_add_u32 s40, s40, 1146880
buffer_load_dwordx4 a[232:235], v0, %[rv], s40 offen nt
v_mfma_f32_4x4x4_16b_bf16 v[12:15], v[22:23], a[200:201], v[12:15] cbsz:2 abid:3
v_mfma_f32_4x4x4_16b_bf16 v[16:19], v[22:23], a[202:203], v[16:19] cbsz:2 abid:3
s_waitcnt vmcnt(15)
s_memrealtime s[56:57]
s_waitcnt lgkmcnt(0)
s_sub_u32 s56, s56, s34
s_cmp_lt_u32 s56, 3456
s_cbranch_scc0 Lfa250_%=
s_waitcnt vmcnt(4)
Lfa250_%=:
s_add_u32 s40, %[rot], 24576
s_and_b32 s40, s40, 28672
s_add_u32 s40, s40, 1146880
buffer_load_dwordx4 a[236:239], v0, %[rv], s40 offen nt
v_mfma_f32_4x4x4_16b_bf16 v[12:15], v[24:25], a[204:205], v[12:15] cbsz:2 abid:0
v_mfma_f32_4x4x4_16b_bf16 v[16:19], v[24:25], a[206:207], v[16:19] cbsz:2 abid:0
s_waitcnt vmcnt(15)
s_add_u32 s40, %[rot], 28672
s_and_b32 s40, s40, 28672
s_add_u32 s40, s40, 1146880
buffer_load_dwordx4 a[240:243], v0, %[rv], s40 offen nt
v_mfma_f32_4x4x4_16b_bf16 v[12:15], v[24:25], a[208:209], v[12:15] cbsz:2 abid:2
v_mfma_f32_4x4x4_16b_bf16 v[16:19], v[24:25], a[210:211], v[16:19] cbsz:2 abid:2
s_waitcnt vmcnt(15)
s_memrealtime s[56:57]
)ASM"
R"ASM(s_waitcnt lgkmcnt(0)
s_sub_u32 s56, s56, s34
s_cmp_lt_u32 s56, 3482
s_cbranch_scc0 Lfa252_%=
s_waitcnt vmcnt(4)
Lfa252_%=:
s_add_u32 s40, %[rot], 0
s_and_b32 s40, s40, 28672
s_add_u32 s40, s40, 1376256
buffer_load_dwordx4 a[244:247], v0, %[rv], s40 offen nt
v_mfma_f32_4x4x4_16b_bf16 v[12:15], v[24:25], a[212:213], v[12:15] cbsz:2 abid:1
v_mfma_f32_4x4x4_16b_bf16 v[16:19], v[24:25], a[214:215], v[16:19] cbsz:2 abid:1
s_waitcnt vmcnt(15)
s_add_u32 s40, %[rot], 4096
s_and_b32 s40, s40, 28672
s_add_u32 s40, s40, 1376256
buffer_load_dwordx4 a[248:251], v0, %[rv], s40 offen nt
v_mfma_f32_4x4x4_16b_bf16 v[12:15], v[24:25], a[216:217], v[12:15] cbsz:2 abid:3
v_mfma_f32_4x4x4_16b_bf16 v[16:19], v[24:25], a[218:219], v[16:19] cbsz:2 abid:3
v_dot2_f32_bf16 v31, v104, v8, 0
v_dot2_f32_bf16 v31, v105, v9, v31
v_dot2_f32_bf16 v31, v106, v10, v31
v_dot2_f32_bf16 v31, v107, v11, v31
v_dot2_f32_bf16 v32, v108, v8, 0
v_dot2_f32_bf16 v32, v109, v9, v32
v_dot2_f32_bf16 v32, v110, v10, v32
v_dot2_f32_bf16 v32, v111, v11, v32
v_dot2_f32_bf16 v33, v112, v8, 0
v_dot2_f32_bf16 v33, v113, v9, v33
v_dot2_f32_bf16 v33, v114, v10, v33
v_dot2_f32_bf16 v33, v115, v11, v33
v_add_f32_dpp v31, v31, v31 row_ror:8 row_mask:0xf bank_mask:0x3
v_dot2_f32_bf16 v34, v116, v8, 0
v_dot2_f32_bf16 v34, v117, v9, v34
v_dot2_f32_bf16 v34, v118, v10, v34
v_dot2_f32_bf16 v34, v119, v11, v34
v_add_f32_dpp v31, v32, v32 row_ror:8 row_mask:0xf bank_mask:0xc
v_dot2_f32_bf16 v35, v120, v8, 0
v_dot2_f32_bf16 v35, v121, v9, v35
v_dot2_f32_bf16 v35, v122, v10, v35
v_dot2_f32_bf16 v35, v123, v11, v35
v_add_f32_dpp v33, v33, v33 row_ror:8 row_mask:0xf bank_mask:0x3
s_waitcnt vmcnt(15)
s_memrealtime s[56:57]
s_waitcnt lgkmcnt(0)
s_sub_u32 s56, s56, s34
s_cmp_lt_u32 s56, 3507
s_cbranch_scc0 Lfa254_%=
s_waitcnt vmcnt(4)
Lfa254_%=:
s_add_u32 s40, %[rot], 8192
s_and_b32 s40, s40, 28672
s_add_u32 s40, s40, 1376256
buffer_load_dwordx4 a[252:255], v0, %[rv], s40 offen nt
v_dot2_f32_bf16 v36, v124, v8, 0
v_dot2_f32_bf16 v36, v125, v9, v36
v_dot2_f32_bf16 v36, v126, v10, v36
v_dot2_f32_bf16 v36, v127, v11, v36
v_add_f32_dpp v33, v34, v34 row_ror:8 row_mask:0xf bank_mask:0xc
v_add_f32_dpp v31, v31, v31 row_shl:4 row_mask:0xf bank_mask:0x5
v_dot2_f32_bf16 v37, v128, v8, 0
v_dot2_f32_bf16 v37, v129, v9, v37
v_dot2_f32_bf16 v37, v130, v10, v37
v_dot2_f32_bf16 v37, v131, v11, v37
v_add_f32_dpp v31, v33, v33 row_shr:4 row_mask:0xf bank_mask:0xa
v_add_f32_dpp v35, v35, v35 row_ror:8 row_mask:0xf bank_mask:0x3
v_dot2_f32_bf16 v32, v132, v8, 0
v_dot2_f32_bf16 v32, v133, v9, v32
v_dot2_f32_bf16 v32, v134, v10, v32
v_dot2_f32_bf16 v32, v135, v11, v32
v_add_f32_dpp v35, v36, v36 row_ror:8 row_mask:0xf bank_mask:0xc
v_dot2_f32_bf16 v34, v136, v8, 0
v_dot2_f32_bf16 v34, v137, v9, v34
v_dot2_f32_bf16 v34, v138, v10, v34
v_dot2_f32_bf16 v34, v139, v11, v34
v_add_f32_dpp v37, v37, v37 row_ror:8 row_mask:0xf bank_mask:0x3
s_waitcnt vmcnt(15)
s_add_u32 s40, %[rot], 12288
s_and_b32 s40, s40, 28672
s_add_u32 s40, s40, 1376256
buffer_load_dwordx4 v[176:179], v0, %[rv], s40 offen nt
v_dot2_f32_bf16 v33, v140, v8, 0
v_dot2_f32_bf16 v33, v141, v9, v33
v_dot2_f32_bf16 v33, v142, v10, v33
v_dot2_f32_bf16 v33, v143, v11, v33
v_add_f32_dpp v37, v32, v32 row_ror:8 row_mask:0xf bank_mask:0xc
v_add_f32_dpp v35, v35, v35 row_shl:4 row_mask:0xf bank_mask:0x5
s_waitcnt vmcnt(15)
s_memrealtime s[56:57]
s_waitcnt lgkmcnt(0)
s_sub_u32 s56, s56, s34
s_cmp_lt_u32 s56, 3532
s_cbranch_scc0 Lfa256_%=
s_waitcnt vmcnt(4)
Lfa256_%=:
s_add_u32 s40, %[rot], 16384
s_and_b32 s40, s40, 28672
s_add_u32 s40, s40, 1376256
buffer_load_dwordx4 v[180:183], v0, %[rv], s40 offen nt
v_dot2_f32_bf16 v36, v144, v8, 0
v_dot2_f32_bf16 v36, v145, v9, v36
v_dot2_f32_bf16 v36, v146, v10, v36
v_dot2_f32_bf16 v36, v147, v11, v36
v_add_f32_dpp v35, v37, v37 row_shr:4 row_mask:0xf bank_mask:0xa
v_cndmask_b32 v30, v35, v31, s[44:45]
v_cndmask_b32 v31, v31, v35, s[44:45]
s_waitcnt vmcnt(15)
s_add_u32 s40, %[rot], 20480
s_and_b32 s40, s40, 28672
s_add_u32 s40, s40, 1376256
buffer_load_dwordx4 v[184:187], v0, %[rv], s40 offen nt
v_dot2_f32_bf16 v32, v148, v8, 0
v_dot2_f32_bf16 v32, v149, v9, v32
v_dot2_f32_bf16 v32, v150, v10, v32
v_dot2_f32_bf16 v32, v151, v11, v32
v_add_f32_dpp v31, v30, v31 quad_perm:[2,3,0,1] row_mask:0xf bank_mask:0xf
v_add_f32_dpp v34, v34, v34 row_ror:8 row_mask:0xf bank_mask:0x3
s_waitcnt vmcnt(15)
s_memrealtime s[56:57]
s_waitcnt lgkmcnt(0)
s_sub_u32 s56, s56, s34
s_cmp_lt_u32 s56, 3558
s_cbranch_scc0 Lfa258_%=
s_waitcnt vmcnt(4)
Lfa258_%=:
s_add_u32 s40, %[rot], 24576
s_and_b32 s40, s40, 28672
s_add_u32 s40, s40, 1376256
buffer_load_dwordx4 v[188:191], v0, %[rv], s40 offen nt
v_dot2_f32_bf16 v37, v152, v8, 0
v_dot2_f32_bf16 v37, v153, v9, v37
v_dot2_f32_bf16 v37, v154, v10, v37
v_dot2_f32_bf16 v37, v155, v11, v37
v_add_f32_dpp v34, v33, v33 row_ror:8 row_mask:0xf bank_mask:0xc
v_add_f32_dpp v36, v36, v36 row_ror:8 row_mask:0xf bank_mask:0x3
s_waitcnt vmcnt(15)
s_add_u32 s40, %[rot], 28672
s_and_b32 s40, s40, 28672
s_add_u32 s40, s40, 1376256
buffer_load_dwordx4 v[192:195], v0, %[rv], s40 offen nt
v_dot2_f32_bf16 v35, v156, v8, 0
v_dot2_f32_bf16 v35, v157, v9, v35
v_dot2_f32_bf16 v35, v158, v10, v35
v_dot2_f32_bf16 v35, v159, v11, v35
v_add_f32_dpp v36, v32, v32 row_ror:8 row_mask:0xf bank_mask:0xc
v_add_f32_dpp v34, v34, v34 row_shl:4 row_mask:0xf bank_mask:0x5
s_waitcnt vmcnt(15)
s_memrealtime s[56:57]
s_waitcnt lgkmcnt(0)
s_sub_u32 s56, s56, s34
s_cmp_lt_u32 s56, 3583
s_cbranch_scc0 Lfa260_%=
s_waitcnt vmcnt(4)
Lfa260_%=:
s_add_u32 s40, %[rot], 0
s_and_b32 s40, s40, 28672
s_add_u32 s40, s40, 1605632
buffer_load_dwordx4 v[196:199], v0, %[rv], s40 offen nt
v_dot2_f32_bf16 v33, v160, v8, 0
v_dot2_f32_bf16 v33, v161, v9, v33
v_dot2_f32_bf16 v33, v162, v10, v33
v_dot2_f32_bf16 v33, v163, v11, v33
v_add_f32_dpp v34, v36, v36 row_shr:4 row_mask:0xf bank_mask:0xa
v_add_f32_dpp v37, v37, v37 row_ror:8 row_mask:0xf bank_mask:0x3
s_waitcnt vmcnt(15)
s_add_u32 s40, %[rot], 4096
s_and_b32 s40, s40, 28672
s_add_u32 s40, s40, 1605632
buffer_load_dwordx4 v[200:203], v0, %[rv], s40 offen nt
v_dot2_f32_bf16 v32, v164, v8, 0
v_dot2_f32_bf16 v32, v165, v9, v32
v_dot2_f32_bf16 v32, v166, v10, v32
v_dot2_f32_bf16 v32, v167, v11, v32
v_add_f32_dpp v37, v35, v35 row_ror:8 row_mask:0xf bank_mask:0xc
v_add_f32_dpp v33, v33, v33 row_ror:8 row_mask:0xf bank_mask:0x3
s_nop 1
v_add_f32_dpp v33, v32, v32 row_ror:8 row_mask:0xf bank_mask:0xc
v_add_f32_dpp v37, v37, v37 row_shl:4 row_mask:0xf bank_mask:0x5
s_nop 1
v_add_f32_dpp v37, v33, v33 row_shr:4 row_mask:0xf bank_mask:0xa
v_cndmask_b32 v30, v37, v34, s[44:45]
v_cndmask_b32 v34, v34, v37, s[44:45]
s_nop 1
v_add_f32_dpp v34, v30, v34 quad_perm:[2,3,0,1] row_mask:0xf bank_mask:0xf
v_cndmask_b32 v30, v34, v31, s[46:47]
v_cndmask_b32 v31, v31, v34, s[46:47]
s_nop 1
v_add_f32_dpp v26, v30, v31 quad_perm:[1,0,3,2] row_mask:0xf bank_mask:0xf
v_cvt_pk_bf16_f32 v27, v26, v26
v_and_b32 v26, 0xffff0000, v27
v_mul_f32 v26, s55, v26
v_cvt_pk_bf16_f32 v27, v26, v26
v_and_b32 v26, 0xffff0000, v27
s_nop 1
v_max_f32_dpp v29, v26, v26 row_ror:8 row_mask:0xf bank_mask:0xf
s_nop 1
v_max_f32_dpp v29, v29, v29 row_ror:4 row_mask:0xf bank_mask:0xf
s_nop 1
v_max_f32_dpp v29, v29, v29 row_ror:2 row_mask:0xf bank_mask:0xf
s_nop 1
v_max_f32_dpp v29, v29, v29 row_ror:1 row_mask:0xf bank_mask:0xf
s_nop 1
v_max_f32_dpp v29, v29, v29 row_bcast:15 row_mask:0xa bank_mask:0xf
s_nop 1
v_max_f32_dpp v29, v29, v29 row_bcast:31 row_mask:0xc bank_mask:0xf
s_nop 0
v_readlane_b32 s50, v29, 63
v_mov_b32 v27, s54
s_nop 0
v_mul_f32 v27, s50, v27
v_max_f32 v30, v27, v20
v_sub_f32 v29, v20, v30
v_mov_b32 v20, v30
v_exp_f32 v29, v29
v_fma_f32 v26, v26, s54, -v20
v_exp_f32 v26, v26
v_mul_f32 v12, v29, v12
v_mul_f32 v13, v29, v13
v_mul_f32 v14, v29, v14
v_mul_f32 v15, v29, v15
v_mul_f32 v16, v29, v16
v_mul_f32 v17, v29, v17
v_mul_f32 v18, v29, v18
v_mul_f32 v19, v29, v19
v_cvt_pk_bf16_f32 v28, v26, v26
v_fma_f32 v21, v21, v29, v26
s_nop 0
v_and_b32_dpp v22, v28, v2 quad_perm:[0,0,0,0] row_mask:0xf bank_mask:0xf
v_and_b32_dpp v23, v28, v3 quad_perm:[0,0,0,0] row_mask:0xf bank_mask:0xf
s_waitcnt vmcnt(15)
s_memrealtime s[56:57]
s_waitcnt lgkmcnt(0)
s_sub_u32 s56, s56, s34
s_cmp_lt_u32 s56, 3609
s_cbranch_scc0 Lfa262_%=
s_waitcnt vmcnt(4)
Lfa262_%=:
s_add_u32 s40, %[rot], 8192
s_and_b32 s40, s40, 28672
s_add_u32 s40, s40, 1605632
buffer_load_dwordx4 v[168:171], v0, %[rk], s40 offen nt
v_mfma_f32_4x4x4_16b_bf16 v[12:15], v[22:23], a[220:221], v[12:15] cbsz:2 abid:0
v_mfma_f32_4x4x4_16b_bf16 v[16:19], v[22:23], a[222:223], v[16:19] cbsz:2 abid:0
v_and_b32_dpp v24, v28, v2 quad_perm:[2,2,2,2] row_mask:0xf bank_mask:0xf
v_and_b32_dpp v25, v28, v3 quad_perm:[2,2,2,2] row_mask:0xf bank_mask:0xf
s_waitcnt vmcnt(15)
s_add_u32 s40, %[rot], 12288
s_and_b32 s40, s40, 28672
s_add_u32 s40, s40, 1605632
buffer_load_dwordx4 v[172:175], v0, %[rk], s40 offen nt
v_mfma_f32_4x4x4_16b_bf16 v[12:15], v[22:23], a[224:225], v[12:15] cbsz:2 abid:2
v_mfma_f32_4x4x4_16b_bf16 v[16:19], v[22:23], a[226:227], v[16:19] cbsz:2 abid:2
s_waitcnt vmcnt(15)
s_memrealtime s[56:57]
s_waitcnt lgkmcnt(0)
s_sub_u32 s56, s56, s34
s_cmp_lt_u32 s56, 3634
s_cbranch_scc0 Lfa264_%=
s_waitcnt vmcnt(4)
Lfa264_%=:
s_add_u32 m0, s53, 26624
s_add_u32 s40, %[rot], 16384
s_and_b32 s40, s40, 28672
s_add_u32 s40, s40, 1605632
buffer_load_dwordx4 v0, %[rk], s40 offen nt lds
v_mfma_f32_4x4x4_16b_bf16 v[12:15], v[22:23], a[228:229], v[12:15] cbsz:2 abid:1
v_mfma_f32_4x4x4_16b_bf16 v[16:19], v[22:23], a[230:231], v[16:19] cbsz:2 abid:1
s_waitcnt vmcnt(15)
s_add_u32 m0, s53, 27648
s_add_u32 s40, %[rot], 20480
s_and_b32 s40, s40, 28672
s_add_u32 s40, s40, 1605632
buffer_load_dwordx4 v0, %[rk], s40 offen nt lds
v_mfma_f32_4x4x4_16b_bf16 v[12:15], v[22:23], a[232:233], v[12:15] cbsz:2 abid:3
v_mfma_f32_4x4x4_16b_bf16 v[16:19], v[22:23], a[234:235], v[16:19] cbsz:2 abid:3
s_waitcnt vmcnt(15)
s_memrealtime s[56:57]
s_waitcnt lgkmcnt(0)
s_sub_u32 s56, s56, s34
s_cmp_lt_u32 s56, 3659
s_cbranch_scc0 Lfa266_%=
s_waitcnt vmcnt(4)
Lfa266_%=:
s_add_u32 m0, s53, 28672
s_add_u32 s40, %[rot], 24576
s_and_b32 s40, s40, 28672
s_add_u32 s40, s40, 1605632
buffer_load_dwordx4 v0, %[rk], s40 offen nt lds
v_mfma_f32_4x4x4_16b_bf16 v[12:15], v[24:25], a[236:237], v[12:15] cbsz:2 abid:0
v_mfma_f32_4x4x4_16b_bf16 v[16:19], v[24:25], a[238:239], v[16:19] cbsz:2 abid:0
v_and_b32_dpp v22, v28, v2 quad_perm:[1,1,1,1] row_mask:0xf bank_mask:0xf
v_and_b32_dpp v23, v28, v3 quad_perm:[1,1,1,1] row_mask:0xf bank_mask:0xf
s_waitcnt vmcnt(15)
s_add_u32 m0, s53, 29696
s_add_u32 s40, %[rot], 28672
s_and_b32 s40, s40, 28672
s_add_u32 s40, s40, 1605632
buffer_load_dwordx4 v0, %[rk], s40 offen nt lds
v_mfma_f32_4x4x4_16b_bf16 v[12:15], v[24:25], a[240:241], v[12:15] cbsz:2 abid:2
v_mfma_f32_4x4x4_16b_bf16 v[16:19], v[24:25], a[242:243], v[16:19] cbsz:2 abid:2
s_waitcnt vmcnt(15)
s_memrealtime s[56:57]
s_waitcnt lgkmcnt(0)
s_sub_u32 s56, s56, s34
s_cmp_lt_u32 s56, 3685
s_cbranch_scc0 Lfa268_%=
s_waitcnt vmcnt(4)
Lfa268_%=:
s_add_u32 m0, s53, 30720
s_add_u32 s40, %[rot], 0
s_and_b32 s40, s40, 28672
s_add_u32 s40, s40, 1835008
buffer_load_dwordx4 v0, %[rk], s40 offen nt lds
v_mfma_f32_4x4x4_16b_bf16 v[12:15], v[24:25], a[244:245], v[12:15] cbsz:2 abid:1
v_mfma_f32_4x4x4_16b_bf16 v[16:19], v[24:25], a[246:247], v[16:19] cbsz:2 abid:1
s_waitcnt vmcnt(15)
s_add_u32 m0, s53, 31744
s_add_u32 s40, %[rot], 4096
s_and_b32 s40, s40, 28672
s_add_u32 s40, s40, 1835008
buffer_load_dwordx4 v0, %[rk], s40 offen nt lds
v_mfma_f32_4x4x4_16b_bf16 v[12:15], v[24:25], a[248:249], v[12:15] cbsz:2 abid:3
v_mfma_f32_4x4x4_16b_bf16 v[16:19], v[24:25], a[250:251], v[16:19] cbsz:2 abid:3
s_waitcnt vmcnt(15)
s_memrealtime s[56:57]
s_waitcnt lgkmcnt(0)
s_sub_u32 s56, s56, s34
s_cmp_lt_u32 s56, 3710
s_cbranch_scc0 Lfa270_%=
s_waitcnt vmcnt(4)
Lfa270_%=:
s_add_u32 m0, s53, 32768
s_add_u32 s40, %[rot], 8192
s_and_b32 s40, s40, 28672
s_add_u32 s40, s40, 1835008
buffer_load_dwordx4 v0, %[rk], s40 offen nt lds
v_mfma_f32_4x4x4_16b_bf16 v[12:15], v[22:23], a[252:253], v[12:15] cbsz:2 abid:0
v_mfma_f32_4x4x4_16b_bf16 v[16:19], v[22:23], a[254:255], v[16:19] cbsz:2 abid:0
v_and_b32_dpp v24, v28, v2 quad_perm:[3,3,3,3] row_mask:0xf bank_mask:0xf
v_and_b32_dpp v25, v28, v3 quad_perm:[3,3,3,3] row_mask:0xf bank_mask:0xf
s_waitcnt vmcnt(15)
s_add_u32 m0, s53, 33792
s_add_u32 s40, %[rot], 12288
s_and_b32 s40, s40, 28672
s_add_u32 s40, s40, 1835008
buffer_load_dwordx4 v0, %[rk], s40 offen nt lds
v_mfma_f32_4x4x4_16b_bf16 v[12:15], v[22:23], v[176:177], v[12:15] cbsz:2 abid:2
v_mfma_f32_4x4x4_16b_bf16 v[16:19], v[22:23], v[178:179], v[16:19] cbsz:2 abid:2
s_waitcnt vmcnt(15)
s_memrealtime s[56:57]
s_waitcnt lgkmcnt(0)
s_sub_u32 s56, s56, s34
s_cmp_lt_u32 s56, 3736
s_cbranch_scc0 Lfa272_%=
s_waitcnt vmcnt(4)
Lfa272_%=:
s_add_u32 m0, s53, 34816
s_add_u32 s40, %[rot], 16384
s_and_b32 s40, s40, 28672
s_add_u32 s40, s40, 1835008
buffer_load_dwordx4 v0, %[rk], s40 offen nt lds
v_mfma_f32_4x4x4_16b_bf16 v[12:15], v[22:23], v[180:181], v[12:15] cbsz:2 abid:1
v_mfma_f32_4x4x4_16b_bf16 v[16:19], v[22:23], v[182:183], v[16:19] cbsz:2 abid:1
s_waitcnt vmcnt(15)
s_add_u32 m0, s53, 35840
s_add_u32 s40, %[rot], 20480
s_and_b32 s40, s40, 28672
s_add_u32 s40, s40, 1835008
buffer_load_dwordx4 v0, %[rk], s40 offen nt lds
v_mfma_f32_4x4x4_16b_bf16 v[12:15], v[22:23], v[184:185], v[12:15] cbsz:2 abid:3
v_mfma_f32_4x4x4_16b_bf16 v[16:19], v[22:23], v[186:187], v[16:19] cbsz:2 abid:3
s_waitcnt vmcnt(15)
s_memrealtime s[56:57]
s_waitcnt lgkmcnt(0)
s_sub_u32 s56, s56, s34
s_cmp_lt_u32 s56, 3761
s_cbranch_scc0 Lfa274_%=
s_waitcnt vmcnt(4)
Lfa274_%=:
s_add_u32 m0, s53, 36864
s_add_u32 s40, %[rot], 24576
s_and_b32 s40, s40, 28672
s_add_u32 s40, s40, 1835008
buffer_load_dwordx4 v0, %[rk], s40 offen nt lds
v_mfma_f32_4x4x4_16b_bf16 v[12:15], v[24:25], v[188:189], v[12:15] cbsz:2 abid:0
v_mfma_f32_4x4x4_16b_bf16 v[16:19], v[24:25], v[190:191], v[16:19] cbsz:2 abid:0
s_waitcnt vmcnt(15)
s_add_u32 m0, s53, 37888
s_add_u32 s40, %[rot], 28672
s_and_b32 s40, s40, 28672
s_add_u32 s40, s40, 1835008
buffer_load_dwordx4 v0, %[rk], s40 offen nt lds
v_mfma_f32_4x4x4_16b_bf16 v[12:15], v[24:25], v[192:193], v[12:15] cbsz:2 abid:2
v_mfma_f32_4x4x4_16b_bf16 v[16:19], v[24:25], v[194:195], v[16:19] cbsz:2 abid:2
s_waitcnt vmcnt(15)
s_memrealtime s[56:57]
s_waitcnt lgkmcnt(0)
s_sub_u32 s56, s56, s34
s_cmp_lt_u32 s56, 3786
s_cbranch_scc0 Lfa276_%=
)ASM"
R"ASM(s_waitcnt vmcnt(4)
Lfa276_%=:
s_add_u32 m0, s53, 38912
s_lshr_b32 s40, %[pf], 12
s_mul_i32 s43, s40, 37
s_lshr_b32 s43, s43, 8
s_mul_i32 s43, s43, 7
s_sub_u32 s40, s40, s43
s_mul_i32 s40, s40, 28672
s_sub_u32 s40, 2064384, s40
buffer_load_dwordx4 v0, %[rk], s40 offen nt lds
v_mfma_f32_4x4x4_16b_bf16 v[12:15], v[24:25], v[196:197], v[12:15] cbsz:2 abid:1
v_mfma_f32_4x4x4_16b_bf16 v[16:19], v[24:25], v[198:199], v[16:19] cbsz:2 abid:1
s_waitcnt vmcnt(15)
s_add_u32 s40, %[rot], 8192
s_and_b32 s40, s40, 28672
s_add_u32 s40, s40, 1605632
buffer_load_dwordx4 v[204:207], v0, %[rv], s40 offen nt
v_mfma_f32_4x4x4_16b_bf16 v[12:15], v[24:25], v[200:201], v[12:15] cbsz:2 abid:3
v_mfma_f32_4x4x4_16b_bf16 v[16:19], v[24:25], v[202:203], v[16:19] cbsz:2 abid:3
s_waitcnt vmcnt(15)
s_memrealtime s[56:57]
s_waitcnt lgkmcnt(0)
s_sub_u32 s56, s56, s34
s_cmp_lt_u32 s56, 3812
s_cbranch_scc0 Lfa278_%=
s_waitcnt vmcnt(4)
Lfa278_%=:
s_add_u32 s40, %[rot], 12288
s_and_b32 s40, s40, 28672
s_add_u32 s40, s40, 1605632
buffer_load_dwordx4 v[208:211], v0, %[rv], s40 offen nt
v_dot2_f32_bf16 v31, v168, v8, 0
v_dot2_f32_bf16 v31, v169, v9, v31
v_dot2_f32_bf16 v31, v170, v10, v31
v_dot2_f32_bf16 v31, v171, v11, v31
s_waitcnt vmcnt(15)
s_add_u32 s40, %[rot], 16384
s_and_b32 s40, s40, 28672
s_add_u32 s40, s40, 1605632
buffer_load_dwordx4 a[104:107], v0, %[rv], s40 offen nt
v_dot2_f32_bf16 v32, v172, v8, 0
v_dot2_f32_bf16 v32, v173, v9, v32
v_dot2_f32_bf16 v32, v174, v10, v32
v_dot2_f32_bf16 v32, v175, v11, v32
s_waitcnt vmcnt(15)
s_memrealtime s[56:57]
s_waitcnt lgkmcnt(0)
s_sub_u32 s56, s56, s34
s_cmp_lt_u32 s56, 3837
s_cbranch_scc0 Lfa280_%=
s_waitcnt vmcnt(4)
Lfa280_%=:
s_add_u32 s40, %[rot], 20480
s_and_b32 s40, s40, 28672
s_add_u32 s40, s40, 1605632
buffer_load_dwordx4 a[108:111], v0, %[rv], s40 offen nt
ds_read_b128 v[244:247], v1 offset:26624
s_waitcnt lgkmcnt(0)
v_dot2_f32_bf16 v33, v244, v8, 0
v_dot2_f32_bf16 v33, v245, v9, v33
v_dot2_f32_bf16 v33, v246, v10, v33
v_dot2_f32_bf16 v33, v247, v11, v33
v_add_f32_dpp v31, v31, v31 row_ror:8 row_mask:0xf bank_mask:0x3
s_waitcnt vmcnt(15)
s_add_u32 s40, %[rot], 24576
s_and_b32 s40, s40, 28672
s_add_u32 s40, s40, 1605632
buffer_load_dwordx4 a[112:115], v0, %[rv], s40 offen nt
ds_read_b128 v[244:247], v1 offset:27648
s_waitcnt lgkmcnt(0)
v_dot2_f32_bf16 v34, v244, v8, 0
v_dot2_f32_bf16 v34, v245, v9, v34
v_dot2_f32_bf16 v34, v246, v10, v34
v_dot2_f32_bf16 v34, v247, v11, v34
v_add_f32_dpp v31, v32, v32 row_ror:8 row_mask:0xf bank_mask:0xc
s_waitcnt vmcnt(15)
s_memrealtime s[56:57]
s_waitcnt lgkmcnt(0)
s_sub_u32 s56, s56, s34
s_cmp_lt_u32 s56, 3863
s_cbranch_scc0 Lfa282_%=
s_waitcnt vmcnt(4)
Lfa282_%=:
s_add_u32 s40, %[rot], 28672
s_and_b32 s40, s40, 28672
s_add_u32 s40, s40, 1605632
buffer_load_dwordx4 a[116:119], v0, %[rv], s40 offen nt
ds_read_b128 v[244:247], v1 offset:28672
s_waitcnt lgkmcnt(0)
v_dot2_f32_bf16 v35, v244, v8, 0
v_dot2_f32_bf16 v35, v245, v9, v35
v_dot2_f32_bf16 v35, v246, v10, v35
v_dot2_f32_bf16 v35, v247, v11, v35
v_add_f32_dpp v33, v33, v33 row_ror:8 row_mask:0xf bank_mask:0x3
s_waitcnt vmcnt(15)
s_add_u32 s40, %[rot], 0
s_and_b32 s40, s40, 28672
s_add_u32 s40, s40, 1835008
buffer_load_dwordx4 a[120:123], v0, %[rv], s40 offen nt
ds_read_b128 v[244:247], v1 offset:29696
s_waitcnt lgkmcnt(0)
v_dot2_f32_bf16 v36, v244, v8, 0
v_dot2_f32_bf16 v36, v245, v9, v36
v_dot2_f32_bf16 v36, v246, v10, v36
v_dot2_f32_bf16 v36, v247, v11, v36
v_add_f32_dpp v33, v34, v34 row_ror:8 row_mask:0xf bank_mask:0xc
v_add_f32_dpp v31, v31, v31 row_shl:4 row_mask:0xf bank_mask:0x5
s_waitcnt vmcnt(15)
s_memrealtime s[56:57]
s_waitcnt lgkmcnt(0)
s_sub_u32 s56, s56, s34
s_cmp_lt_u32 s56, 3888
s_cbranch_scc0 Lfa284_%=
s_waitcnt vmcnt(4)
Lfa284_%=:
s_add_u32 s40, %[rot], 4096
s_and_b32 s40, s40, 28672
s_add_u32 s40, s40, 1835008
buffer_load_dwordx4 a[124:127], v0, %[rv], s40 offen nt
ds_read_b128 v[244:247], v1 offset:30720
s_waitcnt lgkmcnt(0)
v_dot2_f32_bf16 v37, v244, v8, 0
v_dot2_f32_bf16 v37, v245, v9, v37
v_dot2_f32_bf16 v37, v246, v10, v37
v_dot2_f32_bf16 v37, v247, v11, v37
v_add_f32_dpp v31, v33, v33 row_shr:4 row_mask:0xf bank_mask:0xa
v_add_f32_dpp v35, v35, v35 row_ror:8 row_mask:0xf bank_mask:0x3
s_waitcnt vmcnt(15)
s_add_u32 s40, %[rot], 8192
s_and_b32 s40, s40, 28672
s_add_u32 s40, s40, 1835008
buffer_load_dwordx4 a[128:131], v0, %[rv], s40 offen nt
ds_read_b128 v[244:247], v1 offset:31744
s_waitcnt lgkmcnt(0)
v_dot2_f32_bf16 v32, v244, v8, 0
v_dot2_f32_bf16 v32, v245, v9, v32
v_dot2_f32_bf16 v32, v246, v10, v32
v_dot2_f32_bf16 v32, v247, v11, v32
v_add_f32_dpp v35, v36, v36 row_ror:8 row_mask:0xf bank_mask:0xc
s_waitcnt vmcnt(15)
s_memrealtime s[56:57]
s_waitcnt lgkmcnt(0)
s_sub_u32 s56, s56, s34
s_cmp_lt_u32 s56, 3913
s_cbranch_scc0 Lfa286_%=
s_waitcnt vmcnt(4)
Lfa286_%=:
s_add_u32 s40, %[rot], 12288
s_and_b32 s40, s40, 28672
s_add_u32 s40, s40, 1835008
buffer_load_dwordx4 a[132:135], v0, %[rv], s40 offen nt
ds_read_b128 v[244:247], v1 offset:32768
s_waitcnt lgkmcnt(0)
v_dot2_f32_bf16 v34, v244, v8, 0
v_dot2_f32_bf16 v34, v245, v9, v34
v_dot2_f32_bf16 v34, v246, v10, v34
v_dot2_f32_bf16 v34, v247, v11, v34
v_add_f32_dpp v37, v37, v37 row_ror:8 row_mask:0xf bank_mask:0x3
s_waitcnt vmcnt(15)
s_add_u32 s40, %[rot], 16384
s_and_b32 s40, s40, 28672
s_add_u32 s40, s40, 1835008
buffer_load_dwordx4 a[136:139], v0, %[rv], s40 offen nt
ds_read_b128 v[244:247], v1 offset:33792
s_waitcnt lgkmcnt(0)
v_dot2_f32_bf16 v33, v244, v8, 0
v_dot2_f32_bf16 v33, v245, v9, v33
v_dot2_f32_bf16 v33, v246, v10, v33
v_dot2_f32_bf16 v33, v247, v11, v33
v_add_f32_dpp v37, v32, v32 row_ror:8 row_mask:0xf bank_mask:0xc
v_add_f32_dpp v35, v35, v35 row_shl:4 row_mask:0xf bank_mask:0x5
s_waitcnt vmcnt(15)
s_memrealtime s[56:57]
s_waitcnt lgkmcnt(0)
s_sub_u32 s56, s56, s34
s_cmp_lt_u32 s56, 3939
s_cbranch_scc0 Lfa288_%=
s_waitcnt vmcnt(4)
Lfa288_%=:
s_add_u32 s40, %[rot], 20480
s_and_b32 s40, s40, 28672
s_add_u32 s40, s40, 1835008
buffer_load_dwordx4 a[140:143], v0, %[rv], s40 offen nt
ds_read_b128 v[244:247], v1 offset:34816
s_waitcnt lgkmcnt(0)
v_dot2_f32_bf16 v36, v244, v8, 0
v_dot2_f32_bf16 v36, v245, v9, v36
v_dot2_f32_bf16 v36, v246, v10, v36
v_dot2_f32_bf16 v36, v247, v11, v36
v_add_f32_dpp v35, v37, v37 row_shr:4 row_mask:0xf bank_mask:0xa
v_cndmask_b32 v30, v35, v31, s[44:45]
v_cndmask_b32 v31, v31, v35, s[44:45]
s_waitcnt vmcnt(15)
s_add_u32 s40, %[rot], 24576
s_and_b32 s40, s40, 28672
s_add_u32 s40, s40, 1835008
buffer_load_dwordx4 a[144:147], v0, %[rv], s40 offen nt
ds_read_b128 v[244:247], v1 offset:35840
s_waitcnt lgkmcnt(0)
v_dot2_f32_bf16 v32, v244, v8, 0
v_dot2_f32_bf16 v32, v245, v9, v32
v_dot2_f32_bf16 v32, v246, v10, v32
v_dot2_f32_bf16 v32, v247, v11, v32
v_add_f32_dpp v31, v30, v31 quad_perm:[2,3,0,1] row_mask:0xf bank_mask:0xf
v_add_f32_dpp v34, v34, v34 row_ror:8 row_mask:0xf bank_mask:0x3
s_waitcnt vmcnt(15)
s_memrealtime s[56:57]
s_waitcnt lgkmcnt(0)
s_sub_u32 s56, s56, s34
s_cmp_lt_u32 s56, 3964
s_cbranch_scc0 Lfa290_%=
s_waitcnt vmcnt(4)
Lfa290_%=:
s_add_u32 s40, %[rot], 28672
s_and_b32 s40, s40, 28672
s_add_u32 s40, s40, 1835008
buffer_load_dwordx4 a[148:151], v0, %[rv], s40 offen nt
ds_read_b128 v[244:247], v1 offset:36864
s_waitcnt lgkmcnt(0)
v_dot2_f32_bf16 v37, v244, v8, 0
v_dot2_f32_bf16 v37, v245, v9, v37
v_dot2_f32_bf16 v37, v246, v10, v37
v_dot2_f32_bf16 v37, v247, v11, v37
v_add_f32_dpp v34, v33, v33 row_ror:8 row_mask:0xf bank_mask:0xc
v_add_f32_dpp v36, v36, v36 row_ror:8 row_mask:0xf bank_mask:0x3
s_waitcnt vmcnt(15)
s_lshr_b32 s40, %[pf], 12
s_mul_i32 s43, s40, 37
s_lshr_b32 s43, s43, 8
s_mul_i32 s43, s43, 7
s_sub_u32 s40, s40, s43
s_mul_i32 s40, s40, 28672
s_sub_u32 s40, 2064384, s40
buffer_load_dwordx4 a[152:155], v0, %[rv], s40 offen nt
ds_read_b128 v[244:247], v1 offset:37888
s_waitcnt lgkmcnt(0)
v_dot2_f32_bf16 v35, v244, v8, 0
v_dot2_f32_bf16 v35, v245, v9, v35
v_dot2_f32_bf16 v35, v246, v10, v35
v_dot2_f32_bf16 v35, v247, v11, v35
v_add_f32_dpp v36, v32, v32 row_ror:8 row_mask:0xf bank_mask:0xc
v_add_f32_dpp v34, v34, v34 row_shl:4 row_mask:0xf bank_mask:0x5
s_waitcnt vmcnt(15)
ds_read_b128 v[244:247], v1 offset:38912
s_waitcnt lgkmcnt(0)
v_dot2_f32_bf16 v33, v244, v8, 0
v_dot2_f32_bf16 v33, v245, v9, v33
v_dot2_f32_bf16 v33, v246, v10, v33
v_dot2_f32_bf16 v33, v247, v11, v33
v_add_f32_dpp v34, v36, v36 row_shr:4 row_mask:0xf bank_mask:0xa
v_add_f32_dpp v37, v37, v37 row_ror:8 row_mask:0xf bank_mask:0x3
s_nop 1
v_add_f32_dpp v37, v35, v35 row_ror:8 row_mask:0xf bank_mask:0xc
v_add_f32_dpp v33, v33, v33 row_ror:8 row_mask:0xf bank_mask:0x3
s_nop 0
v_add_f32_dpp v37, v37, v37 row_shl:4 row_mask:0xf bank_mask:0x5
s_nop 1
v_add_f32_dpp v37, v33, v33 row_shr:4 row_mask:0xf bank_mask:0xa
v_cndmask_b32 v30, v37, v34, s[44:45]
v_cndmask_b32 v34, v34, v37, s[44:45]
s_nop 1
v_add_f32_dpp v34, v30, v34 quad_perm:[2,3,0,1] row_mask:0xf bank_mask:0xf
v_cndmask_b32 v30, v34, v31, s[46:47]
v_cndmask_b32 v31, v31, v34, s[46:47]
s_nop 1
v_add_f32_dpp v26, v30, v31 quad_perm:[1,0,3,2] row_mask:0xf bank_mask:0xf
v_cvt_pk_bf16_f32 v27, v26, v26
v_and_b32 v26, 0xffff0000, v27
v_mul_f32 v26, s55, v26
v_cvt_pk_bf16_f32 v27, v26, v26
v_and_b32 v26, 0xffff0000, v27
s_mov_b32 s48, 0x7fff7fff
s_mov_b32 s49, 0x7fff7fff
v_mov_b32 v27, 0xff800000
v_cndmask_b32 v26, v27, v26, s[48:49]
s_nop 1
v_max_f32_dpp v29, v26, v26 row_ror:8 row_mask:0xf bank_mask:0xf
s_nop 1
v_max_f32_dpp v29, v29, v29 row_ror:4 row_mask:0xf bank_mask:0xf
s_nop 1
v_max_f32_dpp v29, v29, v29 row_ror:2 row_mask:0xf bank_mask:0xf
s_nop 1
v_max_f32_dpp v29, v29, v29 row_ror:1 row_mask:0xf bank_mask:0xf
s_nop 1
v_max_f32_dpp v29, v29, v29 row_bcast:15 row_mask:0xa bank_mask:0xf
s_nop 1
v_max_f32_dpp v29, v29, v29 row_bcast:31 row_mask:0xc bank_mask:0xf
s_nop 0
v_readlane_b32 s50, v29, 63
v_mov_b32 v27, s54
s_nop 0
v_mul_f32 v27, s50, v27
v_max_f32 v30, v27, v20
v_sub_f32 v29, v20, v30
v_mov_b32 v20, v30
v_exp_f32 v29, v29
v_fma_f32 v26, v26, s54, -v20
v_exp_f32 v26, v26
v_mul_f32 v12, v29, v12
v_mul_f32 v13, v29, v13
v_mul_f32 v14, v29, v14
v_mul_f32 v15, v29, v15
v_mul_f32 v16, v29, v16
v_mul_f32 v17, v29, v17
v_mul_f32 v18, v29, v18
v_mul_f32 v19, v29, v19
v_cvt_pk_bf16_f32 v28, v26, v26
v_fma_f32 v21, v21, v29, v26
s_nop 1
v_add_f32_dpp v21, v21, v21 row_ror:8 row_mask:0xf bank_mask:0xf
s_nop 1
v_add_f32_dpp v21, v21, v21 row_ror:4 row_mask:0xf bank_mask:0xf
s_nop 1
v_add_f32_dpp v21, v21, v21 row_ror:2 row_mask:0xf bank_mask:0xf
s_nop 1
v_add_f32_dpp v21, v21, v21 row_ror:1 row_mask:0xf bank_mask:0xf
s_nop 1
v_add_f32_dpp v21, v21, v21 row_bcast:15 row_mask:0xa bank_mask:0xf
s_nop 1
v_add_f32_dpp v21, v21, v21 row_bcast:31 row_mask:0xc bank_mask:0xf
v_and_b32_dpp v22, v28, v2 quad_perm:[0,0,0,0] row_mask:0xf bank_mask:0xf
v_and_b32_dpp v23, v28, v3 quad_perm:[0,0,0,0] row_mask:0xf bank_mask:0xf
s_add_u32 s41, %[lb], 161828
v_mov_b32 v27, s41
Lrel_%=:
ds_read_b32 v26, v27
s_waitcnt lgkmcnt(0)
v_readfirstlane_b32 s50, v26
s_cmp_eq_u32 s52, 0
s_cbranch_scc1 Lrel2_%=
s_sub_u32 s52, s52, 1
s_cmp_lt_u32 s50, 5
s_cbranch_scc1 Lrel_%=
Lrel2_%=:
s_waitcnt vmcnt(14)
v_mfma_f32_4x4x4_16b_bf16 v[12:15], v[22:23], v[204:205], v[12:15] cbsz:2 abid:0
v_mfma_f32_4x4x4_16b_bf16 v[16:19], v[22:23], v[206:207], v[16:19] cbsz:2 abid:0
v_and_b32_dpp v24, v28, v2 quad_perm:[2,2,2,2] row_mask:0xf bank_mask:0xf
v_and_b32_dpp v25, v28, v3 quad_perm:[2,2,2,2] row_mask:0xf bank_mask:0xf
s_waitcnt vmcnt(13)
v_mfma_f32_4x4x4_16b_bf16 v[12:15], v[22:23], v[208:209], v[12:15] cbsz:2 abid:2
v_mfma_f32_4x4x4_16b_bf16 v[16:19], v[22:23], v[210:211], v[16:19] cbsz:2 abid:2
s_waitcnt vmcnt(12)
v_mfma_f32_4x4x4_16b_bf16 v[12:15], v[22:23], a[104:105], v[12:15] cbsz:2 abid:1
v_mfma_f32_4x4x4_16b_bf16 v[16:19], v[22:23], a[106:107], v[16:19] cbsz:2 abid:1
s_waitcnt vmcnt(11)
v_mfma_f32_4x4x4_16b_bf16 v[12:15], v[22:23], a[108:109], v[12:15] cbsz:2 abid:3
v_mfma_f32_4x4x4_16b_bf16 v[16:19], v[22:23], a[110:111], v[16:19] cbsz:2 abid:3
s_waitcnt vmcnt(10)
v_mfma_f32_4x4x4_16b_bf16 v[12:15], v[24:25], a[112:113], v[12:15] cbsz:2 abid:0
v_mfma_f32_4x4x4_16b_bf16 v[16:19], v[24:25], a[114:115], v[16:19] cbsz:2 abid:0
v_and_b32_dpp v22, v28, v2 quad_perm:[1,1,1,1] row_mask:0xf bank_mask:0xf
v_and_b32_dpp v23, v28, v3 quad_perm:[1,1,1,1] row_mask:0xf bank_mask:0xf
s_waitcnt vmcnt(9)
v_mfma_f32_4x4x4_16b_bf16 v[12:15], v[24:25], a[116:117], v[12:15] cbsz:2 abid:2
v_mfma_f32_4x4x4_16b_bf16 v[16:19], v[24:25], a[118:119], v[16:19] cbsz:2 abid:2
s_waitcnt vmcnt(8)
v_mfma_f32_4x4x4_16b_bf16 v[12:15], v[24:25], a[120:121], v[12:15] cbsz:2 abid:1
v_mfma_f32_4x4x4_16b_bf16 v[16:19], v[24:25], a[122:123], v[16:19] cbsz:2 abid:1
s_waitcnt vmcnt(7)
v_mfma_f32_4x4x4_16b_bf16 v[12:15], v[24:25], a[124:125], v[12:15] cbsz:2 abid:3
v_mfma_f32_4x4x4_16b_bf16 v[16:19], v[24:25], a[126:127], v[16:19] cbsz:2 abid:3
s_waitcnt vmcnt(6)
v_mfma_f32_4x4x4_16b_bf16 v[12:15], v[22:23], a[128:129], v[12:15] cbsz:2 abid:0
v_mfma_f32_4x4x4_16b_bf16 v[16:19], v[22:23], a[130:131], v[16:19] cbsz:2 abid:0
v_and_b32_dpp v24, v28, v2 quad_perm:[3,3,3,3] row_mask:0xf bank_mask:0xf
v_and_b32_dpp v25, v28, v3 quad_perm:[3,3,3,3] row_mask:0xf bank_mask:0xf
s_waitcnt vmcnt(5)
v_mfma_f32_4x4x4_16b_bf16 v[12:15], v[22:23], a[132:133], v[12:15] cbsz:2 abid:2
v_mfma_f32_4x4x4_16b_bf16 v[16:19], v[22:23], a[134:135], v[16:19] cbsz:2 abid:2
s_waitcnt vmcnt(4)
v_mfma_f32_4x4x4_16b_bf16 v[12:15], v[22:23], a[136:137], v[12:15] cbsz:2 abid:1
v_mfma_f32_4x4x4_16b_bf16 v[16:19], v[22:23], a[138:139], v[16:19] cbsz:2 abid:1
s_waitcnt vmcnt(3)
v_mfma_f32_4x4x4_16b_bf16 v[12:15], v[22:23], a[140:141], v[12:15] cbsz:2 abid:3
v_mfma_f32_4x4x4_16b_bf16 v[16:19], v[22:23], a[142:143], v[16:19] cbsz:2 abid:3
s_waitcnt vmcnt(2)
v_mfma_f32_4x4x4_16b_bf16 v[12:15], v[24:25], a[144:145], v[12:15] cbsz:2 abid:0
v_mfma_f32_4x4x4_16b_bf16 v[16:19], v[24:25], a[146:147], v[16:19] cbsz:2 abid:0
s_waitcnt vmcnt(1)
v_mfma_f32_4x4x4_16b_bf16 v[12:15], v[24:25], a[148:149], v[12:15] cbsz:2 abid:2
v_mfma_f32_4x4x4_16b_bf16 v[16:19], v[24:25], a[150:151], v[16:19] cbsz:2 abid:2
s_waitcnt vmcnt(0)
v_mfma_f32_4x4x4_16b_bf16 v[12:15], v[24:25], a[152:153], v[12:15] cbsz:2 abid:1
v_mfma_f32_4x4x4_16b_bf16 v[16:19], v[24:25], a[154:155], v[16:19] cbsz:2 abid:1
s_mov_b32 s42, 2
s_branch Le1_%=
Le1ret1_%=:
s_barrier
s_add_u32 m0, s53, 13312
s_add_u32 s40, %[rot], 20480
s_and_b32 s40, s40, 28672
s_add_u32 s40, s40, 229376
buffer_load_dwordx4 v0, %[rk], s40 offen nt lds
s_add_u32 m0, s53, 14336
s_add_u32 s40, %[rot], 24576
s_and_b32 s40, s40, 28672
s_add_u32 s40, s40, 229376
buffer_load_dwordx4 v0, %[rk], s40 offen nt lds
)ASM"
R"ASM(s_add_u32 m0, s53, 15360
s_add_u32 s40, %[rot], 28672
s_and_b32 s40, s40, 28672
s_add_u32 s40, s40, 229376
buffer_load_dwordx4 v0, %[rk], s40 offen nt lds
s_add_u32 m0, s53, 16384
s_add_u32 s40, %[rot], 0
s_and_b32 s40, s40, 28672
s_add_u32 s40, s40, 458752
buffer_load_dwordx4 v0, %[rk], s40 offen nt lds
s_add_u32 m0, s53, 17408
s_add_u32 s40, %[rot], 4096
s_and_b32 s40, s40, 28672
s_add_u32 s40, s40, 458752
buffer_load_dwordx4 v0, %[rk], s40 offen nt lds
s_add_u32 m0, s53, 18432
s_add_u32 s40, %[rot], 8192
s_and_b32 s40, s40, 28672
s_add_u32 s40, s40, 458752
buffer_load_dwordx4 v0, %[rk], s40 offen nt lds
s_add_u32 m0, s53, 19456
s_add_u32 s40, %[rot], 12288
s_and_b32 s40, s40, 28672
s_add_u32 s40, s40, 458752
buffer_load_dwordx4 v0, %[rk], s40 offen nt lds
s_add_u32 m0, s53, 20480
s_add_u32 s40, %[rot], 16384
s_and_b32 s40, s40, 28672
s_add_u32 s40, s40, 458752
buffer_load_dwordx4 v0, %[rk], s40 offen nt lds
s_add_u32 m0, s53, 21504
s_add_u32 s40, %[rot], 20480
s_and_b32 s40, s40, 28672
s_add_u32 s40, s40, 458752
buffer_load_dwordx4 v0, %[rk], s40 offen nt lds
s_add_u32 m0, s53, 22528
s_add_u32 s40, %[rot], 24576
s_and_b32 s40, s40, 28672
s_add_u32 s40, s40, 458752
buffer_load_dwordx4 v0, %[rk], s40 offen nt lds
s_add_u32 m0, s53, 23552
s_add_u32 s40, %[rot], 28672
s_and_b32 s40, s40, 28672
s_add_u32 s40, s40, 458752
buffer_load_dwordx4 v0, %[rk], s40 offen nt lds
s_add_u32 m0, s53, 24576
s_add_u32 s40, %[rot], 0
s_and_b32 s40, s40, 28672
s_add_u32 s40, s40, 688128
buffer_load_dwordx4 v0, %[rk], s40 offen nt lds
s_add_u32 m0, s53, 25600
s_add_u32 s40, %[rot], 4096
s_and_b32 s40, s40, 28672
s_add_u32 s40, s40, 688128
buffer_load_dwordx4 v0, %[rk], s40 offen nt lds
s_add_u32 s40, %[rot], 4096
s_and_b32 s40, s40, 28672
s_add_u32 s40, s40, 0
buffer_load_dwordx4 a[4:7], v0, %[rv], s40 offen nt
s_waitcnt vmcnt(13)
s_add_u32 s40, %[rot], 8192
s_and_b32 s40, s40, 28672
s_add_u32 s40, s40, 0
buffer_load_dwordx4 a[8:11], v0, %[rv], s40 offen nt
s_waitcnt vmcnt(13)
s_add_u32 s40, %[rot], 12288
s_and_b32 s40, s40, 28672
s_add_u32 s40, s40, 0
buffer_load_dwordx4 a[12:15], v0, %[rv], s40 offen nt
s_waitcnt vmcnt(13)
s_add_u32 s40, %[rot], 16384
s_and_b32 s40, s40, 28672
s_add_u32 s40, s40, 0
buffer_load_dwordx4 a[16:19], v0, %[rv], s40 offen nt
s_waitcnt vmcnt(13)
s_add_u32 s40, %[rot], 20480
s_and_b32 s40, s40, 28672
s_add_u32 s40, s40, 0
buffer_load_dwordx4 a[20:23], v0, %[rv], s40 offen nt
s_waitcnt vmcnt(13)
s_add_u32 s40, %[rot], 24576
s_and_b32 s40, s40, 28672
s_add_u32 s40, s40, 0
buffer_load_dwordx4 a[24:27], v0, %[rv], s40 offen nt
s_waitcnt vmcnt(13)
s_add_u32 s40, %[rot], 28672
s_and_b32 s40, s40, 28672
s_add_u32 s40, s40, 0
buffer_load_dwordx4 a[28:31], v0, %[rv], s40 offen nt
s_waitcnt vmcnt(13)
s_add_u32 s40, %[rot], 0
s_and_b32 s40, s40, 28672
s_add_u32 s40, s40, 229376
buffer_load_dwordx4 a[32:35], v0, %[rv], s40 offen nt
s_waitcnt vmcnt(13)
s_add_u32 s40, %[rot], 4096
s_and_b32 s40, s40, 28672
s_add_u32 s40, s40, 229376
buffer_load_dwordx4 a[36:39], v0, %[rv], s40 offen nt
s_waitcnt vmcnt(13)
s_add_u32 s40, %[rot], 8192
s_and_b32 s40, s40, 28672
s_add_u32 s40, s40, 229376
buffer_load_dwordx4 a[40:43], v0, %[rv], s40 offen nt
s_waitcnt vmcnt(13)
s_add_u32 s40, %[rot], 12288
s_and_b32 s40, s40, 28672
s_add_u32 s40, s40, 229376
buffer_load_dwordx4 a[44:47], v0, %[rv], s40 offen nt
s_waitcnt vmcnt(13)
s_add_u32 s40, %[rot], 16384
s_and_b32 s40, s40, 28672
s_add_u32 s40, s40, 229376
buffer_load_dwordx4 a[48:51], v0, %[rv], s40 offen nt
s_waitcnt vmcnt(13)
s_add_u32 s40, %[rot], 20480
s_and_b32 s40, s40, 28672
s_add_u32 s40, s40, 229376
buffer_load_dwordx4 a[52:55], v0, %[rv], s40 offen nt
s_waitcnt vmcnt(13)
s_add_u32 s40, %[rot], 24576
s_and_b32 s40, s40, 28672
s_add_u32 s40, s40, 229376
buffer_load_dwordx4 a[56:59], v0, %[rv], s40 offen nt
s_waitcnt vmcnt(13)
s_add_u32 s40, %[rot], 28672
s_and_b32 s40, s40, 28672
s_add_u32 s40, s40, 229376
buffer_load_dwordx4 a[60:63], v0, %[rv], s40 offen nt
s_waitcnt vmcnt(13)
s_add_u32 s40, %[rot], 0
s_and_b32 s40, s40, 28672
s_add_u32 s40, s40, 458752
buffer_load_dwordx4 a[64:67], v0, %[rv], s40 offen nt
s_waitcnt vmcnt(13)
s_add_u32 s40, %[rot], 4096
s_and_b32 s40, s40, 28672
s_add_u32 s40, s40, 458752
buffer_load_dwordx4 a[68:71], v0, %[rv], s40 offen nt
s_waitcnt vmcnt(13)
s_add_u32 s40, %[rot], 8192
s_and_b32 s40, s40, 28672
s_add_u32 s40, s40, 458752
buffer_load_dwordx4 a[72:75], v0, %[rv], s40 offen nt
s_waitcnt vmcnt(13)
s_add_u32 s40, %[rot], 12288
s_and_b32 s40, s40, 28672
s_add_u32 s40, s40, 458752
buffer_load_dwordx4 a[76:79], v0, %[rv], s40 offen nt
s_waitcnt vmcnt(13)
s_add_u32 s40, %[rot], 16384
s_and_b32 s40, s40, 28672
s_add_u32 s40, s40, 458752
buffer_load_dwordx4 a[80:83], v0, %[rv], s40 offen nt
s_waitcnt vmcnt(13)
s_add_u32 s40, %[rot], 20480
s_and_b32 s40, s40, 28672
s_add_u32 s40, s40, 458752
buffer_load_dwordx4 a[84:87], v0, %[rv], s40 offen nt
s_waitcnt vmcnt(13)
s_add_u32 s40, %[rot], 24576
s_and_b32 s40, s40, 28672
s_add_u32 s40, s40, 458752
buffer_load_dwordx4 a[88:91], v0, %[rv], s40 offen nt
s_waitcnt vmcnt(13)
s_add_u32 s40, %[rot], 28672
s_and_b32 s40, s40, 28672
s_add_u32 s40, s40, 458752
buffer_load_dwordx4 a[92:95], v0, %[rv], s40 offen nt
s_waitcnt vmcnt(13)
s_add_u32 s40, %[rot], 0
s_and_b32 s40, s40, 28672
s_add_u32 s40, s40, 688128
buffer_load_dwordx4 a[96:99], v0, %[rv], s40 offen nt
s_waitcnt vmcnt(13)
s_add_u32 s40, %[rot], 4096
s_and_b32 s40, s40, 28672
s_add_u32 s40, s40, 688128
buffer_load_dwordx4 a[100:103], v0, %[rv], s40 offen nt
s_waitcnt vmcnt(13)
s_add_u32 m0, s53, 0
s_add_u32 s40, %[rot], 0
s_and_b32 s40, s40, 28672
s_add_u32 s40, s40, 0
buffer_load_dwordx4 v0, %[rk], s40 offen nt lds
s_waitcnt vmcnt(13)
s_add_u32 m0, s53, 1024
s_add_u32 s40, %[rot], 4096
s_and_b32 s40, s40, 28672
s_add_u32 s40, s40, 0
buffer_load_dwordx4 v0, %[rk], s40 offen nt lds
s_waitcnt vmcnt(13)
s_add_u32 m0, s53, 2048
s_add_u32 s40, %[rot], 8192
s_and_b32 s40, s40, 28672
s_add_u32 s40, s40, 0
buffer_load_dwordx4 v0, %[rk], s40 offen nt lds
s_waitcnt vmcnt(13)
s_add_u32 m0, s53, 3072
s_add_u32 s40, %[rot], 12288
s_and_b32 s40, s40, 28672
s_add_u32 s40, s40, 0
buffer_load_dwordx4 v0, %[rk], s40 offen nt lds
s_waitcnt vmcnt(13)
s_add_u32 m0, s53, 4096
s_add_u32 s40, %[rot], 16384
s_and_b32 s40, s40, 28672
s_add_u32 s40, s40, 0
buffer_load_dwordx4 v0, %[rk], s40 offen nt lds
s_waitcnt vmcnt(13)
s_add_u32 m0, s53, 5120
s_add_u32 s40, %[rot], 20480
s_and_b32 s40, s40, 28672
s_add_u32 s40, s40, 0
buffer_load_dwordx4 v0, %[rk], s40 offen nt lds
s_waitcnt vmcnt(13)
s_add_u32 m0, s53, 6144
s_add_u32 s40, %[rot], 24576
s_and_b32 s40, s40, 28672
s_add_u32 s40, s40, 0
buffer_load_dwordx4 v0, %[rk], s40 offen nt lds
s_waitcnt vmcnt(13)
s_add_u32 m0, s53, 7168
s_add_u32 s40, %[rot], 28672
s_and_b32 s40, s40, 28672
s_add_u32 s40, s40, 0
buffer_load_dwordx4 v0, %[rk], s40 offen nt lds
s_waitcnt vmcnt(13)
s_add_u32 m0, s53, 8192
s_add_u32 s40, %[rot], 0
s_and_b32 s40, s40, 28672
s_add_u32 s40, s40, 229376
buffer_load_dwordx4 v0, %[rk], s40 offen nt lds
s_waitcnt vmcnt(13)
s_add_u32 m0, s53, 9216
s_add_u32 s40, %[rot], 4096
s_and_b32 s40, s40, 28672
s_add_u32 s40, s40, 229376
buffer_load_dwordx4 v0, %[rk], s40 offen nt lds
s_waitcnt vmcnt(13)
s_add_u32 m0, s53, 10240
s_add_u32 s40, %[rot], 8192
s_and_b32 s40, s40, 28672
s_add_u32 s40, s40, 229376
buffer_load_dwordx4 v0, %[rk], s40 offen nt lds
s_waitcnt vmcnt(13)
s_add_u32 m0, s53, 11264
s_add_u32 s40, %[rot], 12288
s_and_b32 s40, s40, 28672
s_add_u32 s40, s40, 229376
buffer_load_dwordx4 v0, %[rk], s40 offen nt lds
s_waitcnt vmcnt(13)
s_add_u32 m0, s53, 12288
s_add_u32 s40, %[rot], 16384
s_and_b32 s40, s40, 28672
s_add_u32 s40, s40, 229376
buffer_load_dwordx4 v0, %[rk], s40 offen nt lds
s_waitcnt vmcnt(13)
s_add_u32 s40, %[rot], 0
s_and_b32 s40, s40, 28672
s_add_u32 s40, s40, 0
buffer_load_dwordx4 a[0:3], v0, %[rv], s40 offen nt
s_mov_b32 s51, 1
s_add_u32 s41, s33, 786432
buffer_load_dwordx4 v[4:7], v39, %[rw], s41 offen sc1
s_waitcnt vmcnt(0)
v_max3_u32 v27, v4, v5, v6
v_max_u32 v27, v27, v7
v_cmp_eq_u32 vcc, -1, v27
s_cbranch_vccnz Lslow_q2b1_%=
Lback_q2b1_%=:
s_branch Lp2_%=
Lp2_%=:
s_nop 4
v_dot2_f32_bf16 v31, v40, v4, 0
v_dot2_f32_bf16 v32, v44, v4, 0
v_dot2_f32_bf16 v33, v48, v4, 0
v_dot2_f32_bf16 v34, v52, v4, 0
v_dot2c_f32_bf16 v31, v41, v5
v_dot2c_f32_bf16 v32, v45, v5
v_dot2c_f32_bf16 v33, v49, v5
v_dot2c_f32_bf16 v34, v53, v5
v_dot2c_f32_bf16 v31, v42, v6
v_dot2c_f32_bf16 v32, v46, v6
v_dot2c_f32_bf16 v33, v50, v6
v_dot2c_f32_bf16 v34, v54, v6
v_dot2c_f32_bf16 v31, v43, v7
v_dot2c_f32_bf16 v32, v47, v7
v_dot2c_f32_bf16 v33, v51, v7
v_dot2c_f32_bf16 v34, v55, v7
v_dot2_f32_bf16 v35, v56, v4, 0
v_dot2_f32_bf16 v36, v60, v4, 0
v_dot2_f32_bf16 v37, v64, v4, 0
v_dot2_f32_bf16 v44, v68, v4, 0
v_add_f32_dpp v31, v31, v31 row_ror:8 row_mask:0xf bank_mask:0x3
v_add_f32_dpp v33, v33, v33 row_ror:8 row_mask:0xf bank_mask:0x3
v_dot2c_f32_bf16 v35, v57, v5
v_dot2c_f32_bf16 v36, v61, v5
v_dot2c_f32_bf16 v37, v65, v5
v_dot2c_f32_bf16 v44, v69, v5
v_add_f32_dpp v31, v32, v32 row_ror:8 row_mask:0xf bank_mask:0xc
v_add_f32_dpp v33, v34, v34 row_ror:8 row_mask:0xf bank_mask:0xc
v_dot2c_f32_bf16 v35, v58, v6
v_dot2c_f32_bf16 v36, v62, v6
v_dot2c_f32_bf16 v37, v66, v6
v_dot2c_f32_bf16 v44, v70, v6
v_add_f32_dpp v31, v31, v31 row_shl:4 row_mask:0xf bank_mask:0x5
v_dot2c_f32_bf16 v35, v59, v7
v_dot2c_f32_bf16 v36, v63, v7
v_dot2c_f32_bf16 v37, v67, v7
v_dot2c_f32_bf16 v44, v71, v7
v_add_f32_dpp v31, v33, v33 row_shr:4 row_mask:0xf bank_mask:0xa
v_dot2_f32_bf16 v45, v72, v4, 0
v_dot2_f32_bf16 v46, v76, v4, 0
v_dot2_f32_bf16 v47, v80, v4, 0
v_dot2_f32_bf16 v32, v84, v4, 0
v_add_f32_dpp v35, v35, v35 row_ror:8 row_mask:0xf bank_mask:0x3
v_add_f32_dpp v37, v37, v37 row_ror:8 row_mask:0xf bank_mask:0x3
v_dot2c_f32_bf16 v45, v73, v5
v_dot2c_f32_bf16 v46, v77, v5
v_dot2c_f32_bf16 v47, v81, v5
v_dot2c_f32_bf16 v32, v85, v5
v_add_f32_dpp v35, v36, v36 row_ror:8 row_mask:0xf bank_mask:0xc
v_add_f32_dpp v37, v44, v44 row_ror:8 row_mask:0xf bank_mask:0xc
v_dot2c_f32_bf16 v45, v74, v6
v_dot2c_f32_bf16 v46, v78, v6
v_dot2c_f32_bf16 v47, v82, v6
v_dot2c_f32_bf16 v32, v86, v6
v_add_f32_dpp v35, v35, v35 row_shl:4 row_mask:0xf bank_mask:0x5
v_dot2c_f32_bf16 v45, v75, v7
v_dot2c_f32_bf16 v46, v79, v7
v_dot2c_f32_bf16 v47, v83, v7
v_dot2c_f32_bf16 v32, v87, v7
v_add_f32_dpp v35, v37, v37 row_shr:4 row_mask:0xf bank_mask:0xa
v_cndmask_b32 v30, v35, v31, s[44:45]
v_cndmask_b32 v31, v31, v35, s[44:45]
v_dot2_f32_bf16 v34, v88, v4, 0
v_dot2_f32_bf16 v33, v92, v4, 0
v_dot2_f32_bf16 v36, v96, v4, 0
v_dot2_f32_bf16 v44, v100, v4, 0
v_add_f32_dpp v31, v30, v31 quad_perm:[2,3,0,1] row_mask:0xf bank_mask:0xf
v_add_f32_dpp v45, v45, v45 row_ror:8 row_mask:0xf bank_mask:0x3
v_dot2c_f32_bf16 v34, v89, v5
v_dot2c_f32_bf16 v33, v93, v5
v_dot2c_f32_bf16 v36, v97, v5
v_dot2c_f32_bf16 v44, v101, v5
v_add_f32_dpp v45, v46, v46 row_ror:8 row_mask:0xf bank_mask:0xc
v_add_f32_dpp v47, v47, v47 row_ror:8 row_mask:0xf bank_mask:0x3
v_dot2c_f32_bf16 v34, v90, v6
v_dot2c_f32_bf16 v33, v94, v6
v_dot2c_f32_bf16 v36, v98, v6
v_dot2c_f32_bf16 v44, v102, v6
v_add_f32_dpp v47, v32, v32 row_ror:8 row_mask:0xf bank_mask:0xc
v_add_f32_dpp v45, v45, v45 row_shl:4 row_mask:0xf bank_mask:0x5
v_dot2c_f32_bf16 v34, v91, v7
v_dot2c_f32_bf16 v33, v95, v7
v_dot2c_f32_bf16 v36, v99, v7
v_dot2c_f32_bf16 v44, v103, v7
v_add_f32_dpp v45, v47, v47 row_shr:4 row_mask:0xf bank_mask:0xa
ds_read_b128 v[60:63], v1 offset:0
ds_read_b128 v[64:67], v1 offset:1024
ds_read_b128 v[68:71], v1 offset:2048
ds_read_b128 v[72:75], v1 offset:3072
ds_read_b128 v[76:79], v1 offset:4096
ds_read_b128 v[80:83], v1 offset:5120
ds_read_b128 v[48:51], v1 offset:6144
ds_read_b128 v[52:55], v1 offset:7168
ds_read_b128 v[56:59], v1 offset:8192
s_cmp_lg_u32 s51, 0
s_cbranch_scc1 Lnb0_%=
s_add_u32 s40, %[rot], 20480
s_and_b32 s40, s40, 28672
s_add_u32 s40, s40, 917504
buffer_load_dwordx4 v[84:87], v0, %[rk], s40 offen nt
s_add_u32 s40, %[rot], 24576
s_and_b32 s40, s40, 28672
s_add_u32 s40, s40, 917504
buffer_load_dwordx4 v[88:91], v0, %[rk], s40 offen nt
s_add_u32 s40, %[rot], 28672
s_and_b32 s40, s40, 28672
s_add_u32 s40, s40, 917504
buffer_load_dwordx4 v[92:95], v0, %[rk], s40 offen nt
s_add_u32 s40, %[rot], 0
s_and_b32 s40, s40, 28672
s_add_u32 s40, s40, 1146880
buffer_load_dwordx4 v[96:99], v0, %[rk], s40 offen nt
s_add_u32 s40, %[rot], 4096
s_and_b32 s40, s40, 28672
s_add_u32 s40, s40, 1146880
buffer_load_dwordx4 v[100:103], v0, %[rk], s40 offen nt
Lnb0_%=:
v_dot2_f32_bf16 v37, v104, v4, 0
v_dot2_f32_bf16 v35, v108, v4, 0
v_dot2_f32_bf16 v46, v112, v4, 0
v_dot2_f32_bf16 v32, v116, v4, 0
v_add_f32_dpp v34, v34, v34 row_ror:8 row_mask:0xf bank_mask:0x3
v_add_f32_dpp v36, v36, v36 row_ror:8 row_mask:0xf bank_mask:0x3
v_dot2c_f32_bf16 v37, v105, v5
v_dot2c_f32_bf16 v35, v109, v5
v_dot2c_f32_bf16 v46, v113, v5
)ASM"
R"ASM(v_dot2c_f32_bf16 v32, v117, v5
v_add_f32_dpp v34, v33, v33 row_ror:8 row_mask:0xf bank_mask:0xc
v_add_f32_dpp v36, v44, v44 row_ror:8 row_mask:0xf bank_mask:0xc
v_dot2c_f32_bf16 v37, v106, v6
v_dot2c_f32_bf16 v35, v110, v6
v_dot2c_f32_bf16 v46, v114, v6
v_dot2c_f32_bf16 v32, v118, v6
v_add_f32_dpp v34, v34, v34 row_shl:4 row_mask:0xf bank_mask:0x5
v_dot2c_f32_bf16 v37, v107, v7
v_dot2c_f32_bf16 v35, v111, v7
v_dot2c_f32_bf16 v46, v115, v7
v_dot2c_f32_bf16 v32, v119, v7
v_add_f32_dpp v34, v36, v36 row_shr:4 row_mask:0xf bank_mask:0xa
v_cndmask_b32 v30, v34, v45, s[44:45]
v_cndmask_b32 v45, v45, v34, s[44:45]
v_dot2_f32_bf16 v47, v120, v4, 0
v_dot2_f32_bf16 v33, v124, v4, 0
v_dot2_f32_bf16 v44, v128, v4, 0
v_dot2_f32_bf16 v36, v132, v4, 0
v_add_f32_dpp v45, v30, v45 quad_perm:[2,3,0,1] row_mask:0xf bank_mask:0xf
v_cndmask_b32 v30, v45, v31, s[46:47]
v_dot2c_f32_bf16 v47, v121, v5
v_dot2c_f32_bf16 v33, v125, v5
v_dot2c_f32_bf16 v44, v129, v5
v_dot2c_f32_bf16 v36, v133, v5
v_cndmask_b32 v31, v31, v45, s[46:47]
v_add_f32_dpp v37, v37, v37 row_ror:8 row_mask:0xf bank_mask:0x3
v_add_f32_dpp v46, v46, v46 row_ror:8 row_mask:0xf bank_mask:0x3
v_dot2c_f32_bf16 v47, v122, v6
v_dot2c_f32_bf16 v33, v126, v6
v_dot2c_f32_bf16 v44, v130, v6
v_dot2c_f32_bf16 v36, v134, v6
v_add_f32_dpp v26, v30, v31 quad_perm:[1,0,3,2] row_mask:0xf bank_mask:0xf
v_cvt_pk_bf16_f32 v27, v26, v26
v_dot2c_f32_bf16 v47, v123, v7
v_dot2c_f32_bf16 v33, v127, v7
v_dot2c_f32_bf16 v44, v131, v7
v_dot2c_f32_bf16 v36, v135, v7
v_and_b32 v26, 0xffff0000, v27
v_mul_f32 v26, s55, v26
v_cvt_pk_bf16_f32 v27, v26, v26
v_dot2_f32_bf16 v34, v136, v4, 0
v_dot2_f32_bf16 v45, v140, v4, 0
v_dot2_f32_bf16 v31, v144, v4, 0
v_and_b32 v26, 0xffff0000, v27
v_add_f32_dpp v37, v35, v35 row_ror:8 row_mask:0xf bank_mask:0xc
v_dot2_f32_bf16 v35, v148, v4, 0
v_add_f32_dpp v46, v32, v32 row_ror:8 row_mask:0xf bank_mask:0xc
v_add_f32_dpp v37, v37, v37 row_shl:4 row_mask:0xf bank_mask:0x5
v_dot2c_f32_bf16 v34, v137, v5
v_dot2c_f32_bf16 v45, v141, v5
v_dot2c_f32_bf16 v31, v145, v5
v_dot2c_f32_bf16 v35, v149, v5
v_add_f32_dpp v37, v46, v46 row_shr:4 row_mask:0xf bank_mask:0xa
v_add_f32_dpp v47, v47, v47 row_ror:8 row_mask:0xf bank_mask:0x3
v_add_f32_dpp v44, v44, v44 row_ror:8 row_mask:0xf bank_mask:0x3
v_dot2c_f32_bf16 v34, v138, v6
v_dot2c_f32_bf16 v45, v142, v6
v_dot2c_f32_bf16 v31, v146, v6
v_dot2c_f32_bf16 v35, v150, v6
v_add_f32_dpp v47, v33, v33 row_ror:8 row_mask:0xf bank_mask:0xc
v_add_f32_dpp v44, v36, v36 row_ror:8 row_mask:0xf bank_mask:0xc
v_dot2c_f32_bf16 v34, v139, v7
v_dot2c_f32_bf16 v45, v143, v7
v_dot2c_f32_bf16 v31, v147, v7
v_dot2c_f32_bf16 v35, v151, v7
v_add_f32_dpp v47, v47, v47 row_shl:4 row_mask:0xf bank_mask:0x5
v_dot2_f32_bf16 v32, v152, v4, 0
v_dot2_f32_bf16 v46, v156, v4, 0
v_dot2_f32_bf16 v33, v160, v4, 0
v_dot2_f32_bf16 v36, v164, v4, 0
v_add_f32_dpp v47, v44, v44 row_shr:4 row_mask:0xf bank_mask:0xa
v_cndmask_b32 v30, v47, v37, s[44:45]
v_dot2c_f32_bf16 v32, v153, v5
v_dot2c_f32_bf16 v46, v157, v5
v_dot2c_f32_bf16 v33, v161, v5
v_dot2c_f32_bf16 v36, v165, v5
v_cndmask_b32 v37, v37, v47, s[44:45]
v_add_f32_dpp v34, v34, v34 row_ror:8 row_mask:0xf bank_mask:0x3
v_add_f32_dpp v31, v31, v31 row_ror:8 row_mask:0xf bank_mask:0x3
v_dot2c_f32_bf16 v32, v154, v6
v_dot2c_f32_bf16 v46, v158, v6
v_dot2c_f32_bf16 v33, v162, v6
v_dot2c_f32_bf16 v36, v166, v6
v_add_f32_dpp v37, v30, v37 quad_perm:[2,3,0,1] row_mask:0xf bank_mask:0xf
v_add_f32_dpp v34, v45, v45 row_ror:8 row_mask:0xf bank_mask:0xc
v_dot2c_f32_bf16 v32, v155, v7
v_dot2c_f32_bf16 v46, v159, v7
v_dot2c_f32_bf16 v33, v163, v7
v_dot2c_f32_bf16 v36, v167, v7
v_add_f32_dpp v31, v35, v35 row_ror:8 row_mask:0xf bank_mask:0xc
v_add_f32_dpp v34, v34, v34 row_shl:4 row_mask:0xf bank_mask:0x5
s_cmp_lg_u32 s51, 0
s_cbranch_scc1 Lnb1_%=
s_add_u32 s40, %[rot], 8192
s_and_b32 s40, s40, 28672
s_add_u32 s40, s40, 1146880
buffer_load_dwordx4 v[104:107], v0, %[rk], s40 offen nt
s_add_u32 s40, %[rot], 12288
s_and_b32 s40, s40, 28672
s_add_u32 s40, s40, 1146880
buffer_load_dwordx4 v[108:111], v0, %[rk], s40 offen nt
s_add_u32 s40, %[rot], 16384
s_and_b32 s40, s40, 28672
s_add_u32 s40, s40, 1146880
buffer_load_dwordx4 v[112:115], v0, %[rk], s40 offen nt
s_add_u32 s40, %[rot], 20480
s_and_b32 s40, s40, 28672
s_add_u32 s40, s40, 1146880
buffer_load_dwordx4 v[116:119], v0, %[rk], s40 offen nt
s_add_u32 s40, %[rot], 24576
s_and_b32 s40, s40, 28672
s_add_u32 s40, s40, 1146880
buffer_load_dwordx4 v[120:123], v0, %[rk], s40 offen nt
s_add_u32 s40, %[rot], 28672
s_and_b32 s40, s40, 28672
s_add_u32 s40, s40, 1146880
buffer_load_dwordx4 v[124:127], v0, %[rk], s40 offen nt
s_add_u32 s40, %[rot], 0
s_and_b32 s40, s40, 28672
s_add_u32 s40, s40, 1376256
buffer_load_dwordx4 v[128:131], v0, %[rk], s40 offen nt
s_add_u32 s40, %[rot], 4096
s_and_b32 s40, s40, 28672
s_add_u32 s40, s40, 1376256
buffer_load_dwordx4 v[132:135], v0, %[rk], s40 offen nt
Lnb1_%=:
s_waitcnt lgkmcnt(7)
v_dot2_f32_bf16 v44, v168, v4, 0
v_dot2_f32_bf16 v47, v172, v4, 0
v_dot2_f32_bf16 v45, v60, v4, 0
v_dot2_f32_bf16 v35, v64, v4, 0
v_add_f32_dpp v34, v31, v31 row_shr:4 row_mask:0xf bank_mask:0xa
v_add_f32_dpp v32, v32, v32 row_ror:8 row_mask:0xf bank_mask:0x3
v_dot2c_f32_bf16 v44, v169, v5
v_dot2c_f32_bf16 v47, v173, v5
v_dot2c_f32_bf16 v45, v61, v5
v_dot2c_f32_bf16 v35, v65, v5
v_add_f32_dpp v32, v46, v46 row_ror:8 row_mask:0xf bank_mask:0xc
v_add_f32_dpp v33, v33, v33 row_ror:8 row_mask:0xf bank_mask:0x3
v_dot2c_f32_bf16 v44, v170, v6
v_dot2c_f32_bf16 v47, v174, v6
v_dot2c_f32_bf16 v45, v62, v6
v_dot2c_f32_bf16 v35, v66, v6
v_add_f32_dpp v33, v36, v36 row_ror:8 row_mask:0xf bank_mask:0xc
v_add_f32_dpp v32, v32, v32 row_shl:4 row_mask:0xf bank_mask:0x5
v_dot2c_f32_bf16 v44, v171, v7
v_dot2c_f32_bf16 v47, v175, v7
v_dot2c_f32_bf16 v45, v63, v7
v_dot2c_f32_bf16 v35, v67, v7
v_add_f32_dpp v32, v33, v33 row_shr:4 row_mask:0xf bank_mask:0xa
v_cndmask_b32 v30, v32, v34, s[44:45]
v_cndmask_b32 v34, v34, v32, s[44:45]
ds_read_b128 v[60:63], v1 offset:9216
ds_read_b128 v[64:67], v1 offset:10240
s_waitcnt lgkmcnt(5)
v_dot2_f32_bf16 v31, v68, v4, 0
v_dot2_f32_bf16 v46, v72, v4, 0
v_dot2_f32_bf16 v36, v76, v4, 0
v_dot2_f32_bf16 v33, v80, v4, 0
v_add_f32_dpp v34, v30, v34 quad_perm:[2,3,0,1] row_mask:0xf bank_mask:0xf
v_cndmask_b32 v30, v34, v37, s[46:47]
v_dot2c_f32_bf16 v31, v69, v5
v_dot2c_f32_bf16 v46, v73, v5
v_dot2c_f32_bf16 v36, v77, v5
v_dot2c_f32_bf16 v33, v81, v5
v_cndmask_b32 v37, v37, v34, s[46:47]
v_add_f32_dpp v44, v44, v44 row_ror:8 row_mask:0xf bank_mask:0x3
v_add_f32_dpp v45, v45, v45 row_ror:8 row_mask:0xf bank_mask:0x3
v_dot2c_f32_bf16 v31, v70, v6
v_dot2c_f32_bf16 v46, v74, v6
v_dot2c_f32_bf16 v36, v78, v6
v_dot2c_f32_bf16 v33, v82, v6
v_add_f32_dpp v40, v30, v37 quad_perm:[1,0,3,2] row_mask:0xf bank_mask:0xf
v_cvt_pk_bf16_f32 v27, v40, v40
v_dot2c_f32_bf16 v31, v71, v7
v_dot2c_f32_bf16 v46, v75, v7
v_dot2c_f32_bf16 v36, v79, v7
v_dot2c_f32_bf16 v33, v83, v7
v_and_b32 v40, 0xffff0000, v27
v_mul_f32 v40, s55, v40
v_cvt_pk_bf16_f32 v27, v40, v40
ds_read_b128 v[68:71], v1 offset:11264
ds_read_b128 v[72:75], v1 offset:12288
ds_read_b128 v[76:79], v1 offset:13312
ds_read_b128 v[80:83], v1 offset:14336
s_waitcnt lgkmcnt(5)
v_dot2_f32_bf16 v32, v48, v4, 0
v_dot2_f32_bf16 v34, v52, v4, 0
v_dot2_f32_bf16 v37, v56, v4, 0
v_and_b32 v40, 0xffff0000, v27
v_add_f32_dpp v44, v47, v47 row_ror:8 row_mask:0xf bank_mask:0xc
v_dot2_f32_bf16 v47, v60, v4, 0
v_add_f32_dpp v45, v35, v35 row_ror:8 row_mask:0xf bank_mask:0xc
v_add_f32_dpp v44, v44, v44 row_shl:4 row_mask:0xf bank_mask:0x5
v_dot2c_f32_bf16 v32, v49, v5
v_dot2c_f32_bf16 v34, v53, v5
v_dot2c_f32_bf16 v37, v57, v5
v_dot2c_f32_bf16 v47, v61, v5
v_add_f32_dpp v44, v45, v45 row_shr:4 row_mask:0xf bank_mask:0xa
v_add_f32_dpp v31, v31, v31 row_ror:8 row_mask:0xf bank_mask:0x3
v_add_f32_dpp v36, v36, v36 row_ror:8 row_mask:0xf bank_mask:0x3
v_dot2c_f32_bf16 v32, v50, v6
v_dot2c_f32_bf16 v34, v54, v6
v_dot2c_f32_bf16 v37, v58, v6
v_dot2c_f32_bf16 v47, v62, v6
v_add_f32_dpp v31, v46, v46 row_ror:8 row_mask:0xf bank_mask:0xc
v_add_f32_dpp v36, v33, v33 row_ror:8 row_mask:0xf bank_mask:0xc
v_dot2c_f32_bf16 v32, v51, v7
v_dot2c_f32_bf16 v34, v55, v7
v_dot2c_f32_bf16 v37, v59, v7
v_dot2c_f32_bf16 v47, v63, v7
v_add_f32_dpp v31, v31, v31 row_shl:4 row_mask:0xf bank_mask:0x5
ds_read_b128 v[48:51], v1 offset:15360
ds_read_b128 v[52:55], v1 offset:16384
ds_read_b128 v[56:59], v1 offset:17408
ds_read_b128 v[60:63], v1 offset:18432
s_waitcnt lgkmcnt(5)
v_dot2_f32_bf16 v35, v64, v4, 0
v_dot2_f32_bf16 v45, v68, v4, 0
v_dot2_f32_bf16 v46, v72, v4, 0
v_dot2_f32_bf16 v33, v76, v4, 0
v_add_f32_dpp v31, v36, v36 row_shr:4 row_mask:0xf bank_mask:0xa
v_cndmask_b32 v30, v31, v44, s[44:45]
v_dot2c_f32_bf16 v35, v65, v5
v_dot2c_f32_bf16 v45, v69, v5
v_dot2c_f32_bf16 v46, v73, v5
v_dot2c_f32_bf16 v33, v77, v5
v_cndmask_b32 v44, v44, v31, s[44:45]
v_add_f32_dpp v32, v32, v32 row_ror:8 row_mask:0xf bank_mask:0x3
v_add_f32_dpp v37, v37, v37 row_ror:8 row_mask:0xf bank_mask:0x3
v_dot2c_f32_bf16 v35, v66, v6
v_dot2c_f32_bf16 v45, v70, v6
v_dot2c_f32_bf16 v46, v74, v6
v_dot2c_f32_bf16 v33, v78, v6
v_add_f32_dpp v44, v30, v44 quad_perm:[2,3,0,1] row_mask:0xf bank_mask:0xf
v_add_f32_dpp v32, v34, v34 row_ror:8 row_mask:0xf bank_mask:0xc
v_dot2c_f32_bf16 v35, v67, v7
v_dot2c_f32_bf16 v45, v71, v7
v_dot2c_f32_bf16 v46, v75, v7
v_dot2c_f32_bf16 v33, v79, v7
v_add_f32_dpp v37, v47, v47 row_ror:8 row_mask:0xf bank_mask:0xc
v_add_f32_dpp v32, v32, v32 row_shl:4 row_mask:0xf bank_mask:0x5
ds_read_b128 v[64:67], v1 offset:19456
ds_read_b128 v[68:71], v1 offset:20480
ds_read_b128 v[72:75], v1 offset:21504
ds_read_b128 v[76:79], v1 offset:22528
s_waitcnt lgkmcnt(5)
v_dot2_f32_bf16 v36, v80, v4, 0
v_dot2_f32_bf16 v31, v48, v4, 0
v_dot2_f32_bf16 v34, v52, v4, 0
v_dot2_f32_bf16 v47, v56, v4, 0
v_add_f32_dpp v32, v37, v37 row_shr:4 row_mask:0xf bank_mask:0xa
v_add_f32_dpp v35, v35, v35 row_ror:8 row_mask:0xf bank_mask:0x3
v_dot2c_f32_bf16 v36, v81, v5
v_dot2c_f32_bf16 v31, v49, v5
v_dot2c_f32_bf16 v34, v53, v5
v_dot2c_f32_bf16 v47, v57, v5
v_add_f32_dpp v35, v45, v45 row_ror:8 row_mask:0xf bank_mask:0xc
v_add_f32_dpp v46, v46, v46 row_ror:8 row_mask:0xf bank_mask:0x3
v_dot2c_f32_bf16 v36, v82, v6
v_dot2c_f32_bf16 v31, v50, v6
v_dot2c_f32_bf16 v34, v54, v6
v_dot2c_f32_bf16 v47, v58, v6
v_add_f32_dpp v46, v33, v33 row_ror:8 row_mask:0xf bank_mask:0xc
v_add_f32_dpp v35, v35, v35 row_shl:4 row_mask:0xf bank_mask:0x5
v_dot2c_f32_bf16 v36, v83, v7
v_dot2c_f32_bf16 v31, v51, v7
v_dot2c_f32_bf16 v34, v55, v7
v_dot2c_f32_bf16 v47, v59, v7
v_add_f32_dpp v35, v46, v46 row_shr:4 row_mask:0xf bank_mask:0xa
v_cndmask_b32 v30, v35, v32, s[44:45]
v_cndmask_b32 v32, v32, v35, s[44:45]
ds_read_b128 v[80:83], v1 offset:23552
ds_read_b128 v[48:51], v1 offset:24576
ds_read_b128 v[52:55], v1 offset:25600
ds_read_b128 v[56:59], v1 offset:26624
s_waitcnt lgkmcnt(5)
v_dot2_f32_bf16 v37, v60, v4, 0
v_dot2_f32_bf16 v45, v64, v4, 0
v_dot2_f32_bf16 v33, v68, v4, 0
v_dot2_f32_bf16 v46, v72, v4, 0
v_add_f32_dpp v32, v30, v32 quad_perm:[2,3,0,1] row_mask:0xf bank_mask:0xf
v_cndmask_b32 v30, v32, v44, s[46:47]
v_dot2c_f32_bf16 v37, v61, v5
v_dot2c_f32_bf16 v45, v65, v5
v_dot2c_f32_bf16 v33, v69, v5
v_dot2c_f32_bf16 v46, v73, v5
v_cndmask_b32 v44, v44, v32, s[46:47]
v_add_f32_dpp v36, v36, v36 row_ror:8 row_mask:0xf bank_mask:0x3
v_add_f32_dpp v34, v34, v34 row_ror:8 row_mask:0xf bank_mask:0x3
v_dot2c_f32_bf16 v37, v62, v6
v_dot2c_f32_bf16 v45, v66, v6
v_dot2c_f32_bf16 v33, v70, v6
v_dot2c_f32_bf16 v46, v74, v6
v_add_f32_dpp v41, v30, v44 quad_perm:[1,0,3,2] row_mask:0xf bank_mask:0xf
v_cvt_pk_bf16_f32 v27, v41, v41
v_dot2c_f32_bf16 v37, v63, v7
v_dot2c_f32_bf16 v45, v67, v7
v_dot2c_f32_bf16 v33, v71, v7
v_dot2c_f32_bf16 v46, v75, v7
v_and_b32 v41, 0xffff0000, v27
v_mul_f32 v41, s55, v41
v_cvt_pk_bf16_f32 v27, v41, v41
ds_read_b128 v[60:63], v1 offset:27648
ds_read_b128 v[64:67], v1 offset:28672
ds_read_b128 v[68:71], v1 offset:29696
ds_read_b128 v[72:75], v1 offset:30720
s_waitcnt lgkmcnt(5)
v_dot2_f32_bf16 v35, v76, v4, 0
v_dot2_f32_bf16 v32, v80, v4, 0
v_dot2_f32_bf16 v44, v48, v4, 0
v_and_b32 v41, 0xffff0000, v27
v_add_f32_dpp v36, v31, v31 row_ror:8 row_mask:0xf bank_mask:0xc
v_dot2_f32_bf16 v31, v52, v4, 0
v_add_f32_dpp v34, v47, v47 row_ror:8 row_mask:0xf bank_mask:0xc
v_add_f32_dpp v36, v36, v36 row_shl:4 row_mask:0xf bank_mask:0x5
v_dot2c_f32_bf16 v35, v77, v5
v_dot2c_f32_bf16 v32, v81, v5
v_dot2c_f32_bf16 v44, v49, v5
v_dot2c_f32_bf16 v31, v53, v5
v_add_f32_dpp v36, v34, v34 row_shr:4 row_mask:0xf bank_mask:0xa
v_add_f32_dpp v37, v37, v37 row_ror:8 row_mask:0xf bank_mask:0x3
v_add_f32_dpp v33, v33, v33 row_ror:8 row_mask:0xf bank_mask:0x3
v_dot2c_f32_bf16 v35, v78, v6
v_dot2c_f32_bf16 v32, v82, v6
v_dot2c_f32_bf16 v44, v50, v6
v_dot2c_f32_bf16 v31, v54, v6
v_add_f32_dpp v37, v45, v45 row_ror:8 row_mask:0xf bank_mask:0xc
v_add_f32_dpp v33, v46, v46 row_ror:8 row_mask:0xf bank_mask:0xc
v_dot2c_f32_bf16 v35, v79, v7
v_dot2c_f32_bf16 v32, v83, v7
v_dot2c_f32_bf16 v44, v51, v7
v_dot2c_f32_bf16 v31, v55, v7
v_add_f32_dpp v37, v37, v37 row_shl:4 row_mask:0xf bank_mask:0x5
ds_read_b128 v[76:79], v1 offset:31744
ds_read_b128 v[80:83], v1 offset:32768
ds_read_b128 v[48:51], v1 offset:33792
ds_read_b128 v[52:55], v1 offset:34816
s_waitcnt lgkmcnt(5)
v_dot2_f32_bf16 v47, v56, v4, 0
v_dot2_f32_bf16 v34, v60, v4, 0
v_dot2_f32_bf16 v45, v64, v4, 0
v_dot2_f32_bf16 v46, v68, v4, 0
v_add_f32_dpp v37, v33, v33 row_shr:4 row_mask:0xf bank_mask:0xa
v_cndmask_b32 v30, v37, v36, s[44:45]
v_dot2c_f32_bf16 v47, v57, v5
v_dot2c_f32_bf16 v34, v61, v5
v_dot2c_f32_bf16 v45, v65, v5
v_dot2c_f32_bf16 v46, v69, v5
v_cndmask_b32 v36, v36, v37, s[44:45]
v_add_f32_dpp v35, v35, v35 row_ror:8 row_mask:0xf bank_mask:0x3
v_add_f32_dpp v44, v44, v44 row_ror:8 row_mask:0xf bank_mask:0x3
v_dot2c_f32_bf16 v47, v58, v6
v_dot2c_f32_bf16 v34, v62, v6
v_dot2c_f32_bf16 v45, v66, v6
v_dot2c_f32_bf16 v46, v70, v6
v_add_f32_dpp v36, v30, v36 quad_perm:[2,3,0,1] row_mask:0xf bank_mask:0xf
v_add_f32_dpp v35, v32, v32 row_ror:8 row_mask:0xf bank_mask:0xc
v_dot2c_f32_bf16 v47, v59, v7
v_dot2c_f32_bf16 v34, v63, v7
v_dot2c_f32_bf16 v45, v67, v7
v_dot2c_f32_bf16 v46, v71, v7
v_add_f32_dpp v44, v31, v31 row_ror:8 row_mask:0xf bank_mask:0xc
v_add_f32_dpp v35, v35, v35 row_shl:4 row_mask:0xf bank_mask:0x5
ds_read_b128 v[56:59], v1 offset:35840
ds_read_b128 v[60:63], v1 offset:36864
ds_read_b128 v[64:67], v1 offset:37888
ds_read_b128 v[68:71], v1 offset:38912
s_waitcnt lgkmcnt(6)
v_dot2_f32_bf16 v33, v72, v4, 0
v_dot2_f32_bf16 v37, v76, v4, 0
v_dot2_f32_bf16 v32, v80, v4, 0
v_add_f32_dpp v35, v44, v44 row_shr:4 row_mask:0xf bank_mask:0xa
v_add_f32_dpp v47, v47, v47 row_ror:8 row_mask:0xf bank_mask:0x3
v_dot2c_f32_bf16 v33, v73, v5
v_dot2c_f32_bf16 v37, v77, v5
v_dot2c_f32_bf16 v32, v81, v5
v_add_f32_dpp v47, v34, v34 row_ror:8 row_mask:0xf bank_mask:0xc
v_add_f32_dpp v45, v45, v45 row_ror:8 row_mask:0xf bank_mask:0x3
v_dot2c_f32_bf16 v33, v74, v6
v_dot2c_f32_bf16 v37, v78, v6
v_dot2c_f32_bf16 v32, v82, v6
v_add_f32_dpp v45, v46, v46 row_ror:8 row_mask:0xf bank_mask:0xc
v_add_f32_dpp v47, v47, v47 row_shl:4 row_mask:0xf bank_mask:0x5
v_dot2c_f32_bf16 v33, v75, v7
v_dot2c_f32_bf16 v37, v79, v7
v_dot2c_f32_bf16 v32, v83, v7
v_add_f32_dpp v47, v45, v45 row_shr:4 row_mask:0xf bank_mask:0xa
v_cndmask_b32 v30, v47, v35, s[44:45]
v_cndmask_b32 v35, v35, v47, s[44:45]
s_waitcnt lgkmcnt(3)
v_dot2_f32_bf16 v31, v48, v4, 0
v_dot2_f32_bf16 v44, v52, v4, 0
v_dot2_f32_bf16 v34, v56, v4, 0
v_add_f32_dpp v35, v30, v35 quad_perm:[2,3,0,1] row_mask:0xf bank_mask:0xf
v_cndmask_b32 v30, v35, v36, s[46:47]
v_dot2c_f32_bf16 v31, v49, v5
v_dot2c_f32_bf16 v44, v53, v5
v_dot2c_f32_bf16 v34, v57, v5
)ASM"
R"ASM(v_cndmask_b32 v36, v36, v35, s[46:47]
v_add_f32_dpp v33, v33, v33 row_ror:8 row_mask:0xf bank_mask:0x3
v_dot2c_f32_bf16 v31, v50, v6
v_dot2c_f32_bf16 v44, v54, v6
v_dot2c_f32_bf16 v34, v58, v6
v_add_f32_dpp v42, v30, v36 quad_perm:[1,0,3,2] row_mask:0xf bank_mask:0xf
v_cvt_pk_bf16_f32 v27, v42, v42
v_dot2c_f32_bf16 v31, v51, v7
v_dot2c_f32_bf16 v44, v55, v7
v_dot2c_f32_bf16 v34, v59, v7
v_and_b32 v42, 0xffff0000, v27
v_mul_f32 v42, s55, v42
v_cvt_pk_bf16_f32 v27, v42, v42
s_waitcnt lgkmcnt(0)
v_dot2_f32_bf16 v46, v60, v4, 0
v_dot2_f32_bf16 v45, v64, v4, 0
v_dot2_f32_bf16 v47, v68, v4, 0
v_and_b32 v42, 0xffff0000, v27
v_add_f32_dpp v33, v37, v37 row_ror:8 row_mask:0xf bank_mask:0xc
v_dot2c_f32_bf16 v46, v61, v5
v_dot2c_f32_bf16 v45, v65, v5
v_dot2c_f32_bf16 v47, v69, v5
v_add_f32_dpp v32, v32, v32 row_ror:8 row_mask:0xf bank_mask:0x3
v_add_f32_dpp v33, v33, v33 row_shl:4 row_mask:0xf bank_mask:0x5
v_add_f32_dpp v44, v44, v44 row_ror:8 row_mask:0xf bank_mask:0x3
v_dot2c_f32_bf16 v46, v62, v6
v_dot2c_f32_bf16 v45, v66, v6
v_dot2c_f32_bf16 v47, v70, v6
v_add_f32_dpp v32, v31, v31 row_ror:8 row_mask:0xf bank_mask:0xc
v_add_f32_dpp v44, v34, v34 row_ror:8 row_mask:0xf bank_mask:0xc
v_dot2c_f32_bf16 v46, v63, v7
v_dot2c_f32_bf16 v45, v67, v7
v_dot2c_f32_bf16 v47, v71, v7
v_add_f32_dpp v33, v32, v32 row_shr:4 row_mask:0xf bank_mask:0xa
v_add_f32_dpp v46, v46, v46 row_ror:8 row_mask:0xf bank_mask:0x3
s_nop 1
v_add_f32_dpp v46, v45, v45 row_ror:8 row_mask:0xf bank_mask:0xc
v_add_f32_dpp v44, v44, v44 row_shl:4 row_mask:0xf bank_mask:0x5
s_nop 1
v_add_f32_dpp v44, v46, v46 row_shr:4 row_mask:0xf bank_mask:0xa
v_cndmask_b32 v30, v44, v33, s[44:45]
v_cndmask_b32 v33, v33, v44, s[44:45]
s_nop 1
v_add_f32_dpp v33, v30, v33 quad_perm:[2,3,0,1] row_mask:0xf bank_mask:0xf
v_add_f32_dpp v47, v47, v47 row_ror:8 row_mask:0xf bank_mask:0x3
s_nop 1
v_add_f32_dpp v47, v47, v47 row_shl:4 row_mask:0xf bank_mask:0x5
s_nop 1
v_add_f32_dpp v47, v47, v47 quad_perm:[2,3,0,1] row_mask:0xf bank_mask:0xf
v_cndmask_b32 v30, v47, v33, s[46:47]
v_cndmask_b32 v33, v33, v47, s[46:47]
s_nop 1
v_add_f32_dpp v43, v30, v33 quad_perm:[1,0,3,2] row_mask:0xf bank_mask:0xf
v_cvt_pk_bf16_f32 v27, v43, v43
v_and_b32 v43, 0xffff0000, v27
v_mul_f32 v43, s55, v43
v_cvt_pk_bf16_f32 v27, v43, v43
v_and_b32 v43, 0xffff0000, v27
s_mov_b32 s48, 0x55575557
s_mov_b32 s49, 0x55575557
v_mov_b32 v27, 0xff800000
v_cndmask_b32 v43, v27, v43, s[48:49]
v_max3_f32 v29, v26, v40, v41
v_max3_f32 v29, v29, v42, v43
s_nop 1
v_max_f32_dpp v29, v29, v29 row_ror:8 row_mask:0xf bank_mask:0xf
s_nop 1
v_max_f32_dpp v29, v29, v29 row_ror:4 row_mask:0xf bank_mask:0xf
s_nop 1
v_max_f32_dpp v29, v29, v29 row_ror:2 row_mask:0xf bank_mask:0xf
s_nop 1
v_max_f32_dpp v29, v29, v29 row_ror:1 row_mask:0xf bank_mask:0xf
s_nop 1
v_max_f32_dpp v29, v29, v29 row_bcast:15 row_mask:0xa bank_mask:0xf
s_nop 1
v_max_f32_dpp v29, v29, v29 row_bcast:31 row_mask:0xc bank_mask:0xf
s_nop 0
v_readlane_b32 s50, v29, 63
v_mov_b32 v27, s54
s_nop 0
v_mul_f32 v80, s50, v27
v_fma_f32 v26, v26, s54, -v80
v_fma_f32 v40, v40, s54, -v80
v_fma_f32 v41, v41, s54, -v80
v_fma_f32 v42, v42, s54, -v80
v_fma_f32 v43, v43, s54, -v80
v_exp_f32 v26, v26
v_exp_f32 v40, v40
v_exp_f32 v41, v41
v_exp_f32 v42, v42
v_exp_f32 v43, v43
v_add_f32 v81, v26, v40
v_add_f32 v29, v41, v42
v_add_f32 v81, v81, v43
v_cvt_pk_bf16_f32 v26, v26, v26
v_cvt_pk_bf16_f32 v40, v40, v40
v_cvt_pk_bf16_f32 v41, v41, v41
v_cvt_pk_bf16_f32 v42, v42, v42
v_cvt_pk_bf16_f32 v43, v43, v43
v_add_f32 v81, v81, v29
v_and_b32_dpp v22, v26, v2 quad_perm:[0,0,0,0] row_mask:0xf bank_mask:0xf
v_and_b32_dpp v23, v26, v3 quad_perm:[0,0,0,0] row_mask:0xf bank_mask:0xf
s_nop 1
v_mfma_f32_4x4x4_16b_bf16 v[44:47], v[22:23], a[156:157], 0 cbsz:2 abid:0
v_mfma_f32_4x4x4_16b_bf16 v[48:51], v[22:23], a[158:159], 0 cbsz:2 abid:0
v_and_b32_dpp v24, v26, v2 quad_perm:[2,2,2,2] row_mask:0xf bank_mask:0xf
v_mfma_f32_4x4x4_16b_bf16 v[52:55], v[22:23], a[160:161], 0 cbsz:2 abid:2
v_mfma_f32_4x4x4_16b_bf16 v[56:59], v[22:23], a[162:163], 0 cbsz:2 abid:2
v_add_f32_dpp v81, v81, v81 row_ror:8 row_mask:0xf bank_mask:0xf
v_mfma_f32_4x4x4_16b_bf16 v[44:47], v[22:23], a[164:165], v[44:47] cbsz:2 abid:1
v_mfma_f32_4x4x4_16b_bf16 v[48:51], v[22:23], a[166:167], v[48:51] cbsz:2 abid:1
v_and_b32_dpp v25, v26, v3 quad_perm:[2,2,2,2] row_mask:0xf bank_mask:0xf
v_mfma_f32_4x4x4_16b_bf16 v[52:55], v[22:23], a[168:169], v[52:55] cbsz:2 abid:3
v_mfma_f32_4x4x4_16b_bf16 v[56:59], v[22:23], a[170:171], v[56:59] cbsz:2 abid:3
v_mfma_f32_4x4x4_16b_bf16 v[44:47], v[24:25], a[172:173], v[44:47] cbsz:2 abid:0
v_mfma_f32_4x4x4_16b_bf16 v[48:51], v[24:25], a[174:175], v[48:51] cbsz:2 abid:0
v_and_b32_dpp v22, v26, v2 quad_perm:[1,1,1,1] row_mask:0xf bank_mask:0xf
v_mfma_f32_4x4x4_16b_bf16 v[52:55], v[24:25], a[176:177], v[52:55] cbsz:2 abid:2
v_mfma_f32_4x4x4_16b_bf16 v[56:59], v[24:25], a[178:179], v[56:59] cbsz:2 abid:2
v_add_f32_dpp v81, v81, v81 row_ror:4 row_mask:0xf bank_mask:0xf
v_mfma_f32_4x4x4_16b_bf16 v[44:47], v[24:25], a[180:181], v[44:47] cbsz:2 abid:1
v_mfma_f32_4x4x4_16b_bf16 v[48:51], v[24:25], a[182:183], v[48:51] cbsz:2 abid:1
v_and_b32_dpp v23, v26, v3 quad_perm:[1,1,1,1] row_mask:0xf bank_mask:0xf
v_mfma_f32_4x4x4_16b_bf16 v[52:55], v[24:25], a[184:185], v[52:55] cbsz:2 abid:3
v_mfma_f32_4x4x4_16b_bf16 v[56:59], v[24:25], a[186:187], v[56:59] cbsz:2 abid:3
v_mfma_f32_4x4x4_16b_bf16 v[44:47], v[22:23], a[188:189], v[44:47] cbsz:2 abid:0
v_mfma_f32_4x4x4_16b_bf16 v[48:51], v[22:23], a[190:191], v[48:51] cbsz:2 abid:0
v_and_b32_dpp v24, v26, v2 quad_perm:[3,3,3,3] row_mask:0xf bank_mask:0xf
v_mfma_f32_4x4x4_16b_bf16 v[52:55], v[22:23], a[192:193], v[52:55] cbsz:2 abid:2
v_mfma_f32_4x4x4_16b_bf16 v[56:59], v[22:23], a[194:195], v[56:59] cbsz:2 abid:2
v_add_f32_dpp v81, v81, v81 row_ror:2 row_mask:0xf bank_mask:0xf
v_mfma_f32_4x4x4_16b_bf16 v[44:47], v[22:23], a[196:197], v[44:47] cbsz:2 abid:1
v_mfma_f32_4x4x4_16b_bf16 v[48:51], v[22:23], a[198:199], v[48:51] cbsz:2 abid:1
v_and_b32_dpp v25, v26, v3 quad_perm:[3,3,3,3] row_mask:0xf bank_mask:0xf
v_mfma_f32_4x4x4_16b_bf16 v[52:55], v[22:23], a[200:201], v[52:55] cbsz:2 abid:3
v_mfma_f32_4x4x4_16b_bf16 v[56:59], v[22:23], a[202:203], v[56:59] cbsz:2 abid:3
v_mfma_f32_4x4x4_16b_bf16 v[44:47], v[24:25], a[204:205], v[44:47] cbsz:2 abid:0
v_mfma_f32_4x4x4_16b_bf16 v[48:51], v[24:25], a[206:207], v[48:51] cbsz:2 abid:0
v_and_b32_dpp v22, v40, v2 quad_perm:[0,0,0,0] row_mask:0xf bank_mask:0xf
v_mfma_f32_4x4x4_16b_bf16 v[52:55], v[24:25], a[208:209], v[52:55] cbsz:2 abid:2
v_mfma_f32_4x4x4_16b_bf16 v[56:59], v[24:25], a[210:211], v[56:59] cbsz:2 abid:2
v_add_f32_dpp v81, v81, v81 row_ror:1 row_mask:0xf bank_mask:0xf
v_mfma_f32_4x4x4_16b_bf16 v[44:47], v[24:25], a[212:213], v[44:47] cbsz:2 abid:1
v_mfma_f32_4x4x4_16b_bf16 v[48:51], v[24:25], a[214:215], v[48:51] cbsz:2 abid:1
v_and_b32_dpp v23, v40, v3 quad_perm:[0,0,0,0] row_mask:0xf bank_mask:0xf
v_mfma_f32_4x4x4_16b_bf16 v[52:55], v[24:25], a[216:217], v[52:55] cbsz:2 abid:3
v_mfma_f32_4x4x4_16b_bf16 v[56:59], v[24:25], a[218:219], v[56:59] cbsz:2 abid:3
s_cmp_lg_u32 s51, 0
s_cbranch_scc1 Lnb2_%=
s_add_u32 s40, %[rot], 8192
s_and_b32 s40, s40, 28672
s_add_u32 s40, s40, 688128
buffer_load_dwordx4 a[156:159], v0, %[rv], s40 offen nt
s_add_u32 s40, %[rot], 12288
s_and_b32 s40, s40, 28672
s_add_u32 s40, s40, 688128
buffer_load_dwordx4 a[160:163], v0, %[rv], s40 offen nt
s_add_u32 s40, %[rot], 16384
s_and_b32 s40, s40, 28672
s_add_u32 s40, s40, 688128
buffer_load_dwordx4 a[164:167], v0, %[rv], s40 offen nt
s_add_u32 s40, %[rot], 20480
s_and_b32 s40, s40, 28672
s_add_u32 s40, s40, 688128
buffer_load_dwordx4 a[168:171], v0, %[rv], s40 offen nt
s_add_u32 s40, %[rot], 24576
s_and_b32 s40, s40, 28672
s_add_u32 s40, s40, 688128
buffer_load_dwordx4 a[172:175], v0, %[rv], s40 offen nt
s_add_u32 s40, %[rot], 28672
s_and_b32 s40, s40, 28672
s_add_u32 s40, s40, 688128
buffer_load_dwordx4 a[176:179], v0, %[rv], s40 offen nt
s_add_u32 s40, %[rot], 0
s_and_b32 s40, s40, 28672
s_add_u32 s40, s40, 917504
buffer_load_dwordx4 a[180:183], v0, %[rv], s40 offen nt
s_add_u32 s40, %[rot], 4096
s_and_b32 s40, s40, 28672
s_add_u32 s40, s40, 917504
buffer_load_dwordx4 a[184:187], v0, %[rv], s40 offen nt
s_add_u32 s40, %[rot], 8192
s_and_b32 s40, s40, 28672
s_add_u32 s40, s40, 917504
buffer_load_dwordx4 a[188:191], v0, %[rv], s40 offen nt
s_add_u32 s40, %[rot], 12288
s_and_b32 s40, s40, 28672
s_add_u32 s40, s40, 917504
buffer_load_dwordx4 a[192:195], v0, %[rv], s40 offen nt
s_add_u32 s40, %[rot], 16384
s_and_b32 s40, s40, 28672
s_add_u32 s40, s40, 917504
buffer_load_dwordx4 a[196:199], v0, %[rv], s40 offen nt
s_add_u32 s40, %[rot], 20480
s_and_b32 s40, s40, 28672
s_add_u32 s40, s40, 917504
buffer_load_dwordx4 a[200:203], v0, %[rv], s40 offen nt
Lnb2_%=:
v_mfma_f32_4x4x4_16b_bf16 v[44:47], v[22:23], a[220:221], v[44:47] cbsz:2 abid:0
v_mfma_f32_4x4x4_16b_bf16 v[48:51], v[22:23], a[222:223], v[48:51] cbsz:2 abid:0
v_and_b32_dpp v24, v40, v2 quad_perm:[2,2,2,2] row_mask:0xf bank_mask:0xf
v_mfma_f32_4x4x4_16b_bf16 v[52:55], v[22:23], a[224:225], v[52:55] cbsz:2 abid:2
v_mfma_f32_4x4x4_16b_bf16 v[56:59], v[22:23], a[226:227], v[56:59] cbsz:2 abid:2
v_add_f32_dpp v81, v81, v81 row_bcast:15 row_mask:0xa bank_mask:0xf
v_mfma_f32_4x4x4_16b_bf16 v[44:47], v[22:23], a[228:229], v[44:47] cbsz:2 abid:1
v_mfma_f32_4x4x4_16b_bf16 v[48:51], v[22:23], a[230:231], v[48:51] cbsz:2 abid:1
v_and_b32_dpp v25, v40, v3 quad_perm:[2,2,2,2] row_mask:0xf bank_mask:0xf
v_mfma_f32_4x4x4_16b_bf16 v[52:55], v[22:23], a[232:233], v[52:55] cbsz:2 abid:3
v_mfma_f32_4x4x4_16b_bf16 v[56:59], v[22:23], a[234:235], v[56:59] cbsz:2 abid:3
v_mfma_f32_4x4x4_16b_bf16 v[44:47], v[24:25], a[236:237], v[44:47] cbsz:2 abid:0
v_mfma_f32_4x4x4_16b_bf16 v[48:51], v[24:25], a[238:239], v[48:51] cbsz:2 abid:0
v_and_b32_dpp v22, v40, v2 quad_perm:[1,1,1,1] row_mask:0xf bank_mask:0xf
v_mfma_f32_4x4x4_16b_bf16 v[52:55], v[24:25], a[240:241], v[52:55] cbsz:2 abid:2
v_mfma_f32_4x4x4_16b_bf16 v[56:59], v[24:25], a[242:243], v[56:59] cbsz:2 abid:2
v_add_f32_dpp v81, v81, v81 row_bcast:31 row_mask:0xc bank_mask:0xf
v_mfma_f32_4x4x4_16b_bf16 v[44:47], v[24:25], a[244:245], v[44:47] cbsz:2 abid:1
v_mfma_f32_4x4x4_16b_bf16 v[48:51], v[24:25], a[246:247], v[48:51] cbsz:2 abid:1
v_and_b32_dpp v23, v40, v3 quad_perm:[1,1,1,1] row_mask:0xf bank_mask:0xf
v_mfma_f32_4x4x4_16b_bf16 v[52:55], v[24:25], a[248:249], v[52:55] cbsz:2 abid:3
v_mfma_f32_4x4x4_16b_bf16 v[56:59], v[24:25], a[250:251], v[56:59] cbsz:2 abid:3
v_mfma_f32_4x4x4_16b_bf16 v[44:47], v[22:23], a[252:253], v[44:47] cbsz:2 abid:0
v_mfma_f32_4x4x4_16b_bf16 v[48:51], v[22:23], a[254:255], v[48:51] cbsz:2 abid:0
v_and_b32_dpp v24, v40, v2 quad_perm:[3,3,3,3] row_mask:0xf bank_mask:0xf
v_mfma_f32_4x4x4_16b_bf16 v[52:55], v[22:23], v[176:177], v[52:55] cbsz:2 abid:2
v_mfma_f32_4x4x4_16b_bf16 v[56:59], v[22:23], v[178:179], v[56:59] cbsz:2 abid:2
v_mfma_f32_4x4x4_16b_bf16 v[44:47], v[22:23], v[180:181], v[44:47] cbsz:2 abid:1
v_mfma_f32_4x4x4_16b_bf16 v[48:51], v[22:23], v[182:183], v[48:51] cbsz:2 abid:1
v_and_b32_dpp v25, v40, v3 quad_perm:[3,3,3,3] row_mask:0xf bank_mask:0xf
v_mfma_f32_4x4x4_16b_bf16 v[52:55], v[22:23], v[184:185], v[52:55] cbsz:2 abid:3
v_mfma_f32_4x4x4_16b_bf16 v[56:59], v[22:23], v[186:187], v[56:59] cbsz:2 abid:3
v_mfma_f32_4x4x4_16b_bf16 v[44:47], v[24:25], v[188:189], v[44:47] cbsz:2 abid:0
v_mfma_f32_4x4x4_16b_bf16 v[48:51], v[24:25], v[190:191], v[48:51] cbsz:2 abid:0
v_and_b32_dpp v22, v41, v2 quad_perm:[0,0,0,0] row_mask:0xf bank_mask:0xf
v_mfma_f32_4x4x4_16b_bf16 v[52:55], v[24:25], v[192:193], v[52:55] cbsz:2 abid:2
v_mfma_f32_4x4x4_16b_bf16 v[56:59], v[24:25], v[194:195], v[56:59] cbsz:2 abid:2
v_mfma_f32_4x4x4_16b_bf16 v[44:47], v[24:25], v[196:197], v[44:47] cbsz:2 abid:1
v_mfma_f32_4x4x4_16b_bf16 v[48:51], v[24:25], v[198:199], v[48:51] cbsz:2 abid:1
v_and_b32_dpp v23, v41, v3 quad_perm:[0,0,0,0] row_mask:0xf bank_mask:0xf
v_mfma_f32_4x4x4_16b_bf16 v[52:55], v[24:25], v[200:201], v[52:55] cbsz:2 abid:3
v_mfma_f32_4x4x4_16b_bf16 v[56:59], v[24:25], v[202:203], v[56:59] cbsz:2 abid:3
v_mfma_f32_4x4x4_16b_bf16 v[44:47], v[22:23], v[204:205], v[44:47] cbsz:2 abid:0
v_mfma_f32_4x4x4_16b_bf16 v[48:51], v[22:23], v[206:207], v[48:51] cbsz:2 abid:0
v_and_b32_dpp v24, v41, v2 quad_perm:[2,2,2,2] row_mask:0xf bank_mask:0xf
v_mfma_f32_4x4x4_16b_bf16 v[52:55], v[22:23], v[208:209], v[52:55] cbsz:2 abid:2
v_mfma_f32_4x4x4_16b_bf16 v[56:59], v[22:23], v[210:211], v[56:59] cbsz:2 abid:2
v_mfma_f32_4x4x4_16b_bf16 v[44:47], v[22:23], a[0:1], v[44:47] cbsz:2 abid:1
v_mfma_f32_4x4x4_16b_bf16 v[48:51], v[22:23], a[2:3], v[48:51] cbsz:2 abid:1
v_and_b32_dpp v25, v41, v3 quad_perm:[2,2,2,2] row_mask:0xf bank_mask:0xf
v_mfma_f32_4x4x4_16b_bf16 v[52:55], v[22:23], a[4:5], v[52:55] cbsz:2 abid:3
v_mfma_f32_4x4x4_16b_bf16 v[56:59], v[22:23], a[6:7], v[56:59] cbsz:2 abid:3
v_mfma_f32_4x4x4_16b_bf16 v[44:47], v[24:25], a[8:9], v[44:47] cbsz:2 abid:0
v_mfma_f32_4x4x4_16b_bf16 v[48:51], v[24:25], a[10:11], v[48:51] cbsz:2 abid:0
v_and_b32_dpp v22, v41, v2 quad_perm:[1,1,1,1] row_mask:0xf bank_mask:0xf
v_mfma_f32_4x4x4_16b_bf16 v[52:55], v[24:25], a[12:13], v[52:55] cbsz:2 abid:2
v_mfma_f32_4x4x4_16b_bf16 v[56:59], v[24:25], a[14:15], v[56:59] cbsz:2 abid:2
v_mfma_f32_4x4x4_16b_bf16 v[44:47], v[24:25], a[16:17], v[44:47] cbsz:2 abid:1
v_mfma_f32_4x4x4_16b_bf16 v[48:51], v[24:25], a[18:19], v[48:51] cbsz:2 abid:1
v_and_b32_dpp v23, v41, v3 quad_perm:[1,1,1,1] row_mask:0xf bank_mask:0xf
v_mfma_f32_4x4x4_16b_bf16 v[52:55], v[24:25], a[20:21], v[52:55] cbsz:2 abid:3
v_mfma_f32_4x4x4_16b_bf16 v[56:59], v[24:25], a[22:23], v[56:59] cbsz:2 abid:3
v_mfma_f32_4x4x4_16b_bf16 v[44:47], v[22:23], a[24:25], v[44:47] cbsz:2 abid:0
v_mfma_f32_4x4x4_16b_bf16 v[48:51], v[22:23], a[26:27], v[48:51] cbsz:2 abid:0
v_and_b32_dpp v24, v41, v2 quad_perm:[3,3,3,3] row_mask:0xf bank_mask:0xf
v_mfma_f32_4x4x4_16b_bf16 v[52:55], v[22:23], a[28:29], v[52:55] cbsz:2 abid:2
v_mfma_f32_4x4x4_16b_bf16 v[56:59], v[22:23], a[30:31], v[56:59] cbsz:2 abid:2
v_mfma_f32_4x4x4_16b_bf16 v[44:47], v[22:23], a[32:33], v[44:47] cbsz:2 abid:1
v_mfma_f32_4x4x4_16b_bf16 v[48:51], v[22:23], a[34:35], v[48:51] cbsz:2 abid:1
v_and_b32_dpp v25, v41, v3 quad_perm:[3,3,3,3] row_mask:0xf bank_mask:0xf
v_mfma_f32_4x4x4_16b_bf16 v[52:55], v[22:23], a[36:37], v[52:55] cbsz:2 abid:3
v_mfma_f32_4x4x4_16b_bf16 v[56:59], v[22:23], a[38:39], v[56:59] cbsz:2 abid:3
v_mfma_f32_4x4x4_16b_bf16 v[44:47], v[24:25], a[40:41], v[44:47] cbsz:2 abid:0
v_mfma_f32_4x4x4_16b_bf16 v[48:51], v[24:25], a[42:43], v[48:51] cbsz:2 abid:0
v_and_b32_dpp v22, v42, v2 quad_perm:[0,0,0,0] row_mask:0xf bank_mask:0xf
v_mfma_f32_4x4x4_16b_bf16 v[52:55], v[24:25], a[44:45], v[52:55] cbsz:2 abid:2
v_mfma_f32_4x4x4_16b_bf16 v[56:59], v[24:25], a[46:47], v[56:59] cbsz:2 abid:2
v_mfma_f32_4x4x4_16b_bf16 v[44:47], v[24:25], a[48:49], v[44:47] cbsz:2 abid:1
v_mfma_f32_4x4x4_16b_bf16 v[48:51], v[24:25], a[50:51], v[48:51] cbsz:2 abid:1
v_and_b32_dpp v23, v42, v3 quad_perm:[0,0,0,0] row_mask:0xf bank_mask:0xf
v_mfma_f32_4x4x4_16b_bf16 v[52:55], v[24:25], a[52:53], v[52:55] cbsz:2 abid:3
v_mfma_f32_4x4x4_16b_bf16 v[56:59], v[24:25], a[54:55], v[56:59] cbsz:2 abid:3
v_mfma_f32_4x4x4_16b_bf16 v[44:47], v[22:23], a[56:57], v[44:47] cbsz:2 abid:0
v_mfma_f32_4x4x4_16b_bf16 v[48:51], v[22:23], a[58:59], v[48:51] cbsz:2 abid:0
v_and_b32_dpp v24, v42, v2 quad_perm:[2,2,2,2] row_mask:0xf bank_mask:0xf
v_mfma_f32_4x4x4_16b_bf16 v[52:55], v[22:23], a[60:61], v[52:55] cbsz:2 abid:2
v_mfma_f32_4x4x4_16b_bf16 v[56:59], v[22:23], a[62:63], v[56:59] cbsz:2 abid:2
v_mfma_f32_4x4x4_16b_bf16 v[44:47], v[22:23], a[64:65], v[44:47] cbsz:2 abid:1
v_mfma_f32_4x4x4_16b_bf16 v[48:51], v[22:23], a[66:67], v[48:51] cbsz:2 abid:1
v_and_b32_dpp v25, v42, v3 quad_perm:[2,2,2,2] row_mask:0xf bank_mask:0xf
v_mfma_f32_4x4x4_16b_bf16 v[52:55], v[22:23], a[68:69], v[52:55] cbsz:2 abid:3
v_mfma_f32_4x4x4_16b_bf16 v[56:59], v[22:23], a[70:71], v[56:59] cbsz:2 abid:3
v_mfma_f32_4x4x4_16b_bf16 v[44:47], v[24:25], a[72:73], v[44:47] cbsz:2 abid:0
v_mfma_f32_4x4x4_16b_bf16 v[48:51], v[24:25], a[74:75], v[48:51] cbsz:2 abid:0
v_and_b32_dpp v22, v42, v2 quad_perm:[1,1,1,1] row_mask:0xf bank_mask:0xf
v_mfma_f32_4x4x4_16b_bf16 v[52:55], v[24:25], a[76:77], v[52:55] cbsz:2 abid:2
v_mfma_f32_4x4x4_16b_bf16 v[56:59], v[24:25], a[78:79], v[56:59] cbsz:2 abid:2
v_mfma_f32_4x4x4_16b_bf16 v[44:47], v[24:25], a[80:81], v[44:47] cbsz:2 abid:1
v_mfma_f32_4x4x4_16b_bf16 v[48:51], v[24:25], a[82:83], v[48:51] cbsz:2 abid:1
v_and_b32_dpp v23, v42, v3 quad_perm:[1,1,1,1] row_mask:0xf bank_mask:0xf
v_mfma_f32_4x4x4_16b_bf16 v[52:55], v[24:25], a[84:85], v[52:55] cbsz:2 abid:3
v_mfma_f32_4x4x4_16b_bf16 v[56:59], v[24:25], a[86:87], v[56:59] cbsz:2 abid:3
v_mfma_f32_4x4x4_16b_bf16 v[44:47], v[22:23], a[88:89], v[44:47] cbsz:2 abid:0
v_mfma_f32_4x4x4_16b_bf16 v[48:51], v[22:23], a[90:91], v[48:51] cbsz:2 abid:0
v_and_b32_dpp v24, v42, v2 quad_perm:[3,3,3,3] row_mask:0xf bank_mask:0xf
v_mfma_f32_4x4x4_16b_bf16 v[52:55], v[22:23], a[92:93], v[52:55] cbsz:2 abid:2
v_mfma_f32_4x4x4_16b_bf16 v[56:59], v[22:23], a[94:95], v[56:59] cbsz:2 abid:2
v_mfma_f32_4x4x4_16b_bf16 v[44:47], v[22:23], a[96:97], v[44:47] cbsz:2 abid:1
v_mfma_f32_4x4x4_16b_bf16 v[48:51], v[22:23], a[98:99], v[48:51] cbsz:2 abid:1
v_and_b32_dpp v25, v42, v3 quad_perm:[3,3,3,3] row_mask:0xf bank_mask:0xf
v_mfma_f32_4x4x4_16b_bf16 v[52:55], v[22:23], a[100:101], v[52:55] cbsz:2 abid:3
v_mfma_f32_4x4x4_16b_bf16 v[56:59], v[22:23], a[102:103], v[56:59] cbsz:2 abid:3
v_mfma_f32_4x4x4_16b_bf16 v[44:47], v[24:25], a[104:105], v[44:47] cbsz:2 abid:0
v_mfma_f32_4x4x4_16b_bf16 v[48:51], v[24:25], a[106:107], v[48:51] cbsz:2 abid:0
v_and_b32_dpp v22, v43, v2 quad_perm:[0,0,0,0] row_mask:0xf bank_mask:0xf
v_mfma_f32_4x4x4_16b_bf16 v[52:55], v[24:25], a[108:109], v[52:55] cbsz:2 abid:2
v_mfma_f32_4x4x4_16b_bf16 v[56:59], v[24:25], a[110:111], v[56:59] cbsz:2 abid:2
v_mfma_f32_4x4x4_16b_bf16 v[44:47], v[24:25], a[112:113], v[44:47] cbsz:2 abid:1
v_mfma_f32_4x4x4_16b_bf16 v[48:51], v[24:25], a[114:115], v[48:51] cbsz:2 abid:1
v_and_b32_dpp v23, v43, v3 quad_perm:[0,0,0,0] row_mask:0xf bank_mask:0xf
v_mfma_f32_4x4x4_16b_bf16 v[52:55], v[24:25], a[116:117], v[52:55] cbsz:2 abid:3
v_mfma_f32_4x4x4_16b_bf16 v[56:59], v[24:25], a[118:119], v[56:59] cbsz:2 abid:3
v_mfma_f32_4x4x4_16b_bf16 v[44:47], v[22:23], a[120:121], v[44:47] cbsz:2 abid:0
v_mfma_f32_4x4x4_16b_bf16 v[48:51], v[22:23], a[122:123], v[48:51] cbsz:2 abid:0
v_and_b32_dpp v24, v43, v2 quad_perm:[2,2,2,2] row_mask:0xf bank_mask:0xf
v_mfma_f32_4x4x4_16b_bf16 v[52:55], v[22:23], a[124:125], v[52:55] cbsz:2 abid:2
v_mfma_f32_4x4x4_16b_bf16 v[56:59], v[22:23], a[126:127], v[56:59] cbsz:2 abid:2
v_mfma_f32_4x4x4_16b_bf16 v[44:47], v[22:23], a[128:129], v[44:47] cbsz:2 abid:1
v_mfma_f32_4x4x4_16b_bf16 v[48:51], v[22:23], a[130:131], v[48:51] cbsz:2 abid:1
v_and_b32_dpp v25, v43, v3 quad_perm:[2,2,2,2] row_mask:0xf bank_mask:0xf
v_mfma_f32_4x4x4_16b_bf16 v[52:55], v[22:23], a[132:133], v[52:55] cbsz:2 abid:3
v_mfma_f32_4x4x4_16b_bf16 v[56:59], v[22:23], a[134:135], v[56:59] cbsz:2 abid:3
v_mfma_f32_4x4x4_16b_bf16 v[44:47], v[24:25], a[136:137], v[44:47] cbsz:2 abid:0
v_mfma_f32_4x4x4_16b_bf16 v[48:51], v[24:25], a[138:139], v[48:51] cbsz:2 abid:0
v_and_b32_dpp v22, v43, v2 quad_perm:[1,1,1,1] row_mask:0xf bank_mask:0xf
v_mfma_f32_4x4x4_16b_bf16 v[52:55], v[24:25], a[140:141], v[52:55] cbsz:2 abid:2
v_mfma_f32_4x4x4_16b_bf16 v[56:59], v[24:25], a[142:143], v[56:59] cbsz:2 abid:2
v_mfma_f32_4x4x4_16b_bf16 v[44:47], v[24:25], a[144:145], v[44:47] cbsz:2 abid:1
v_mfma_f32_4x4x4_16b_bf16 v[48:51], v[24:25], a[146:147], v[48:51] cbsz:2 abid:1
v_and_b32_dpp v23, v43, v3 quad_perm:[1,1,1,1] row_mask:0xf bank_mask:0xf
v_mfma_f32_4x4x4_16b_bf16 v[52:55], v[24:25], a[148:149], v[52:55] cbsz:2 abid:3
v_mfma_f32_4x4x4_16b_bf16 v[56:59], v[24:25], a[150:151], v[56:59] cbsz:2 abid:3
v_mfma_f32_4x4x4_16b_bf16 v[44:47], v[22:23], a[152:153], v[44:47] cbsz:2 abid:0
v_mfma_f32_4x4x4_16b_bf16 v[48:51], v[22:23], a[154:155], v[48:51] cbsz:2 abid:0
s_nop 3
v_add_f32 v44, v44, v52
v_add_f32 v45, v45, v53
v_add_f32 v46, v46, v54
v_add_f32 v47, v47, v55
v_add_f32 v48, v48, v56
v_add_f32 v49, v49, v57
v_add_f32 v50, v50, v58
v_add_f32 v51, v51, v59
s_lshl_b32 s42, s51, 1
s_add_u32 s42, s42, 1
v_permlane32_swap_b32 v44, v48
v_permlane32_swap_b32 v45, v49
v_permlane32_swap_b32 v46, v50
v_permlane32_swap_b32 v47, v51
v_add_f32 v44, v44, v48
v_add_f32 v45, v45, v49
v_add_f32 v46, v46, v50
v_add_f32 v47, v47, v51
s_nop 0
v_permlane16_swap_b32 v44, v46
v_permlane16_swap_b32 v45, v47
v_add_f32 v44, v44, v46
v_add_f32 v45, v45, v47
s_lshl_b32 s41, %[w], 9
s_add_u32 s41, s41, %[lb]
v_add_u32 v48, s41, v38
s_lshl_b32 s41, %[w], 3
s_add_u32 s41, s41, %[lb]
v_mov_b32 v50, s41
v_add_u32 v48, 159744, v48
v_add_u32 v50, 161792, v50
ds_write_b64 v48, v[44:45]
s_bfm_b64 exec, 1, 63
ds_write_b64 v50, v[80:81]
s_lshl_b32 s41, s42, 2
s_add_u32 s41, s41, %[lb]
s_add_u32 s41, s41, 161824
v_mov_b32 v50, s41
v_mov_b32 v49, 1
ds_add_rtn_u32 v51, v50, v49
s_mov_b64 exec, -1
s_add_u32 s41, %[lb], 159744
v_add_u32 v48, s41, v38
v_mov_b32 v46, s41
ds_read_b64 v[60:61], v48 offset:0
ds_read_b64 v[62:63], v48 offset:512
ds_read_b64 v[64:65], v48 offset:1024
ds_read_b64 v[66:67], v48 offset:1536
ds_read_b128 v[68:71], v46 offset:2048
ds_read_b128 v[72:75], v46 offset:2064
s_mul_i32 s40, s42, 262144
s_add_u32 s40, s40, s32
s_waitcnt lgkmcnt(0)
v_readlane_b32 s50, v51, 63
v_max3_f32 v46, v68, v70, v72
v_max_f32 v46, v46, v74
s_cmp_lg_u32 s50, 3
)ASM"
R"ASM(s_cbranch_scc1 Lskip_e2_%=
v_sub_f32 v68, v68, v46
v_sub_f32 v70, v70, v46
v_sub_f32 v72, v72, v46
v_sub_f32 v74, v74, v46
v_exp_f32 v68, v68
v_exp_f32 v70, v70
v_exp_f32 v72, v72
v_exp_f32 v74, v74
v_mul_f32 v44, v68, v60
v_mul_f32 v45, v68, v61
v_mul_f32 v47, v68, v69
v_fma_f32 v44, v70, v62, v44
v_fma_f32 v45, v70, v63, v45
v_fma_f32 v47, v70, v71, v47
v_fma_f32 v44, v72, v64, v44
v_fma_f32 v45, v72, v65, v45
v_fma_f32 v47, v72, v73, v47
v_fma_f32 v44, v74, v66, v44
v_fma_f32 v45, v74, v67, v45
v_fma_f32 v47, v74, v75, v47
v_min_u32 v44, -2, v44
v_min_u32 v45, -2, v45
v_min_u32 v46, -2, v46
v_min_u32 v47, -2, v47
buffer_store_dwordx2 v[44:45], v38, %[rw], s40 offen sc1
s_bfm_b64 exec, 1, 63
buffer_store_dwordx2 v[46:47], off, %[rw], s40 offset:512 sc1
ds_add_u32 v50, v49
s_mov_b64 exec, -1
s_waitcnt lgkmcnt(0)
Lskip_e2_%=:
s_nop 4
s_cmp_eq_u32 s51, 0
s_cbranch_scc1 Lp2ret0_%=
s_branch Lp2ret1_%=
Le1_%=:
s_nop 4
v_permlane32_swap_b32 v12, v16
v_permlane32_swap_b32 v13, v17
v_permlane32_swap_b32 v14, v18
v_permlane32_swap_b32 v15, v19
v_add_f32 v12, v12, v16
v_add_f32 v13, v13, v17
v_add_f32 v14, v14, v18
v_add_f32 v15, v15, v19
s_nop 0
v_permlane16_swap_b32 v12, v14
v_permlane16_swap_b32 v13, v15
v_add_f32 v12, v12, v14
v_add_f32 v13, v13, v15
s_lshl_b32 s41, %[w], 9
s_add_u32 s41, s41, %[lb]
v_add_u32 v16, s41, v38
s_lshl_b32 s41, %[w], 3
s_add_u32 s41, s41, %[lb]
v_mov_b32 v18, s41
v_add_u32 v16, 159744, v16
v_add_u32 v18, 161792, v18
ds_write_b64 v16, v[12:13]
s_bfm_b64 exec, 1, 63
ds_write_b64 v18, v[20:21]
s_lshl_b32 s41, s42, 2
s_add_u32 s41, s41, %[lb]
s_add_u32 s41, s41, 161824
v_mov_b32 v18, s41
v_mov_b32 v17, 1
ds_add_rtn_u32 v19, v18, v17
s_mov_b64 exec, -1
s_add_u32 s41, %[lb], 159744
v_add_u32 v16, s41, v38
v_mov_b32 v14, s41
ds_read_b64 v[22:23], v16 offset:0
ds_read_b64 v[24:25], v16 offset:512
ds_read_b64 v[26:27], v16 offset:1024
ds_read_b64 v[28:29], v16 offset:1536
ds_read_b128 v[30:33], v14 offset:2048
ds_read_b128 v[34:37], v14 offset:2064
s_mul_i32 s40, s42, 262144
s_add_u32 s40, s40, s32
s_waitcnt lgkmcnt(0)
v_readlane_b32 s50, v19, 63
v_max3_f32 v14, v30, v32, v34
v_max_f32 v14, v14, v36
s_cmp_lg_u32 s50, 3
s_cbranch_scc1 Lskip_e1_%=
v_sub_f32 v30, v30, v14
v_sub_f32 v32, v32, v14
v_sub_f32 v34, v34, v14
v_sub_f32 v36, v36, v14
v_exp_f32 v30, v30
v_exp_f32 v32, v32
v_exp_f32 v34, v34
v_exp_f32 v36, v36
v_mul_f32 v12, v30, v22
v_mul_f32 v13, v30, v23
v_mul_f32 v15, v30, v31
v_fma_f32 v12, v32, v24, v12
v_fma_f32 v13, v32, v25, v13
v_fma_f32 v15, v32, v33, v15
v_fma_f32 v12, v34, v26, v12
v_fma_f32 v13, v34, v27, v13
v_fma_f32 v15, v34, v35, v15
v_fma_f32 v12, v36, v28, v12
v_fma_f32 v13, v36, v29, v13
v_fma_f32 v15, v36, v37, v15
v_min_u32 v12, -2, v12
v_min_u32 v13, -2, v13
v_min_u32 v14, -2, v14
v_min_u32 v15, -2, v15
buffer_store_dwordx2 v[12:13], v38, %[rw], s40 offen sc1
s_bfm_b64 exec, 1, 63
buffer_store_dwordx2 v[14:15], off, %[rw], s40 offset:512 sc1
ds_add_u32 v18, v17
s_mov_b64 exec, -1
s_waitcnt lgkmcnt(0)
Lskip_e1_%=:
s_nop 4
s_cmp_eq_u32 s42, 0
s_cbranch_scc1 Le1ret0_%=
s_branch Le1ret1_%=
Lslow_cf_%=:
s_cmp_eq_u32 s52, 0
s_cbranch_scc1 Lback_cf_%=
s_sub_u32 s52, s52, 1
buffer_load_dwordx2 v[4:5], off, %[rw], %[cf] sc1
s_waitcnt vmcnt(0)
v_xor_b32 v4, s58, v4
v_xor_b32 v5, s59, v5
v_or_b32 v4, v4, v5
v_cmp_ne_u32 vcc, 0, v4
s_cbranch_vccnz Lslow_cf_%=
s_branch Lback_cf_%=
Lslow_q1b0_%=:
s_cmp_eq_u32 s52, 0
s_cbranch_scc1 Lback_q1b0_%=
s_sub_u32 s52, s52, 1
s_add_u32 s41, %[qo], 0
buffer_load_dwordx4 v[4:7], v39, %[rw], s41 offen sc1
s_waitcnt vmcnt(0)
v_max3_u32 v27, v4, v5, v6
v_max_u32 v27, v27, v7
v_cmp_eq_u32 vcc, -1, v27
s_cbranch_vccnz Lslow_q1b0_%=
s_branch Lback_q1b0_%=
Lslow_p_%=:
s_cmp_eq_u32 s52, 0
s_cbranch_scc1 Lback_p_%=
s_sub_u32 s52, s52, 1
s_mov_b32 s41, 2109440
buffer_load_dwordx4 v[8:11], off, %[rw], s41 sc1
s_waitcnt vmcnt(0)
v_readfirstlane_b32 s36, v8
v_readfirstlane_b32 s37, v9
v_readfirstlane_b32 s38, v10
v_readfirstlane_b32 s39, v11
s_mov_b32 s42, %[xh]
s_cmp_eq_u64 s[36:37], s[58:59]
s_cbranch_scc1 Lback_p_%=
s_xor_b32 s42, %[xh], 0x2000
s_cmp_eq_u64 s[38:39], s[58:59]
s_cbranch_scc0 Lslow_p_%=
s_branch Lback_p_%=
Lslow_q1b1_%=:
s_cmp_eq_u32 s52, 0
s_cbranch_scc1 Lback_q1b1_%=
s_sub_u32 s52, s52, 1
s_add_u32 s41, %[qo], 256
buffer_load_dwordx4 v[8:11], v39, %[rw], s41 offen sc1
s_waitcnt vmcnt(0)
v_max3_u32 v27, v8, v9, v10
v_max_u32 v27, v27, v11
v_cmp_eq_u32 vcc, -1, v27
s_cbranch_vccnz Lslow_q1b1_%=
s_branch Lback_q1b1_%=
Lslow_cf2_%=:
s_cmp_eq_u32 s52, 0
s_cbranch_scc1 Lback_cf2_%=
s_sub_u32 s52, s52, 1
buffer_load_dwordx2 v[4:5], off, %[rw], %[cf2] sc1
s_waitcnt vmcnt(0)
v_xor_b32 v4, s58, v4
v_xor_b32 v5, s59, v5
v_or_b32 v4, v4, v5
v_cmp_ne_u32 vcc, 0, v4
s_cbranch_vccnz Lslow_cf2_%=
s_branch Lback_cf2_%=
Lslow_q2b0_%=:
s_cmp_eq_u32 s52, 0
s_cbranch_scc1 Lback_q2b0_%=
s_sub_u32 s52, s52, 1
s_add_u32 s41, s33, 262144
buffer_load_dwordx4 v[4:7], v39, %[rw], s41 offen sc1
s_waitcnt vmcnt(0)
v_max3_u32 v27, v4, v5, v6
v_max_u32 v27, v27, v7
v_cmp_eq_u32 vcc, -1, v27
s_cbranch_vccnz Lslow_q2b0_%=
s_branch Lback_q2b0_%=
Lslow_q2b1_%=:
s_cmp_eq_u32 s52, 0
s_cbranch_scc1 Lback_q2b1_%=
s_sub_u32 s52, s52, 1
s_add_u32 s41, s33, 786432
buffer_load_dwordx4 v[4:7], v39, %[rw], s41 offen sc1
s_waitcnt vmcnt(0)
v_max3_u32 v27, v4, v5, v6
v_max_u32 v27, v27, v7
v_cmp_eq_u32 vcc, -1, v27
s_cbranch_vccnz Lslow_q2b1_%=
s_branch Lback_q2b1_%=
Lp2ret1_%=:
.Lenda:
)ASM"
        :
        : [rk] "s"(rk), [rv] "s"(rv), [rw] "s"(rw), [ep] "s"(ep), [w] "s"(w), [lb] "s"(lb), [po] "s"(po), [q2] "s"(q2), [xh] "s"(xh), [fl] "s"(fl), [cf] "s"(cf), [cf2] "s"(cf2), [qo] "s"(qo), [pf] "s"(pf), [dbg] "s"(dbg),
          [pq] "s"(pq), [po2] "s"(po2), [p1] "s"(p1), [p2] "s"(p2), [n2] "s"(n2), [rot] "s"(rot), [t0] "s"((u32)t0)
        : "v0","v1","v2","v3","v4","v5","v6","v7","v8","v9","v10","v11","v12","v13","v14","v15","v16","v17","v18","v19","v20","v21","v22","v23","v24","v25","v26","v27","v28","v29","v30","v31","v32","v33","v34","v35","v36","v37","v38","v39","v40","v41","v42","v43","v44","v45","v46","v47","v48","v49","v50","v51","v52","v53","v54","v55","v56","v57","v58","v59","v60","v61","v62","v63","v64","v65","v66","v67","v68","v69","v70","v71","v72","v73","v74","v75","v76","v77","v78","v79","v80","v81","v82","v83","v84","v85","v86","v87","v88","v89","v90","v91","v92","v93","v94","v95","v96","v97","v98","v99","v100","v101","v102","v103","v104","v105","v106","v107","v108","v109","v110","v111","v112","v113","v114","v115","v116","v117","v118","v119","v120","v121","v122","v123","v124","v125","v126","v127","v128","v129","v130","v131","v132","v133","v134","v135","v136","v137","v138","v139","v140","v141","v142","v143","v144","v145","v146","v147","v148","v149","v150","v151","v152","v153","v154","v155","v156","v157","v158","v159","v160","v161","v162","v163","v164","v165","v166","v167","v168","v169","v170","v171","v172","v173","v174","v175","v176","v177","v178","v179","v180","v181","v182","v183","v184","v185","v186","v187","v188","v189","v190","v191","v192","v193","v194","v195","v196","v197","v198","v199","v200","v201","v202","v203","v204","v205","v206","v207","v208","v209","v210","v211","v212","v213","v214","v215","v216","v217","v218","v219","v220","v221","v222","v223","v224","v225","v226","v227","v228","v229","v230","v231","v232","v233","v234","v235","v236","v237","v238","v239","v240","v241","v242","v243","v244","v245","v246","v247","v248","v249","v250","v251","v252","v253","v254","v255","a0","a1","a2","a3","a4","a5","a6","a7","a8","a9","a10","a11","a12","a13","a14","a15","a16","a17","a18","a19","a20","a21","a22","a23","a24","a25","a26","a27","a28","a29","a30","a31","a32","a33","a34","a35","a36","a37","a38","a39","a40","a41","a42","a43","a44","a45","a46","a47","a48","a49","a50","a51","a52","a53","a54","a55","a56","a57","a58","a59","a60","a61","a62","a63","a64","a65","a66","a67","a68","a69","a70","a71","a72","a73","a74","a75","a76","a77","a78","a79","a80","a81","a82","a83","a84","a85","a86","a87","a88","a89","a90","a91","a92","a93","a94","a95","a96","a97","a98","a99","a100","a101","a102","a103","a104","a105","a106","a107","a108","a109","a110","a111","a112","a113","a114","a115","a116","a117","a118","a119","a120","a121","a122","a123","a124","a125","a126","a127","a128","a129","a130","a131","a132","a133","a134","a135","a136","a137","a138","a139","a140","a141","a142","a143","a144","a145","a146","a147","a148","a149","a150","a151","a152","a153","a154","a155","a156","a157","a158","a159","a160","a161","a162","a163","a164","a165","a166","a167","a168","a169","a170","a171","a172","a173","a174","a175","a176","a177","a178","a179","a180","a181","a182","a183","a184","a185","a186","a187","a188","a189","a190","a191","a192","a193","a194","a195","a196","a197","a198","a199","a200","a201","a202","a203","a204","a205","a206","a207","a208","a209","a210","a211","a212","a213","a214","a215","a216","a217","a218","a219","a220","a221","a222","a223","a224","a225","a226","a227","a228","a229","a230","a231","a232","a233","a234","a235","a236","a237","a238","a239","a240","a241","a242","a243","a244","a245","a246","a247","a248","a249","a250","a251","a252","a253","a254","a255","s32","s33","s34","s35","s36","s37","s38","s39","s40","s41","s42","s43","s44","s45","s46","s47","s48","s49","s50","s51","s52","s53","s54","s55","s56","s57","s58","s59", "vcc", "scc", "memory");
  }
}

extern "C" void kernel_launch(void* const* d_in, const int* in_sizes, int n_in, void* d_out, int out_size, void* d_ws, size_t ws_size, hipStream_t stream) {
  fused_layers<<<256, 256, 0, stream>>>(d_in[0], d_in[1], d_in[2], d_in[3], d_in[4], d_in[6], d_in[7], d_out, d_ws);
}
```
